```python
import torch

def _softmax(x, axis=-1):
    return torch.softmax(x, dim=axis)
def _gelu(x):
    return torch.nn.functional.gelu(x, approximate="tanh")
def _silu(x):
    return torch.nn.functional.silu(x)
def _sigmoid(x):
    return torch.sigmoid(x)
def _layernorm(x, axis=-1, eps=1e-5):
    m = x.mean(dim=axis, keepdim=True)
    v = x.var(dim=axis, keepdim=True, unbiased=False)
    return (x - m) / torch.sqrt(v + eps)
def _scatter_add(tgt, idx, src, axis=0):
    return tgt.clone().index_add_(axis, idx, src)
def _segment_sum(src, seg, n):
    out = torch.zeros((n,) + src.shape[1:], dtype=src.dtype)
    return out.index_add_(0, seg, src)
def _pad(x, pw):
    flat = tuple(v for pair in reversed(pw) for v in pair)
    return torch.nn.functional.pad(x, flat)

kernel_name = 'recurrent_scan__sliding_block_cached__moe_arch_mod__prompt'

def setup_inputs():

    _w = (lambda a: (lambda t: t.to(torch.bfloat16) if t.is_floating_point() else t)(torch.as_tensor(a)))
    def _A_setup():
        B, Sq, D = 1, 2048, 512
        Hq, Hkv, Sk, Dh = 32, 32, 2048, 128
        assert Hq % Hkv == 0, (Hq, Hkv)
        return {
            "x": _w((lambda *s: torch.randn(*s, dtype=torch.bfloat16) * 0.05)(B, Sq, D)),
            "k_cache": _w((lambda *s: torch.randn(*s, dtype=torch.bfloat16) * 0.8)(B, Hkv, Sk, Dh)),
            "v_cache": _w((lambda *s: torch.randn(*s, dtype=torch.bfloat16) * 0.2)(B, Hkv, Sk, Dh)),
            "wq": _w((lambda *s: torch.randn(*s, dtype=torch.bfloat16) * 0.8)(D, Hq * Dh) * (D ** -0.5)),
            "wo": _w((lambda *s: torch.randn(*s, dtype=torch.bfloat16) * 0.2)(Hq * Dh, D) * ((Hq * Dh) ** -0.5)),
        }
    _A_in = _A_setup()

    def _B_setup():
        B, S, D, F, C = 1, 2048, 512, 1376, 2
        return {
            "x": _w((lambda *s: torch.randn(*s, dtype=torch.bfloat16) * 0.05)(B, S, D)),
            "w_r": _w((lambda *s: torch.randn(*s, dtype=torch.bfloat16) * 0.2)(D)),
            "w_gate": _w((lambda *s: torch.randn(*s, dtype=torch.bfloat16) * 0.1)(D, F)),
            "w_up": _w((lambda *s: torch.randn(*s, dtype=torch.bfloat16) * 0.1)(D, F)),
            "w_down": _w((lambda *s: torch.randn(*s, dtype=torch.bfloat16) * 0.1)(F, D)),
        }
    _B_in = _B_setup()

    return {
        "A_x": _A_in["x"],
        "A_k_cache": _A_in["k_cache"],
        "A_v_cache": _A_in["v_cache"],
        "A_wq": _A_in["wq"],
        "A_wo": _A_in["wo"],
        "B_x": _B_in["x"],
        "B_w_r": _B_in["w_r"],
        "B_w_gate": _B_in["w_gate"],
        "B_w_up": _B_in["w_up"],
        "B_w_down": _B_in["w_down"],
    }

def reference(A_x, A_k_cache, A_v_cache, A_wq, A_wo, B_x, B_w_r, B_w_gate, B_w_up, B_w_down):
    def _A_ref(x, k_cache, v_cache, wq, wo):
        B, Sq, D = x.shape
        Hq, Dh = 32, 128
        Hkv, Sk = k_cache.shape[1], k_cache.shape[2]
        W = min(4096, max(256, Sk // 4))
        rep = Hq // Hkv
        q = (lambda x, *d: x.permute(*d))((x @ wq).reshape(B, Sq, Hq, Dh), 0, 2, 1, 3)
        k = (lambda x, n, axis: torch.repeat_interleave(x, n, dim=axis))(k_cache[:, :, Sk - W:, :], rep, axis=1)
        v = (lambda x, n, axis: torch.repeat_interleave(x, n, dim=axis))(v_cache[:, :, Sk - W:, :], rep, axis=1)
        scores = torch.einsum("bhqd,bhkd->bhqk", q, k) * (Dh ** -0.5)
        p = _softmax(scores, axis=-1)
        o = torch.einsum("bhqk,bhkd->bhqd", p, v)
        return (lambda x, *d: x.permute(*d))(o, 0, 2, 1, 3).reshape(B, Sq, Hq * Dh) @ wo

    def _B_ref(x, w_r, w_gate, w_up, w_down):
        B, S, D = x.shape
        C = 2
        r = x @ w_r
        idx = torch.argsort(-r, axis=-1)[:, :C]
        idx_e = (lambda x, n, axis: torch.repeat_interleave(x, n, dim=axis))(idx.reshape(B, C, 1), D, axis=2)
        x_sel = (lambda x, i, axis: torch.take_along_dim(x, i, dim=axis))(x, idx_e, axis=1)
        g = _silu(x_sel @ w_gate)
        u = x_sel @ w_up
        y = (g * u) @ w_down
        r_sel = _sigmoid((lambda x, i, axis: torch.take_along_dim(x, i, dim=axis))(r, idx, axis=1))
        y = y * r_sel.reshape(B, C, 1)
        b_off = (torch.arange(B) * S).reshape(B, 1)
        flat_idx = (idx + b_off).reshape(B * C)
        zero = (x * 0.0).reshape(B * S, D)
        upd = _scatter_add(zero, flat_idx, y.reshape(B * C, D), axis=0)
        return x + upd.reshape(B, S, D)

    x = A_x
    xn = _layernorm(x, axis=-1)
    s = _A_ref(xn, A_k_cache, A_v_cache, A_wq, A_wo)
    g = _silu(_B_ref(xn, B_w_r, B_w_gate, B_w_up, B_w_down))
    return x + s * g
```

```cpp
#include <hip/hip_runtime.h>
#include <hip/hip_bf16.h>
typedef unsigned short u16;
__device__ __forceinline__ float b2f(u16 v){ return __uint_as_float(((unsigned)v)<<16); }
__device__ __forceinline__ u16 f2b(float f){ __bf16 b = (__bf16)f; return __builtin_bit_cast(u16,b); }
__device__ __forceinline__ float rb(float f){ return b2f(f2b(f)); }
constexpr int S=2048,D=512,H=32,DH=128,SK=2048,W=512,F=1376;

__global__ void k_ln(const u16* x,const u16* wr,u16* xn,float* r){
  int row=blockIdx.x; int t=threadIdx.x;
  float v[8]; float s=0;
  for(int i=0;i<8;i++){v[i]=b2f(x[row*D+t*8+i]); s+=v[i];}
  for(int o=32;o;o>>=1) s+=__shfl_xor(s,o);
  float mean=s/D; float q=0;
  for(int i=0;i<8;i++){float d=v[i]-mean;q+=d*d;}
  for(int o=32;o;o>>=1) q+=__shfl_xor(q,o);
  float var=rb(q/D); float m=rb(mean);
  float den=rb(sqrtf(rb(var+1e-5f)));
  float acc=0;
  for(int i=0;i<8;i++){float y=rb(rb(v[i]-m)/den); xn[row*D+t*8+i]=f2b(y); acc+=y*b2f(wr[t*8+i]);}
  for(int o=32;o;o>>=1) acc+=__shfl_xor(acc,o);
  if(t==0) r[row]=rb(acc);
}
__global__ void k_top2(const float* r,int* idx){
  if(threadIdx.x==0){
    int b0=0; for(int i=1;i<S;i++) if(r[i]>r[b0]) b0=i;
    int b1=(b0==0)?1:0; for(int i=0;i<S;i++) if(i!=b0 && r[i]>r[b1]) b1=i;
    idx[0]=b0; idx[1]=b1;
  }
}
__global__ void k_ffn1(const u16* xn,const int* idx,const u16* wg,const u16* wu,float* hbuf){
  int f=blockIdx.x*64+threadIdx.x; int c=blockIdx.y; if(f>=F) return;
  const u16* xr=xn+idx[c]*D; float g=0,u=0;
  for(int k=0;k<D;k++){float xv=b2f(xr[k]); g+=xv*b2f(wg[k*F+f]); u+=xv*b2f(wu[k*F+f]);}
  g=rb(g); u=rb(u); float sg=rb(g/(1.f+__expf(-g)));
  hbuf[c*F+f]=rb(sg*u);
}
__global__ void k_ffn2(const float* hbuf,const int* idx,const float* r,const u16* wd,float* upd){
  int d=blockIdx.x*64+threadIdx.x; int c=blockIdx.y; float y=0;
  for(int k=0;k<F;k++) y+=hbuf[c*F+k]*b2f(wd[k*D+d]);
  y=rb(y); float rs=rb(1.f/(1.f+__expf(-r[idx[c]])));
  upd[c*D+d]=rb(y*rs);
}
__global__ void k_q(const u16* xn,const u16* wq,u16* q){
  int n=blockIdx.x*256+threadIdx.x; int row=blockIdx.y; float a=0;
  for(int k=0;k<D;k++) a+=b2f(xn[row*D+k])*b2f(wq[k*4096+n]);
  q[row*4096+n]=f2b(a);
}
__global__ void k_attn(const u16* q,const u16* kc,const u16* vc,u16* o){
  __shared__ float p[W]; __shared__ float red[8];
  int row=blockIdx.x,h=blockIdx.y,t=threadIdx.x;
  const u16* kr=kc+((size_t)h*SK+(SK-W)+t)*DH; const u16* qr=q+row*4096+h*DH;
  float s=0; for(int d=0;d<DH;d++) s+=b2f(qr[d])*b2f(kr[d]);
  s=s*0.08838834764831845f;
  float m=s; for(int o2=32;o2;o2>>=1) m=fmaxf(m,__shfl_xor(m,o2));
  if((t&63)==0) red[t>>6]=m; __syncthreads();
  m=red[0]; for(int i=1;i<8;i++) m=fmaxf(m,red[i]); __syncthreads();
  float e=__expf(s-m); float l=e; for(int o2=32;o2;o2>>=1) l+=__shfl_xor(l,o2);
  if((t&63)==0) red[t>>6]=l; __syncthreads();
  l=0; for(int i=0;i<8;i++) l+=red[i];
  p[t]=e/l; __syncthreads();
  if(t<DH){ float a=0; for(int k=0;k<W;k++) a+=p[k]*b2f(vc[((size_t)h*SK+(SK-W)+k)*DH+t]); o[row*4096+h*DH+t]=f2b(a);}
}
__global__ void k_out(const u16* o,const u16* wo,const u16* x,const u16* xn,const int* idx,const float* upd,u16* out){
  int n=blockIdx.x*256+threadIdx.x; int row=blockIdx.y; float a=0;
  for(int k=0;k<4096;k++) a+=b2f(o[row*4096+k])*b2f(wo[k*D+n]);
  a=rb(a);
  float z=b2f(xn[row*D+n]); if(row==idx[0]) z=rb(z+upd[n]); if(row==idx[1]) z=rb(z+upd[D+n]);
  float g=rb(z/(1.f+__expf(-z)));
  out[row*D+n]=f2b(b2f(x[row*D+n])+rb(a*g));
}
extern "C" void kernel_launch(void* const* d_in, const int* in_sizes, int n_in, void* d_out, int out_size, void* d_ws, size_t ws_size, hipStream_t stream){
  const u16* x=(const u16*)d_in[0]; const u16* kc=(const u16*)d_in[1]; const u16* vc=(const u16*)d_in[2];
  const u16* wq=(const u16*)d_in[3]; const u16* wo=(const u16*)d_in[4]; const u16* wr=(const u16*)d_in[6];
  const u16* wg=(const u16*)d_in[7]; const u16* wu=(const u16*)d_in[8]; const u16* wd=(const u16*)d_in[9];
  char* ws=(char*)d_ws;
  u16* xn=(u16*)ws; float* r=(float*)(ws+(4<<20)); int* idx=(int*)(ws+(5<<20)); float* hb=(float*)(ws+(6<<20)); float* upd=(float*)(ws+(7<<20));
  u16* q=(u16*)(ws+(16<<20)); u16* o=(u16*)(ws+(48<<20));
  k_ln<<<S,64,0,stream>>>(x,wr,xn,r);
  k_top2<<<1,64,0,stream>>>(r,idx);
  k_ffn1<<<dim3(22,2),64,0,stream>>>(xn,idx,wg,wu,hb);
  k_ffn2<<<dim3(8,2),64,0,stream>>>(hb,idx,r,wd,upd);
  k_q<<<dim3(16,S),256,0,stream>>>(xn,wq,q);
  k_attn<<<dim3(S,H),512,0,stream>>>(q,kc,vc,o);
  k_out<<<dim3(2,S),256,0,stream>>>(o,wo,x,xn,idx,upd,(u16*)d_out);
}
```

```cpp
#include <hip/hip_runtime.h>

__global__ __launch_bounds__(256, 1) __attribute__((aligned(4096))) void fused_kernel(const void* x, const void* wq, const void* kc, const void* vc, const void* wo, const void* wr,
                                                       const void* wg, const void* wu, const void* wd, void* out, void* ws) {
  __shared__ __attribute__((aligned(16))) char lds[163840];
  const unsigned wave = __builtin_amdgcn_readfirstlane(threadIdx.x >> 6);
  asm volatile(
"s_load_dwordx4 s[20:23], %0, 0x0\n"
"s_mov_b32 s16, %1\n"
"s_mov_b32 s17, %2\n"
"v_mbcnt_lo_u32_b32 v40, -1, 0\n"
"v_mbcnt_hi_u32_b32 v40, -1, v40\n"
"s_getpc_b64 s[8:9]\n"
".Lpc0:\n"
"s_and_b32 s9, s9, 0xffff\n"
"s_mov_b32 s10, .Lhotend-.Lpc0\n"
"s_mov_b32 s11, 0x20000\n"
"s_lshr_b32 s94, s16, 3\n"
"s_lshl_b32 s94, s94, 2\n"
"s_add_u32 s94, s94, s17\n"
"s_lshl_b32 s94, s94, 7\n"
"v_lshrrev_b32 v89, 1, v40\n"
"v_lshlrev_b32 v89, 14, v89\n"
"v_and_b32 v81, 1, v40\n"
"v_lshl_add_u32 v89, v81, 6, v89\n"
"v_add_u32 v89, s94, v89\n"
"buffer_load_dword a144, v89, s[8:11], 0 offen\n"
"s_bfe_u32 s91, s16, 0x30003\n"
"s_lshr_b32 s95, s16, 6\n"
"s_and_b32 s94, s16, 6\n"
"s_lshl_b32 s94, s94, 2\n"
"s_and_b32 s97, s16, 1\n"
"s_lshl_b32 s96, s95, 1\n"
"s_and_b32 s90, s16, 3\n"
"s_lshl_b32 s90, s90, 3\n"
"s_add_u32 s96, s96, s90\n"
"s_bfe_u32 s90, s16, 0x10002\n"
"s_add_u32 s90, s90, s96\n"
"s_and_b32 s15, s91, 3\n"
"s_and_b32 s94, s16, 7\n"
"s_lshl_b32 s94, s94, 2\n"
"s_add_u32 s88, s94, s15\n"
"s_lshl_b32 s15, s15, 1\n"
"s_lshr_b32 s89, s16, 5\n"
"s_and_b32 s95, s89, 3\n"
"v_lshrrev_b32 v41, 3, v40\n"
"v_and_b32 v42, 7, v40\n"
"v_lshrrev_b32 v43, 4, v40\n"
"v_xor_b32 v92, v42, v43\n"
"v_lshlrev_b32 v92, 4, v92\n"
"s_lshl_b32 s94, s89, 2\n"
"s_add_u32 s94, s94, s17\n"
"s_lshl_b32 s94, s94, 16\n"
"s_and_b32 s96, s15, 1\n"
"s_lshl_b32 s96, s96, 6\n"
"s_add_u32 s94, s94, s96\n"
"v_lshl_add_u32 v16, v41, 10, v92\n"
"v_xor_b32 v16, s94, v16\n"
"v_xor_b32 v17, 64, v16\n"
"s_lshl_b32 s98, s15, 13\n"
"s_lshl_b32 s40, s17, 13\n"
"s_lshl_b32 s14, s15, 10\n"
"s_add_u32 s14, s14, s40\n"
"v_and_b32 v82, 1, v43\n"
"v_lshlrev_b32 v82, 1, v82\n"
"v_xor_b32 v82, v42, v82\n"
"v_lshlrev_b32 v82, 4, v82\n"
"s_lshr_b32 s96, s17, 1\n"
"s_and_b32 s97, s17, 1\n"
"s_lshl_b32 s41, s97, 13\n"
"s_lshl_b32 s94, s96, 12\n"
"s_add_u32 s41, s41, s94\n"
"s_lshl_b32 s6, s95, 10\n"
"s_add_u32 s6, s6, s41\n"
"s_lshl_b32 s99, s95, 16\n"
"s_lshl_b32 s94, s96, 11\n"
"s_add_u32 s94, s94, s97\n"
"s_lshl_b32 s94, s94, 1\n"
"s_and_b32 s95, s95, 1\n"
"s_add_u32 s94, s94, s95\n"
"s_lshl_b32 s95, s88, 2\n"
"s_add_u32 s94, s94, s95\n"
"s_lshl_b32 s94, s94, 6\n"
"v_lshl_add_u32 v18, v41, 13, v82\n"
"v_xor_b32 v18, s94, v18\n"
"v_xor_b32 v19, 64, v18\n"
"s_and_b32 s94, s16, 7\n"
"s_lshl_b32 s95, s94, 7\n"
"s_add_u32 s98, s98, s95\n"
"s_lshl_b32 s95, s94, 19\n"
"s_add_u32 s99, s99, s95\n"
"v_lshlrev_b32 v0, 4, v40\n"
"s_lshl_b32 s95, s89, 8\n"
"s_lshl_b32 s94, s88, 3\n"
"s_add_u32 s95, s95, s94\n"
"s_lshl_b32 s94, s17, 1\n"
"s_add_u32 s95, s95, s94\n"
"v_lshrrev_b32 v95, 5, v40\n"
"v_add_u32 v95, s95, v95\n"
"v_and_b32 v94, 0x1f0, v0\n"
"v_lshl_add_u32 v93, v95, 10, v94\n"
"s_mov_b32 s50, 4194304\n"
"s_mov_b32 s51, 0x20000\n"
"s_mov_b32 s46, 2097152\n"
"s_mov_b32 s47, 0x20000\n"
"s_waitcnt lgkmcnt(0)\n"
"s_load_dwordx16 s[24:39], %0, 0x10\n"
"s_load_dwordx2 s[18:19], %0, 0x50\n"
"s_mov_b32 s48, s22\n"
"s_and_b32 s49, s23, 0xffff\n"
"s_mov_b32 s44, s20\n"
"s_and_b32 s45, s21, 0xffff\n"
"buffer_load_dwordx4 v[44:47], v93, s[44:47], 0 offen\n"
"buffer_load_dwordx4 v[48:51], v93, s[44:47], 0 offen offset:512\n"
"s_add_u32 m0, s6, 32768\n"
"s_xor_b32 s94, s99, 0\n"
"buffer_load_dwordx4 v18, s[48:51], s94 offen lds\n"
"s_add_u32 m0, s14, 0\n"
"s_xor_b32 s94, s98, 0\n"
"buffer_load_dwordx4 v16, s[44:47], s94 offen lds\n"
"s_xor_b32 s96, s14, 1024\n"
"s_add_u32 m0, s96, 0\n"
"s_xor_b32 s94, s98, 8192\n"
"buffer_load_dwordx4 v17, s[44:47], s94 offen lds\n"
"s_add_u32 m0, s6, 49152\n"
"s_xor_b32 s94, s99, 524288\n"
"buffer_load_dwordx4 v18, s[48:51], s94 offen lds\n"
"s_add_u32 m0, s14, 81920\n"
"s_xor_b32 s94, s98, 128\n"
"buffer_load_dwordx4 v16, s[44:47], s94 offen lds\n"
"s_xor_b32 s96, s14, 1024\n"
"s_add_u32 m0, s96, 81920\n"
"s_xor_b32 s94, s98, 8320\n"
"buffer_load_dwordx4 v17, s[44:47], s94 offen lds\n"
"s_xor_b32 s96, s6, 1024\n"
"s_add_u32 m0, s96, 32768\n"
"s_xor_b32 s94, s99, 65536\n"
"buffer_load_dwordx4 v19, s[48:51], s94 offen lds\n"
"s_xor_b32 s96, s6, 2048\n"
"s_add_u32 m0, s96, 32768\n"
"s_xor_b32 s94, s99, 131072\n"
"buffer_load_dwordx4 v18, s[48:51], s94 offen lds\n"
"s_lshr_b32 s96, s17, 1\n"
"s_lshl_b32 s94, s96, 13\n"
"s_and_b32 s95, s17, 1\n"
"s_lshl_b32 s95, s95, 7\n"
"s_add_u32 s94, s94, s95\n"
"v_lshl_add_u32 v1, v41, 8, v92\n"
"v_add_u32 v1, s94, v1\n"
"v_xor_b32 v2, 64, v1\n"
"s_lshl_b32 s42, s17, 11\n"
"s_lshl_b32 s43, s17, 12\n"
"s_add_u32 s43, s43, 114688\n"
"v_and_b32 v76, 15, v40\n"
"v_bfe_u32 v77, v40, 2, 2\n"
"v_and_b32 v78, 3, v40\n"
"v_lshrrev_b32 v79, 1, v76\n"
"v_xor_b32 v79, v43, v79\n"
"v_lshlrev_b32 v79, 4, v79\n"
"v_lshl_add_u32 v79, v76, 7, v79\n"
"v_add_u32 v20, s40, v79\n"
"v_xor_b32 v21, 64, v20\n"
"v_add_u32 v22, 81920, v20\n"
"v_add_u32 v23, 81920, v21\n"
"s_lshr_b32 s94, s17, 1\n"
"s_lshl_b32 s94, s94, 13\n"
"s_and_b32 s95, s17, 1\n"
"s_lshl_b32 s95, s95, 6\n"
"s_or_b32 s94, s94, s95\n"
"v_xor_b32 v79, s94, v79\n"
"v_xor_b32 v4, 0, v79\n"
"v_add_u32 v4, 98304, v4\n"
"v_xor_b32 v5, 64, v79\n"
"v_add_u32 v5, 98304, v5\n"
"v_xor_b32 v6, 8192, v79\n"
"v_add_u32 v6, 98304, v6\n"
"v_xor_b32 v7, 8256, v79\n"
"v_add_u32 v7, 98304, v7\n"
"v_lshl_add_u32 v80, v43, 3, v77\n"
"v_lshlrev_b32 v80, 7, v80\n"
"v_lshrrev_b32 v81, 1, v77\n"
"v_and_b32 v82, 1, v43\n"
"v_lshl_or_b32 v81, v82, 1, v81\n"
"v_lshl_add_u32 v80, v81, 5, v80\n"
"v_and_b32 v82, 1, v78\n"
"v_lshl_add_u32 v80, v82, 4, v80\n"
"v_lshrrev_b32 v82, 1, v78\n"
"v_lshl_add_u32 v80, v82, 3, v80\n"
"v_xor_b32 v80, s94, v80\n"
"v_xor_b32 v24, 0, v80\n"
"v_add_u32 v24, 32768, v24\n"
"v_xor_b32 v25, 32, v80\n"
"v_add_u32 v25, 32768, v25\n"
"v_xor_b32 v26, 64, v80\n"
"v_add_u32 v26, 32768, v26\n"
"v_xor_b32 v27, 96, v80\n"
"v_add_u32 v27, 32768, v27\n"
"v_xor_b32 v28, 8192, v80\n"
"v_add_u32 v28, 32768, v28\n"
"v_xor_b32 v29, 8224, v80\n"
"v_add_u32 v29, 32768, v29\n"
"v_xor_b32 v30, 8256, v80\n"
"v_add_u32 v30, 32768, v30\n"
"v_xor_b32 v31, 8288, v80\n"
"v_add_u32 v31, 32768, v31\n"
"v_lshl_add_u32 v80, v43, 2, v77\n"
"v_bfe_u32 v81, v80, 1, 2\n"
"v_lshlrev_b32 v80, 7, v80\n"
"v_lshl_add_u32 v80, v81, 5, v80\n"
"v_lshl_add_u32 v80, v78, 3, v80\n"
"v_xor_b32 v8, 0, v80\n"
"v_add_u32 v8, 65536, v8\n"
"v_xor_b32 v9, 32, v80\n"
"v_add_u32 v9, 65536, v9\n"
"v_xor_b32 v10, 64, v80\n"
"v_add_u32 v10, 65536, v10\n"
"v_xor_b32 v11, 96, v80\n"
"v_add_u32 v11, 65536, v11\n"
"v_lshlrev_b32 v81, 1, v43\n"
"v_xor_b32 v81, v42, v81\n"
"v_lshlrev_b32 v81, 4, v81\n"
"v_lshl_add_u32 v3, v41, 8, v81\n"
"s_lshl_b32 s94, s17, 12\n"
"v_add_u32 v3, s94, v3\n"
"v_lshl_add_u32 v13, v41, 10, v81\n"
"v_mov_b32 v81, 0x3f803f80\n"
"v_accvgpr_write_b32 a0, v81\n"
"v_accvgpr_write_b32 a1, v81\n"
"v_accvgpr_write_b32 a2, v81\n"
"v_accvgpr_write_b32 a3, v81\n"
"v_lshlrev_b32 v90, 3, v95\n"
"v_mov_b32 v91, 2130706432\n"
"v_and_b32 v81, 31, v40\n"
"v_cmp_eq_u32 vcc, 0, v81\n"
"s_nop 1\n"
"v_cndmask_b32 v90, v91, v90, vcc\n"
"s_lshl_b32 s94, s89, 2\n"
"s_add_u32 s94, s94, s17\n"
"s_lshl_b32 s94, s94, 9\n"
"v_lshlrev_b32 v91, 3, v76\n"
"v_add_u32 v91, s94, v91\n"
"s_lshl_b32 s94, s17, 9\n"
"s_add_u32 s94, s94, 24576\n"
"v_lshl_add_u32 v41, v40, 2, s94\n"
"v_lshl_add_u32 v43, v43, 4, s94\n"
"v_and_b32 v81, 32, v40\n"
"v_lshlrev_b32 v14, 2, v40\n"
"v_lshl_or_b32 v14, v81, 24, v14\n"
"s_lshl_b32 s94, s16, 2\n"
"s_add_u32 s94, s94, s17\n"
"s_and_b32 s95, s94, 7\n"
"s_lshr_b32 s96, s94, 3\n"
"s_lshl_b32 s95, s95, 7\n"
"s_add_u32 s15, s95, s96\n"
"s_lshl_b32 s96, s96, 15\n"
"s_add_u32 s95, s95, s96\n"
"v_add_u32 v92, s95, v13\n"
"s_mov_b32 s54, 131072\n"
"s_mov_b32 s55, 0x20000\n"
"s_mov_b32 s58, 131072\n"
"s_mov_b32 s59, 0x20000\n"
"s_mov_b32 s62, 4194304\n"
"s_mov_b32 s63, 0x20000\n"
"s_mov_b32 s66, 67108864\n"
"s_mov_b32 s67, 0x20000\n"
"s_mov_b32 s70, 2097152\n"
"s_mov_b32 s71, 0x20000\n"
"s_mov_b32 s74, 1024\n"
"s_mov_b32 s75, 0x20000\n"
"s_mov_b32 s78, 1409024\n"
"s_mov_b32 s79, 0x20000\n"
"s_mov_b32 s82, 1409024\n"
"s_mov_b32 s83, 0x20000\n"
"s_mov_b32 s86, 1409024\n"
"s_mov_b32 s87, 0x20000\n"
"v_mov_b32 v88, 0\n"
"s_and_b32 s7, s89, 3\n"
"s_lshl_b32 s7, s7, 2\n"
"s_add_u32 s7, s7, s17\n"
"s_lshl_b32 s7, s7, 10\n"
"s_mov_b64 exec, 0\n"
"s_cmp_eq_u32 s17, 0\n"
"s_cbranch_scc1 .Lpg1\n"
"s_cmp_eq_u32 s17, 1\n"
"s_cbranch_scc1 .Lpg2\n"
"s_cmp_eq_u32 s17, 2\n"
"s_cbranch_scc1 .Lpg3\n"
"s_cmp_eq_u32 s17, 3\n"
"s_cbranch_scc1 .Lpg4\n"
".Lpgret0:\n"
"s_mov_b64 exec, -1\n"
"s_xor_b32 s96, s6, 3072\n"
"s_add_u32 m0, s96, 32768\n"
"s_xor_b32 s94, s99, 196608\n"
"buffer_load_dwordx4 v19, s[48:51], s94 offen lds\n"
"s_waitcnt vmcnt(10)\n"
"v_lshlrev_b32 v60, 16, v44\n"
"v_and_b32 v61, 0xffff0000, v44\n"
"v_lshlrev_b32 v62, 16, v45\n"
"v_and_b32 v63, 0xffff0000, v45\n"
"v_lshlrev_b32 v64, 16, v46\n"
"v_and_b32 v65, 0xffff0000, v46\n"
"s_xor_b32 s96, s14, 2048\n"
"s_add_u32 m0, s96, 0\n"
"s_xor_b32 s94, s98, 16384\n"
"buffer_load_dwordx4 v16, s[44:47], s94 offen lds\n"
"v_lshlrev_b32 v66, 16, v47\n"
"v_and_b32 v67, 0xffff0000, v47\n"
"s_waitcnt vmcnt(10)\n"
"v_lshlrev_b32 v68, 16, v48\n"
"v_and_b32 v69, 0xffff0000, v48\n"
"v_lshlrev_b32 v70, 16, v49\n"
"v_and_b32 v71, 0xffff0000, v49\n"
"s_xor_b32 s96, s14, 3072\n"
"s_add_u32 m0, s96, 0\n"
"s_xor_b32 s94, s98, 24576\n"
"buffer_load_dwordx4 v17, s[44:47], s94 offen lds\n"
"v_lshlrev_b32 v72, 16, v50\n"
"v_and_b32 v73, 0xffff0000, v50\n"
"v_lshlrev_b32 v74, 16, v51\n"
"v_and_b32 v75, 0xffff0000, v51\n"
"v_add_f32 v76, v60, v61\n"
"v_add_f32 v77, v62, v63\n"
"s_xor_b32 s96, s14, 4096\n"
"s_add_u32 m0, s96, 0\n"
"s_xor_b32 s94, s98, 32768\n"
"buffer_load_dwordx4 v16, s[44:47], s94 offen lds\n"
"v_add_f32 v76, v76, v64\n"
"v_add_f32 v77, v77, v65\n"
"v_add_f32 v76, v76, v66\n"
"v_add_f32 v77, v77, v67\n"
"v_add_f32 v76, v76, v68\n"
"v_add_f32 v77, v77, v69\n"
"s_xor_b32 s96, s14, 5120\n"
"s_add_u32 m0, s96, 0\n"
"s_xor_b32 s94, s98, 40960\n"
"buffer_load_dwordx4 v17, s[44:47], s94 offen lds\n"
"v_add_f32 v76, v76, v70\n"
"v_add_f32 v77, v77, v71\n"
"v_add_f32 v76, v76, v72\n"
"v_add_f32 v77, v77, v73\n"
"v_add_f32 v76, v76, v74\n"
"v_add_f32 v77, v77, v75\n"
"s_xor_b32 s96, s14, 6144\n"
"s_add_u32 m0, s96, 0\n"
"s_xor_b32 s94, s98, 49152\n"
"buffer_load_dwordx4 v16, s[44:47], s94 offen lds\n"
"v_add_f32 v76, v76, v77\n"
"s_nop 1\n"
"v_add_f32_dpp v76, v76, v76 quad_perm:[1,0,3,2] row_mask:0xf bank_mask:0xf\n"
"s_nop 1\n"
"v_add_f32_dpp v76, v76, v76 quad_perm:[2,3,0,1] row_mask:0xf bank_mask:0xf\n"
"s_nop 1\n"
"v_add_f32_dpp v76, v76, v76 row_half_mirror row_mask:0xf bank_mask:0xf\n"
"s_nop 1\n"
"v_add_f32_dpp v76, v76, v76 row_mirror row_mask:0xf bank_mask:0xf\n"
"v_mov_b32 v77, v76\n"
"s_xor_b32 s96, s14, 7168\n"
"s_add_u32 m0, s96, 0\n"
"s_xor_b32 s94, s98, 57344\n"
"buffer_load_dwordx4 v17, s[44:47], s94 offen lds\n"
"v_permlane16_swap_b32 v76, v77\n"
"v_add_f32 v76, v76, v77\n"
"v_mul_f32 v76, 0x3b000000, v76\n"
"v_cvt_pk_bf16_f32 v78, v76, v76\n"
"v_and_b32 v78, 0xffff0000, v78\n"
"v_sub_f32 v60, v60, v78\n"
"s_xor_b32 s96, s6, 1024\n"
"s_add_u32 m0, s96, 49152\n"
"s_xor_b32 s94, s99, 589824\n"
"buffer_load_dwordx4 v19, s[48:51], s94 offen lds\n"
"v_sub_f32 v61, v61, v78\n"
"v_sub_f32 v62, v62, v78\n"
"v_sub_f32 v63, v63, v78\n"
"v_sub_f32 v64, v64, v78\n"
"v_sub_f32 v65, v65, v78\n"
"v_sub_f32 v66, v66, v78\n"
"s_xor_b32 s96, s6, 2048\n"
"s_add_u32 m0, s96, 49152\n"
"s_xor_b32 s94, s99, 655360\n"
"buffer_load_dwordx4 v18, s[48:51], s94 offen lds\n"
"v_sub_f32 v67, v67, v78\n"
"v_sub_f32 v68, v68, v78\n"
"v_sub_f32 v69, v69, v78\n"
"v_sub_f32 v70, v70, v78\n"
"v_sub_f32 v71, v71, v78\n"
"v_sub_f32 v72, v72, v78\n"
"s_xor_b32 s96, s6, 3072\n"
"s_add_u32 m0, s96, 49152\n"
"s_xor_b32 s94, s99, 720896\n"
"buffer_load_dwordx4 v19, s[48:51], s94 offen lds\n"
"v_sub_f32 v73, v73, v78\n"
"v_sub_f32 v74, v74, v78\n"
"v_sub_f32 v75, v75, v78\n"
"v_sub_f32 v76, v76, v78\n"
"v_mul_f32 v79, v60, v60\n"
"v_fmac_f32 v79, v61, v61\n"
"s_xor_b32 s96, s14, 2048\n"
"s_add_u32 m0, s96, 81920\n"
"s_xor_b32 s94, s98, 16512\n"
"buffer_load_dwordx4 v16, s[44:47], s94 offen lds\n"
"v_fmac_f32 v79, v62, v62\n"
"v_fmac_f32 v79, v63, v63\n"
"v_fmac_f32 v79, v64, v64\n"
"v_fmac_f32 v79, v65, v65\n"
"v_fmac_f32 v79, v66, v66\n"
"v_fmac_f32 v79, v67, v67\n"
"s_xor_b32 s96, s14, 3072\n"
"s_add_u32 m0, s96, 81920\n"
"s_xor_b32 s94, s98, 24704\n"
"buffer_load_dwordx4 v17, s[44:47], s94 offen lds\n"
"v_fmac_f32 v79, v68, v68\n"
"v_fmac_f32 v79, v69, v69\n"
"v_fmac_f32 v79, v70, v70\n"
"v_fmac_f32 v79, v71, v71\n"
"v_fmac_f32 v79, v72, v72\n"
"v_fmac_f32 v79, v73, v73\n"
"s_xor_b32 s96, s14, 4096\n"
"s_add_u32 m0, s96, 81920\n"
"s_xor_b32 s94, s98, 32896\n"
"buffer_load_dwordx4 v16, s[44:47], s94 offen lds\n"
"v_fmac_f32 v79, v74, v74\n"
"v_fmac_f32 v79, v75, v75\n"
"s_xor_b32 s96, s14, 5120\n"
"s_add_u32 m0, s96, 81920\n"
"s_xor_b32 s94, s98, 41088\n"
"buffer_load_dwordx4 v17, s[44:47], s94 offen lds\n"
"s_xor_b32 s96, s14, 6144\n"
"s_add_u32 m0, s96, 81920\n"
"s_xor_b32 s94, s98, 49280\n"
"buffer_load_dwordx4 v16, s[44:47], s94 offen lds\n"
"s_xor_b32 s96, s14, 7168\n"
"s_add_u32 m0, s96, 81920\n"
"s_xor_b32 s94, s98, 57472\n"
"buffer_load_dwordx4 v17, s[44:47], s94 offen lds\n"
"s_lshl_b32 s97, s88, 19\n"
"s_add_u32 s97, s97, 393216\n"
"s_waitcnt lgkmcnt(0)\n"
"s_mov_b32 s72, s30\n"
"s_and_b32 s73, s31, 0xffff\n"
"s_mov_b32 s64, s18\n"
"s_and_b32 s65, s19, 0xffff\n"
"s_add_u32 s52, s24, s97\n"
"s_addc_u32 s53, s25, 0\n"
"s_and_b32 s53, s53, 0xffff\n"
"s_add_u32 s56, s26, s97\n"
"s_addc_u32 s57, s27, 0\n"
"s_and_b32 s57, s57, 0xffff\n"
"s_mov_b32 s60, s28\n"
"s_and_b32 s61, s29, 0xffff\n"
"s_mov_b32 s68, s38\n"
"s_and_b32 s69, s39, 0xffff\n"
"s_mov_b32 s76, s32\n"
"s_and_b32 s77, s33, 0xffff\n"
"s_mov_b32 s80, s34\n"
"s_and_b32 s81, s35, 0xffff\n"
"s_mov_b32 s84, s36\n"
"s_and_b32 s85, s37, 0xffff\n"
"s_add_u32 m0, s6, 65536\n"
"s_xor_b32 s94, s99, 1048576\n"
"buffer_load_dwordx4 v18, s[48:51], s94 offen lds\n"
"s_add_u32 m0, s14, 131072\n"
"s_xor_b32 s94, s98, 256\n"
"buffer_load_dwordx4 v16, s[44:47], s94 offen lds\n"
"s_xor_b32 s96, s14, 1024\n"
"s_add_u32 m0, s96, 131072\n"
"s_xor_b32 s94, s98, 8448\n"
"buffer_load_dwordx4 v17, s[44:47], s94 offen lds\n"
"buffer_load_dwordx4 v[52:55], v94, s[72:75], 0 offen\n"
"buffer_load_dwordx4 v[56:59], v94, s[72:75], 0 offen offset:512\n"
"buffer_load_dword v12, v88, s[64:67], 0 offen sc1\n"
"s_cmp_lt_u32 s89, 4\n"
"s_cselect_b64 s[72:73], s[52:53], s[56:57]\n"
"s_mov_b32 s74, s54\n"
"s_add_u32 s97, s7, 0\n"
"buffer_load_dwordx4 a[144:147], v0, s[72:75], s97 offen\n"
"s_add_u32 s97, s7, 16384\n"
"buffer_load_dwordx4 a[144:147], v0, s[72:75], s97 offen\n"
"s_add_u32 s97, s7, 32768\n"
"buffer_load_dwordx4 a[144:147], v0, s[72:75], s97 offen\n"
"s_waitcnt vmcnt(18)\n"
"s_barrier\n"
"ds_read_b64_tr_b16 a[68:69], v24 offset:0\n"
"ds_read_b64_tr_b16 a[70:71], v24 offset:512\n"
"ds_read_b64_tr_b16 a[72:73], v24 offset:4096\n"
"ds_read_b64_tr_b16 a[74:75], v24 offset:4608\n"
"ds_read_b128 a[4:7], v20 offset:0\n"
"ds_read_b128 a[20:23], v21 offset:0\n"
"ds_read_b128 a[8:11], v20 offset:2048\n"
"ds_read_b128 a[24:27], v21 offset:2048\n"
"ds_read_b128 a[12:15], v20 offset:4096\n"
"ds_read_b128 a[28:31], v21 offset:4096\n"
"ds_read_b128 a[16:19], v20 offset:6144\n"
"ds_read_b128 a[32:35], v21 offset:6144\n"
"s_waitcnt lgkmcnt(7)\n"
"v_mfma_f32_16x16x32_bf16 v[128:131], a[68:71], a[4:7], 0\n"
"s_waitcnt lgkmcnt(6)\n"
"s_xor_b32 s96, s6, 1024\n"
"ds_read_b64_tr_b16 a[76:77], v25 offset:0\n"
"v_mfma_f32_16x16x32_bf16 v[128:131], a[72:75], a[20:23], v[128:131]\n"
"s_waitcnt lgkmcnt(5)\n"
"s_add_u32 m0, s96, 65536\n"
"ds_read_b64_tr_b16 a[78:79], v25 offset:512\n"
"v_mfma_f32_16x16x32_bf16 v[132:135], a[72:75], a[24:27], 0\n"
"s_xor_b32 s94, s99, 1114112\n"
"ds_read_b64_tr_b16 a[80:81], v25 offset:4096\n"
"v_mfma_f32_16x16x32_bf16 v[132:135], a[68:71], a[8:11], v[132:135]\n"
"s_waitcnt lgkmcnt(6)\n"
"ds_read_b64_tr_b16 a[82:83], v25 offset:4608\n"
"buffer_load_dwordx4 v19, s[48:51], s94 offen lds\n"
"v_mfma_f32_16x16x32_bf16 v[136:139], a[68:71], a[12:15], 0\n"
"s_waitcnt lgkmcnt(6)\n"
"ds_read_b64_tr_b16 a[84:85], v26 offset:0\n"
"v_mfma_f32_16x16x32_bf16 v[136:139], a[72:75], a[28:31], v[136:139]\n"
"s_waitcnt lgkmcnt(5)\n"
"s_xor_b32 s96, s6, 2048\n"
"ds_read_b64_tr_b16 a[86:87], v26 offset:512\n"
"v_mfma_f32_16x16x32_bf16 v[140:143], a[72:75], a[32:35], 0\n"
"s_add_u32 m0, s96, 65536\n"
"ds_read_b64_tr_b16 a[88:89], v26 offset:4096\n"
"v_mfma_f32_16x16x32_bf16 v[140:143], a[68:71], a[16:19], v[140:143]\n"
"s_xor_b32 s94, s99, 1179648\n"
"ds_read_b64_tr_b16 a[90:91], v26 offset:4608\n"
"v_mfma_f32_16x16x32_bf16 v[32:35], a[0:3], a[68:71], 0\n"
"buffer_load_dwordx4 v18, s[48:51], s94 offen lds\n"
"ds_read_b64_tr_b16 a[92:93], v27 offset:0\n"
"v_mfma_f32_16x16x32_bf16 v[32:35], a[0:3], a[72:75], v[32:35]\n"
"s_waitcnt lgkmcnt(7)\n"
"ds_read_b64_tr_b16 a[94:95], v27 offset:512\n"
"v_mfma_f32_16x16x32_bf16 v[156:159], a[76:79], a[16:19], 0\n"
"s_waitcnt lgkmcnt(6)\n"
"ds_read_b64_tr_b16 a[96:97], v27 offset:4096\n"
"v_mfma_f32_16x16x32_bf16 v[156:159], a[80:83], a[32:35], v[156:159]\n"
"s_xor_b32 s96, s6, 3072\n"
"ds_read_b64_tr_b16 a[98:99], v27 offset:4608\n"
"v_mfma_f32_16x16x32_bf16 v[152:155], a[80:83], a[28:31], 0\n"
"s_add_u32 m0, s96, 65536\n"
"ds_read_b64_tr_b16 a[68:69], v28 offset:0\n"
"v_mfma_f32_16x16x32_bf16 v[152:155], a[76:79], a[12:15], v[152:155]\n"
"s_xor_b32 s94, s99, 1245184\n"
"ds_read_b64_tr_b16 a[70:71], v28 offset:512\n"
"v_mfma_f32_16x16x32_bf16 v[148:151], a[76:79], a[8:11], 0\n"
"buffer_load_dwordx4 v19, s[48:51], s94 offen lds\n"
"ds_read_b64_tr_b16 a[72:73], v28 offset:4096\n"
"v_mfma_f32_16x16x32_bf16 v[148:151], a[80:83], a[24:27], v[148:151]\n"
"ds_read_b64_tr_b16 a[74:75], v28 offset:4608\n"
"v_mfma_f32_16x16x32_bf16 v[144:147], a[80:83], a[20:23], 0\n"
"v_mfma_f32_16x16x32_bf16 v[144:147], a[76:79], a[4:7], v[144:147]\n"
"s_xor_b32 s96, s14, 2048\n"
"v_mfma_f32_16x16x32_bf16 v[36:39], a[0:3], a[76:79], 0\n"
"s_add_u32 m0, s96, 131072\n"
"v_mfma_f32_16x16x32_bf16 v[36:39], a[0:3], a[80:83], v[36:39]\n"
"s_waitcnt lgkmcnt(8)\n"
"s_xor_b32 s94, s98, 16640\n"
"ds_read_b64_tr_b16 a[76:77], v29 offset:0\n"
"v_mfma_f32_16x16x32_bf16 v[160:163], a[84:87], a[4:7], 0\n"
"buffer_load_dwordx4 v16, s[44:47], s94 offen lds\n"
"ds_read_b64_tr_b16 a[78:79], v29 offset:512\n"
"v_mfma_f32_16x16x32_bf16 v[160:163], a[88:91], a[20:23], v[160:163]\n"
"ds_read_b64_tr_b16 a[80:81], v29 offset:4096\n"
"v_mfma_f32_16x16x32_bf16 v[164:167], a[88:91], a[24:27], 0\n"
"ds_read_b64_tr_b16 a[82:83], v29 offset:4608\n"
"v_mfma_f32_16x16x32_bf16 v[164:167], a[84:87], a[8:11], v[164:167]\n"
"s_xor_b32 s96, s14, 3072\n"
"v_mfma_f32_16x16x32_bf16 v[168:171], a[84:87], a[12:15], 0\n"
"s_add_u32 m0, s96, 131072\n"
"v_mfma_f32_16x16x32_bf16 v[168:171], a[88:91], a[28:31], v[168:171]\n"
"s_xor_b32 s94, s98, 24832\n"
"v_mfma_f32_16x16x32_bf16 v[172:175], a[88:91], a[32:35], 0\n"
"buffer_load_dwordx4 v17, s[44:47], s94 offen lds\n"
"v_mfma_f32_16x16x32_bf16 v[172:175], a[84:87], a[16:19], v[172:175]\n"
"s_waitcnt lgkmcnt(4)\n"
"ds_read_b64_tr_b16 a[84:85], v30 offset:0\n"
"v_mfma_f32_16x16x32_bf16 v[188:191], a[92:95], a[16:19], 0\n"
"ds_read_b64_tr_b16 a[86:87], v30 offset:512\n"
"v_mfma_f32_16x16x32_bf16 v[188:191], a[96:99], a[32:35], v[188:191]\n"
"s_xor_b32 s96, s14, 4096\n"
"ds_read_b64_tr_b16 a[88:89], v30 offset:4096\n"
"v_mfma_f32_16x16x32_bf16 v[184:187], a[96:99], a[28:31], 0\n"
"s_add_u32 m0, s96, 131072\n"
"ds_read_b64_tr_b16 a[90:91], v30 offset:4608\n"
"v_mfma_f32_16x16x32_bf16 v[184:187], a[92:95], a[12:15], v[184:187]\n"
"s_xor_b32 s94, s98, 33024\n"
"v_mfma_f32_16x16x32_bf16 v[180:183], a[92:95], a[8:11], 0\n"
"buffer_load_dwordx4 v16, s[44:47], s94 offen lds\n"
"v_mfma_f32_16x16x32_bf16 v[180:183], a[96:99], a[24:27], v[180:183]\n"
"v_mfma_f32_16x16x32_bf16 v[176:179], a[96:99], a[20:23], 0\n"
"v_mfma_f32_16x16x32_bf16 v[176:179], a[92:95], a[4:7], v[176:179]\n"
"s_xor_b32 s96, s14, 5120\n"
"ds_read_b64_tr_b16 a[92:93], v31 offset:0\n"
"v_mfma_f32_16x16x32_bf16 v[192:195], a[68:71], a[4:7], 0\n"
"s_add_u32 m0, s96, 131072\n"
"ds_read_b64_tr_b16 a[94:95], v31 offset:512\n"
"v_mfma_f32_16x16x32_bf16 v[192:195], a[72:75], a[20:23], v[192:195]\n"
"s_xor_b32 s94, s98, 41216\n"
"ds_read_b64_tr_b16 a[96:97], v31 offset:4096\n"
"v_mfma_f32_16x16x32_bf16 v[196:199], a[72:75], a[24:27], 0\n"
"buffer_load_dwordx4 v17, s[44:47], s94 offen lds\n"
"ds_read_b64_tr_b16 a[98:99], v31 offset:4608\n"
"v_mfma_f32_16x16x32_bf16 v[196:199], a[68:71], a[8:11], v[196:199]\n"
"s_waitcnt vmcnt(10)\n"
"v_readfirstlane_b32 s93, v12\n"
"v_lshlrev_b32 v82, 16, v52\n"
"v_mfma_f32_16x16x32_bf16 v[200:203], a[68:71], a[12:15], 0\n"
"v_and_b32 v83, 0xffff0000, v52\n"
"v_mul_f32 v80, v60, v82\n"
"v_mfma_f32_16x16x32_bf16 v[200:203], a[72:75], a[28:31], v[200:203]\n"
"s_xor_b32 s96, s14, 6144\n"
"v_fmac_f32 v80, v61, v83\n"
"v_lshlrev_b32 v82, 16, v53\n"
"v_mfma_f32_16x16x32_bf16 v[204:207], a[72:75], a[32:35], 0\n"
"s_add_u32 m0, s96, 131072\n"
"v_and_b32 v83, 0xffff0000, v53\n"
"v_fmac_f32 v80, v62, v82\n"
"v_mfma_f32_16x16x32_bf16 v[204:207], a[68:71], a[16:19], v[204:207]\n"
"s_waitcnt lgkmcnt(4)\n"
"s_xor_b32 s94, s98, 49408\n"
"v_fmac_f32 v80, v63, v83\n"
"v_mfma_f32_16x16x32_bf16 v[220:223], a[76:79], a[16:19], 0\n"
"s_mul_i32 s92, s93, 0x9e3779b1\n"
"buffer_load_dwordx4 v16, s[44:47], s94 offen lds\n"
"v_lshlrev_b32 v82, 16, v54\n"
"v_mfma_f32_16x16x32_bf16 v[220:223], a[80:83], a[32:35], v[220:223]\n"
"v_and_b32 v83, 0xffff0000, v54\n"
"v_fmac_f32 v80, v64, v82\n"
"v_mfma_f32_16x16x32_bf16 v[216:219], a[80:83], a[28:31], 0\n"
"v_fmac_f32 v80, v65, v83\n"
"v_mfma_f32_16x16x32_bf16 v[216:219], a[76:79], a[12:15], v[216:219]\n"
"s_xor_b32 s96, s14, 7168\n"
"v_mfma_f32_16x16x32_bf16 v[212:215], a[76:79], a[8:11], 0\n"
"s_add_u32 m0, s96, 131072\n"
"v_mfma_f32_16x16x32_bf16 v[212:215], a[80:83], a[24:27], v[212:215]\n"
"s_xor_b32 s94, s98, 57600\n"
"v_mfma_f32_16x16x32_bf16 v[208:211], a[80:83], a[20:23], 0\n"
"s_waitcnt lgkmcnt(0)\n"
"buffer_load_dwordx4 v17, s[44:47], s94 offen lds\n"
"v_mfma_f32_16x16x32_bf16 v[208:211], a[76:79], a[4:7], v[208:211]\n"
"s_barrier\n"
"v_mfma_f32_16x16x32_bf16 v[224:227], a[84:87], a[4:7], 0\n"
"s_xor_b32 s92, s92, 0x5bd1e995\n"
"ds_read_b128 a[36:39], v22 offset:0\n"
"v_lshlrev_b32 v82, 16, v55\n"
"v_mfma_f32_16x16x32_bf16 v[224:227], a[88:91], a[20:23], v[224:227]\n"
"ds_read_b64_tr_b16 a[68:69], v24 offset:16384\n"
"v_and_b32 v83, 0xffff0000, v55\n"
"v_mfma_f32_16x16x32_bf16 v[228:231], a[88:91], a[24:27], 0\n"
"ds_read_b64_tr_b16 a[70:71], v24 offset:16896\n"
"v_fmac_f32 v80, v66, v82\n"
"v_mfma_f32_16x16x32_bf16 v[228:231], a[84:87], a[8:11], v[228:231]\n"
"ds_read_b64_tr_b16 a[72:73], v24 offset:20480\n"
"v_fmac_f32 v80, v67, v83\n"
"v_mfma_f32_16x16x32_bf16 v[232:235], a[84:87], a[12:15], 0\n"
"s_add_u32 m0, s6, 32768\n"
"ds_read_b64_tr_b16 a[74:75], v24 offset:20992\n"
"v_lshlrev_b32 v82, 16, v56\n"
"v_mfma_f32_16x16x32_bf16 v[232:235], a[88:91], a[28:31], v[232:235]\n"
"s_xor_b32 s94, s99, 1572864\n"
"ds_read_b128 a[52:55], v23 offset:0\n"
"v_and_b32 v83, 0xffff0000, v56\n"
"v_mfma_f32_16x16x32_bf16 v[236:239], a[88:91], a[32:35], 0\n"
"ds_read_b128 a[40:43], v22 offset:2048\n"
"buffer_load_dwordx4 v18, s[48:51], s94 offen lds\n"
"v_mfma_f32_16x16x32_bf16 v[236:239], a[84:87], a[16:19], v[236:239]\n"
"ds_read_b128 a[56:59], v23 offset:2048\n"
"v_fmac_f32 v80, v68, v82\n"
"v_mfma_f32_16x16x32_bf16 v[252:255], a[92:95], a[16:19], 0\n"
"s_add_u32 m0, s14, 0\n"
"ds_read_b128 a[44:47], v22 offset:4096\n"
".Lpg1:\n"
"s_cbranch_execz .Lpg5\n"
"s_nop 0\n"
"v_fmac_f32 v80, v69, v83\n"
"v_mfma_f32_16x16x32_bf16 v[252:255], a[96:99], a[32:35], v[252:255]\n"
"s_xor_b32 s94, s98, 384\n"
"ds_read_b128 a[60:63], v23 offset:4096\n"
"v_lshlrev_b32 v82, 16, v57\n"
"v_mfma_f32_16x16x32_bf16 v[248:251], a[96:99], a[28:31], 0\n"
"buffer_load_dwordx4 v16, s[44:47], s94 offen lds\n"
"ds_read_b128 a[48:51], v22 offset:6144\n"
"v_mfma_f32_16x16x32_bf16 v[248:251], a[92:95], a[12:15], v[248:251]\n"
"ds_read_b128 a[64:67], v23 offset:6144\n"
"v_and_b32 v83, 0xffff0000, v57\n"
"v_mfma_f32_16x16x32_bf16 v[244:247], a[92:95], a[8:11], 0\n"
"s_waitcnt lgkmcnt(9)\n"
"ds_read_b64_tr_b16 a[76:77], v25 offset:16384\n"
"v_fmac_f32 v80, v70, v82\n"
"v_mfma_f32_16x16x32_bf16 v[244:247], a[96:99], a[24:27], v[244:247]\n"
"s_xor_b32 s96, s14, 1024\n"
"ds_read_b64_tr_b16 a[78:79], v25 offset:16896\n"
"v_fmac_f32 v80, v71, v83\n"
"v_mfma_f32_16x16x32_bf16 v[240:243], a[96:99], a[20:23], 0\n"
"s_add_u32 m0, s96, 0\n"
"ds_read_b64_tr_b16 a[80:81], v25 offset:20480\n"
"v_lshlrev_b32 v82, 16, v58\n"
"v_mfma_f32_16x16x32_bf16 v[240:243], a[92:95], a[4:7], v[240:243]\n"
"s_xor_b32 s94, s98, 8576\n"
"ds_read_b64_tr_b16 a[82:83], v25 offset:20992\n"
"v_and_b32 v83, 0xffff0000, v58\n"
"v_mfma_f32_16x16x32_bf16 v[128:131], a[68:71], a[36:39], v[128:131]\n"
"s_waitcnt lgkmcnt(10)\n"
"buffer_load_dwordx4 v17, s[44:47], s94 offen lds\n"
"ds_read_b64_tr_b16 a[84:85], v26 offset:16384\n"
"v_mfma_f32_16x16x32_bf16 v[128:131], a[72:75], a[52:55], v[128:131]\n"
"s_waitcnt lgkmcnt(8)\n"
"ds_read_b64_tr_b16 a[86:87], v26 offset:16896\n"
"v_fmac_f32 v80, v72, v82\n"
"v_mfma_f32_16x16x32_bf16 v[132:135], a[72:75], a[56:59], v[132:135]\n"
"s_xor_b32 s96, s6, 1024\n"
"ds_read_b64_tr_b16 a[88:89], v26 offset:20480\n"
"v_fmac_f32 v80, v73, v83\n"
"v_mfma_f32_16x16x32_bf16 v[132:135], a[68:71], a[40:43], v[132:135]\n"
"s_add_u32 m0, s96, 32768\n"
"ds_read_b64_tr_b16 a[90:91], v26 offset:20992\n"
"v_lshlrev_b32 v82, 16, v59\n"
"v_mfma_f32_16x16x32_bf16 v[136:139], a[68:71], a[44:47], v[136:139]\n"
"s_waitcnt lgkmcnt(8)\n"
"s_xor_b32 s94, s99, 1638400\n"
"ds_read_b64_tr_b16 a[92:93], v27 offset:16384\n"
"v_and_b32 v83, 0xffff0000, v59\n"
"v_mfma_f32_16x16x32_bf16 v[136:139], a[72:75], a[60:63], v[136:139]\n"
"buffer_load_dwordx4 v19, s[48:51], s94 offen lds\n"
"ds_read_b64_tr_b16 a[94:95], v27 offset:16896\n"
"v_mfma_f32_16x16x32_bf16 v[140:143], a[72:75], a[64:67], v[140:143]\n"
"s_xor_b32 s96, s6, 2048\n"
"ds_read_b64_tr_b16 a[96:97], v27 offset:20480\n"
"v_fmac_f32 v80, v74, v82\n"
"v_mfma_f32_16x16x32_bf16 v[140:143], a[68:71], a[48:51], v[140:143]\n"
"s_add_u32 m0, s96, 32768\n"
"ds_read_b64_tr_b16 a[98:99], v27 offset:20992\n"
"v_fmac_f32 v80, v75, v83\n"
"v_mfma_f32_16x16x32_bf16 v[32:35], a[0:3], a[68:71], v[32:35]\n"
"s_xor_b32 s94, s99, 1703936\n"
"v_mov_b32 v12, s92\n"
"v_add_f32_dpp v79, v79, v79 quad_perm:[1,0,3,2] row_mask:0xf bank_mask:0xf\n"
"v_mfma_f32_16x16x32_bf16 v[32:35], a[0:3], a[72:75], v[32:35]\n"
"s_waitcnt lgkmcnt(10)\n"
"buffer_load_dwordx4 v18, s[48:51], s94 offen lds\n"
"ds_read_b64_tr_b16 a[68:69], v28 offset:16384\n"
"v_mfma_f32_16x16x32_bf16 v[156:159], a[76:79], a[48:51], v[156:159]\n"
"s_waitcnt lgkmcnt(9)\n"
"ds_read_b64_tr_b16 a[70:71], v28 offset:16896\n"
"v_add_f32_dpp v80, v80, v80 quad_perm:[1,0,3,2] row_mask:0xf bank_mask:0xf\n"
"v_mfma_f32_16x16x32_bf16 v[156:159], a[80:83], a[64:67], v[156:159]\n"
"s_xor_b32 s96, s6, 3072\n"
"ds_read_b64_tr_b16 a[72:73], v28 offset:20480\n"
"v_add_f32_dpp v79, v79, v79 quad_perm:[2,3,0,1] row_mask:0xf bank_mask:0xf\n"
"v_mfma_f32_16x16x32_bf16 v[152:155], a[80:83], a[60:63], v[152:155]\n"
"s_add_u32 m0, s96, 32768\n"
"ds_read_b64_tr_b16 a[74:75], v28 offset:20992\n"
"v_add_f32_dpp v80, v80, v80 quad_perm:[2,3,0,1] row_mask:0xf bank_mask:0xf\n"
"v_mfma_f32_16x16x32_bf16 v[152:155], a[76:79], a[44:47], v[152:155]\n"
"s_xor_b32 s94, s99, 1769472\n"
"v_add_f32_dpp v79, v79, v79 row_half_mirror row_mask:0xf bank_mask:0xf\n"
"v_add_f32_dpp v80, v80, v80 row_half_mirror row_mask:0xf bank_mask:0xf\n"
"v_mfma_f32_16x16x32_bf16 v[148:151], a[76:79], a[40:43], v[148:151]\n"
"buffer_load_dwordx4 v19, s[48:51], s94 offen lds\n"
"v_add_f32_dpp v79, v79, v79 row_mirror row_mask:0xf bank_mask:0xf\n"
"v_mfma_f32_16x16x32_bf16 v[148:151], a[80:83], a[56:59], v[148:151]\n"
"v_add_f32_dpp v80, v80, v80 row_mirror row_mask:0xf bank_mask:0xf\n"
"v_mov_b32 v82, v79\n"
"v_mfma_f32_16x16x32_bf16 v[144:147], a[80:83], a[52:55], v[144:147]\n"
"s_xor_b32 s96, s14, 2048\n"
"v_mov_b32 v83, v80\n"
"v_permlane16_swap_b32 v79, v82\n"
"v_mfma_f32_16x16x32_bf16 v[144:147], a[76:79], a[36:39], v[144:147]\n"
"s_add_u32 m0, s96, 0\n"
"v_permlane16_swap_b32 v80, v83\n"
"v_add_f32 v79, v79, v82\n"
"v_mfma_f32_16x16x32_bf16 v[36:39], a[0:3], a[76:79], v[36:39]\n"
"s_xor_b32 s94, s98, 16768\n"
"v_add_f32 v80, v80, v83\n"
"v_mul_f32 v79, 0x3b000000, v79\n"
"v_mfma_f32_16x16x32_bf16 v[36:39], a[0:3], a[80:83], v[36:39]\n"
"s_waitcnt lgkmcnt(4)\n"
"buffer_load_dwordx4 v16, s[44:47], s94 offen lds\n"
"ds_read_b64_tr_b16 a[76:77], v29 offset:16384\n"
"v_mfma_f32_16x16x32_bf16 v[160:163], a[84:87], a[36:39], v[160:163]\n"
"s_xor_b32 s96, s14, 3072\n"
"ds_read_b64_tr_b16 a[78:79], v29 offset:16896\n"
"v_fma_f32 v79, -v76, v76, v79\n"
"v_mfma_f32_16x16x32_bf16 v[160:163], a[88:91], a[52:55], v[160:163]\n"
"s_add_u32 m0, s96, 0\n"
"ds_read_b64_tr_b16 a[80:81], v29 offset:20480\n"
"v_max_f32 v79, 0, v79\n"
"v_mfma_f32_16x16x32_bf16 v[164:167], a[88:91], a[56:59], v[164:167]\n"
"s_xor_b32 s94, s98, 24960\n"
"ds_read_b64_tr_b16 a[82:83], v29 offset:20992\n"
"v_cvt_pk_bf16_f32 v79, v79, v79\n"
"v_mfma_f32_16x16x32_bf16 v[164:167], a[84:87], a[40:43], v[164:167]\n"
"buffer_load_dwordx4 v17, s[44:47], s94 offen lds\n"
"v_and_b32 v79, 0xffff0000, v79\n"
"v_mfma_f32_16x16x32_bf16 v[168:171], a[84:87], a[44:47], v[168:171]\n"
"v_add_f32 v79, 0x3727c5ac, v79\n"
"v_cvt_pk_bf16_f32 v79, v79, v79\n"
"v_mfma_f32_16x16x32_bf16 v[168:171], a[88:91], a[60:63], v[168:171]\n"
"s_xor_b32 s96, s14, 4096\n"
"v_and_b32 v79, 0xffff0000, v79\n"
"v_mfma_f32_16x16x32_bf16 v[172:175], a[88:91], a[64:67], v[172:175]\n"
"s_add_u32 m0, s96, 0\n"
"v_sqrt_f32 v79, v79\n"
"v_mfma_f32_16x16x32_bf16 v[172:175], a[84:87], a[48:51], v[172:175]\n"
"s_xor_b32 s94, s98, 33152\n"
"ds_read_b64_tr_b16 a[84:85], v30 offset:16384\n"
"v_mov_b32 v84, s92\n"
"v_mfma_f32_16x16x32_bf16 v[188:191], a[92:95], a[48:51], v[188:191]\n"
"buffer_load_dwordx4 v16, s[44:47], s94 offen lds\n"
"ds_read_b64_tr_b16 a[86:87], v30 offset:16896\n"
"v_mfma_f32_16x16x32_bf16 v[188:191], a[96:99], a[64:67], v[188:191]\n"
"ds_read_b64_tr_b16 a[88:89], v30 offset:20480\n"
"v_cvt_pk_bf16_f32 v79, v79, v79\n"
"v_mfma_f32_16x16x32_bf16 v[184:187], a[96:99], a[60:63], v[184:187]\n"
"s_xor_b32 s96, s14, 5120\n"
"ds_read_b64_tr_b16 a[90:91], v30 offset:20992\n"
"v_and_b32 v79, 0xffff0000, v79\n"
"v_mfma_f32_16x16x32_bf16 v[184:187], a[92:95], a[44:47], v[184:187]\n"
"s_add_u32 m0, s96, 0\n"
"v_rcp_f32 v81, v79\n"
"v_mfma_f32_16x16x32_bf16 v[180:183], a[92:95], a[40:43], v[180:183]\n"
"s_xor_b32 s94, s98, 41344\n"
"v_lshrrev_b32 v85, 16, v78\n"
"v_or_b32 v85, v85, v79\n"
"v_mfma_f32_16x16x32_bf16 v[180:183], a[96:99], a[56:59], v[180:183]\n"
"buffer_load_dwordx4 v17, s[44:47], s94 offen lds\n"
"v_mul_f32 v80, v80, v81\n"
"v_mfma_f32_16x16x32_bf16 v[176:179], a[96:99], a[52:55], v[176:179]\n"
"s_xor_b32 s96, s14, 6144\n"
"v_mfma_f32_16x16x32_bf16 v[176:179], a[92:95], a[36:39], v[176:179]\n"
"s_waitcnt lgkmcnt(4)\n"
"s_add_u32 m0, s96, 0\n"
"ds_read_b64_tr_b16 a[92:93], v31 offset:16384\n"
"v_mfma_f32_16x16x32_bf16 v[192:195], a[68:71], a[36:39], v[192:195]\n"
"s_xor_b32 s94, s98, 49536\n"
"ds_read_b64_tr_b16 a[94:95], v31 offset:16896\n"
"v_mfma_f32_16x16x32_bf16 v[192:195], a[72:75], a[52:55], v[192:195]\n"
"buffer_load_dwordx4 v16, s[44:47], s94 offen lds\n"
"ds_read_b64_tr_b16 a[96:97], v31 offset:20480\n"
"v_mfma_f32_16x16x32_bf16 v[196:199], a[72:75], a[56:59], v[196:199]\n"
"s_mov_b32 s24, 4096\n"
"ds_read_b64_tr_b16 a[98:99], v31 offset:20992\n"
"v_mfma_f32_16x16x32_bf16 v[196:199], a[68:71], a[40:43], v[196:199]\n"
"s_xor_b32 s96, s14, 7168\n"
"buffer_store_dwordx2 v[84:85], v90, s[64:67], s24 offen sc1\n"
"v_cvt_pk_bf16_f32 v80, v80, v80\n"
"v_mfma_f32_16x16x32_bf16 v[200:203], a[68:71], a[44:47], v[200:203]\n"
"s_add_u32 m0, s96, 0\n"
"v_ashrrev_i32 v81, 31, v80\n"
"v_or_b32 v81, 0x80000000, v81\n"
"v_mfma_f32_16x16x32_bf16 v[200:203], a[72:75], a[60:63], v[200:203]\n"
"s_xor_b32 s94, s98, 57728\n"
"v_xor_b32 v80, v80, v81\n"
"v_and_b32 v80, 0xffff0000, v80\n"
"v_mfma_f32_16x16x32_bf16 v[204:207], a[72:75], a[64:67], v[204:207]\n"
"buffer_load_dwordx4 v17, s[44:47], s94 offen lds\n"
"v_sub_u32 v81, 0x7ff, v95\n"
"v_mfma_f32_16x16x32_bf16 v[204:207], a[68:71], a[48:51], v[204:207]\n"
"v_mov_b32 v86, s92\n"
"v_or_b32 v87, v80, v81\n"
"v_mfma_f32_16x16x32_bf16 v[220:223], a[76:79], a[48:51], v[220:223]\n"
"s_mov_b32 s25, 32768\n"
"v_mfma_f32_16x16x32_bf16 v[220:223], a[80:83], a[64:67], v[220:223]\n"
"buffer_store_dwordx2 v[86:87], v90, s[64:67], s25 offen sc1\n"
"v_mfma_f32_16x16x32_bf16 v[216:219], a[80:83], a[60:63], v[216:219]\n"
"v_mfma_f32_16x16x32_bf16 v[216:219], a[76:79], a[44:47], v[216:219]\n"
"s_waitcnt vmcnt(14) lgkmcnt(0)\n"
"ds_read_b128 a[4:7], v22 offset:49152\n"
"v_mfma_f32_16x16x32_bf16 v[212:215], a[76:79], a[40:43], v[212:215]\n"
"s_barrier\n"
"v_mfma_f32_16x16x32_bf16 v[212:215], a[80:83], a[56:59], v[212:215]\n"
"s_add_u32 m0, s6, 49152\n"
"ds_read_b64_tr_b16 a[68:69], v24 offset:32768\n"
"v_mfma_f32_16x16x32_bf16 v[208:211], a[80:83], a[52:55], v[208:211]\n"
"s_xor_b32 s94, s99, 2097152\n"
"ds_read_b64_tr_b16 a[70:71], v24 offset:33280\n"
"v_mfma_f32_16x16x32_bf16 v[208:211], a[76:79], a[36:39], v[208:211]\n"
"buffer_load_dwordx4 v18, s[48:51], s94 offen lds\n"
"ds_read_b64_tr_b16 a[72:73], v24 offset:36864\n"
"v_mfma_f32_16x16x32_bf16 v[224:227], a[84:87], a[36:39], v[224:227]\n"
"s_add_u32 m0, s14, 81920\n"
"ds_read_b64_tr_b16 a[74:75], v24 offset:37376\n"
"v_mfma_f32_16x16x32_bf16 v[224:227], a[88:91], a[52:55], v[224:227]\n"
"s_xor_b32 s94, s98, 512\n"
"ds_read_b128 a[20:23], v23 offset:49152\n"
"v_mfma_f32_16x16x32_bf16 v[228:231], a[88:91], a[56:59], v[228:231]\n"
"buffer_load_dwordx4 v16, s[44:47], s94 offen lds\n"
"ds_read_b128 a[8:11], v22 offset:51200\n"
"v_mfma_f32_16x16x32_bf16 v[228:231], a[84:87], a[40:43], v[228:231]\n"
"s_xor_b32 s96, s14, 1024\n"
"ds_read_b128 a[24:27], v23 offset:51200\n"
"v_mfma_f32_16x16x32_bf16 v[232:235], a[84:87], a[44:47], v[232:235]\n"
"s_add_u32 m0, s96, 81920\n"
"ds_read_b128 a[12:15], v22 offset:53248\n"
"v_mfma_f32_16x16x32_bf16 v[232:235], a[88:91], a[60:63], v[232:235]\n"
"s_xor_b32 s94, s98, 8704\n"
"ds_read_b128 a[28:31], v23 offset:53248\n"
"v_mfma_f32_16x16x32_bf16 v[236:239], a[88:91], a[64:67], v[236:239]\n"
"buffer_load_dwordx4 v17, s[44:47], s94 offen lds\n"
"ds_read_b128 a[16:19], v22 offset:55296\n"
"v_mfma_f32_16x16x32_bf16 v[236:239], a[84:87], a[48:51], v[236:239]\n"
"ds_read_b128 a[32:35], v23 offset:55296\n"
"v_mfma_f32_16x16x32_bf16 v[252:255], a[92:95], a[48:51], v[252:255]\n"
"ds_read_b64_tr_b16 a[76:77], v25 offset:32768\n"
"v_mfma_f32_16x16x32_bf16 v[252:255], a[96:99], a[64:67], v[252:255]\n"
"s_xor_b32 s96, s6, 1024\n"
"ds_read_b64_tr_b16 a[78:79], v25 offset:33280\n"
"v_mfma_f32_16x16x32_bf16 v[248:251], a[96:99], a[60:63], v[248:251]\n"
"s_add_u32 m0, s96, 49152\n"
"ds_read_b64_tr_b16 a[80:81], v25 offset:36864\n"
"v_mfma_f32_16x16x32_bf16 v[248:251], a[92:95], a[44:47], v[248:251]\n"
"s_xor_b32 s94, s99, 2162688\n"
"ds_read_b64_tr_b16 a[82:83], v25 offset:37376\n"
"v_mfma_f32_16x16x32_bf16 v[244:247], a[92:95], a[40:43], v[244:247]\n"
"buffer_load_dwordx4 v19, s[48:51], s94 offen lds\n"
"ds_read_b64_tr_b16 a[84:85], v26 offset:32768\n"
"v_mfma_f32_16x16x32_bf16 v[244:247], a[96:99], a[56:59], v[244:247]\n"
"ds_read_b64_tr_b16 a[86:87], v26 offset:33280\n"
"v_mfma_f32_16x16x32_bf16 v[240:243], a[96:99], a[52:55], v[240:243]\n"
"s_xor_b32 s96, s6, 2048\n"
"ds_read_b64_tr_b16 a[88:89], v26 offset:36864\n"
"v_mfma_f32_16x16x32_bf16 v[240:243], a[92:95], a[36:39], v[240:243]\n"
"s_waitcnt lgkmcnt(11)\n"
"s_add_u32 m0, s96, 49152\n"
"ds_read_b64_tr_b16 a[90:91], v26 offset:37376\n"
"v_mfma_f32_16x16x32_bf16 v[128:131], a[68:71], a[4:7], v[128:131]\n"
"s_xor_b32 s94, s99, 2228224\n"
"ds_read_b64_tr_b16 a[92:93], v27 offset:32768\n"
"v_mfma_f32_16x16x32_bf16 v[128:131], a[72:75], a[20:23], v[128:131]\n"
"buffer_load_dwordx4 v18, s[48:51], s94 offen lds\n"
"ds_read_b64_tr_b16 a[94:95], v27 offset:33280\n"
"v_mfma_f32_16x16x32_bf16 v[132:135], a[72:75], a[24:27], v[132:135]\n"
"ds_read_b64_tr_b16 a[96:97], v27 offset:36864\n"
"v_mfma_f32_16x16x32_bf16 v[132:135], a[68:71], a[8:11], v[132:135]\n"
"s_waitcnt lgkmcnt(11)\n"
"ds_read_b64_tr_b16 a[98:99], v27 offset:37376\n"
"v_mfma_f32_16x16x32_bf16 v[136:139], a[68:71], a[12:15], v[136:139]\n"
"s_xor_b32 s96, s6, 3072\n"
"v_mfma_f32_16x16x32_bf16 v[136:139], a[72:75], a[28:31], v[136:139]\n"
"s_add_u32 m0, s96, 49152\n"
"v_mfma_f32_16x16x32_bf16 v[140:143], a[72:75], a[32:35], v[140:143]\n"
"s_xor_b32 s94, s99, 2293760\n"
"v_mfma_f32_16x16x32_bf16 v[140:143], a[68:71], a[16:19], v[140:143]\n"
"buffer_load_dwordx4 v19, s[48:51], s94 offen lds\n"
"v_mfma_f32_16x16x32_bf16 v[32:35], a[0:3], a[68:71], v[32:35]\n"
"v_mfma_f32_16x16x32_bf16 v[32:35], a[0:3], a[72:75], v[32:35]\n"
"s_waitcnt lgkmcnt(6)\n"
"ds_read_b64_tr_b16 a[68:69], v28 offset:32768\n"
"v_mfma_f32_16x16x32_bf16 v[156:159], a[76:79], a[16:19], v[156:159]\n"
"s_xor_b32 s96, s14, 2048\n"
"ds_read_b64_tr_b16 a[70:71], v28 offset:33280\n"
"v_mfma_f32_16x16x32_bf16 v[156:159], a[80:83], a[32:35], v[156:159]\n"
"s_add_u32 m0, s96, 81920\n"
"ds_read_b64_tr_b16 a[72:73], v28 offset:36864\n"
"v_mfma_f32_16x16x32_bf16 v[152:155], a[80:83], a[28:31], v[152:155]\n"
"s_xor_b32 s94, s98, 16896\n"
"ds_read_b64_tr_b16 a[74:75], v28 offset:37376\n"
"v_mfma_f32_16x16x32_bf16 v[152:155], a[76:79], a[12:15], v[152:155]\n"
"buffer_load_dwordx4 v16, s[44:47], s94 offen lds\n"
"v_mfma_f32_16x16x32_bf16 v[148:151], a[76:79], a[8:11], v[148:151]\n"
"v_mfma_f32_16x16x32_bf16 v[148:151], a[80:83], a[24:27], v[148:151]\n"
"s_xor_b32 s96, s14, 3072\n"
"v_mfma_f32_16x16x32_bf16 v[144:147], a[80:83], a[20:23], v[144:147]\n"
"s_add_u32 m0, s96, 81920\n"
"v_mfma_f32_16x16x32_bf16 v[144:147], a[76:79], a[4:7], v[144:147]\n"
"s_xor_b32 s94, s98, 25088\n"
"v_mfma_f32_16x16x32_bf16 v[36:39], a[0:3], a[76:79], v[36:39]\n"
"buffer_load_dwordx4 v17, s[44:47], s94 offen lds\n"
"v_mfma_f32_16x16x32_bf16 v[36:39], a[0:3], a[80:83], v[36:39]\n"
"ds_read_b64_tr_b16 a[76:77], v29 offset:32768\n"
"v_mfma_f32_16x16x32_bf16 v[160:163], a[84:87], a[4:7], v[160:163]\n"
"s_waitcnt lgkmcnt(5)\n"
"ds_read_b64_tr_b16 a[78:79], v29 offset:33280\n"
"v_mfma_f32_16x16x32_bf16 v[160:163], a[88:91], a[20:23], v[160:163]\n"
"s_xor_b32 s96, s14, 4096\n"
"ds_read_b64_tr_b16 a[80:81], v29 offset:36864\n"
"v_mfma_f32_16x16x32_bf16 v[164:167], a[88:91], a[24:27], v[164:167]\n"
"s_add_u32 m0, s96, 81920\n"
"ds_read_b64_tr_b16 a[82:83], v29 offset:37376\n"
"v_mfma_f32_16x16x32_bf16 v[164:167], a[84:87], a[8:11], v[164:167]\n"
"s_xor_b32 s94, s98, 33280\n"
"v_mfma_f32_16x16x32_bf16 v[168:171], a[84:87], a[12:15], v[168:171]\n"
"buffer_load_dwordx4 v16, s[44:47], s94 offen lds\n"
"v_mfma_f32_16x16x32_bf16 v[168:171], a[88:91], a[28:31], v[168:171]\n"
"v_mfma_f32_16x16x32_bf16 v[172:175], a[88:91], a[32:35], v[172:175]\n"
"v_mfma_f32_16x16x32_bf16 v[172:175], a[84:87], a[16:19], v[172:175]\n"
"s_xor_b32 s96, s14, 5120\n"
"ds_read_b64_tr_b16 a[84:85], v30 offset:32768\n"
"v_mfma_f32_16x16x32_bf16 v[188:191], a[92:95], a[16:19], v[188:191]\n"
"s_add_u32 m0, s96, 81920\n"
"ds_read_b64_tr_b16 a[86:87], v30 offset:33280\n"
"v_mfma_f32_16x16x32_bf16 v[188:191], a[96:99], a[32:35], v[188:191]\n"
"s_xor_b32 s94, s98, 41472\n"
"ds_read_b64_tr_b16 a[88:89], v30 offset:36864\n"
"v_mfma_f32_16x16x32_bf16 v[184:187], a[96:99], a[28:31], v[184:187]\n"
"buffer_load_dwordx4 v17, s[44:47], s94 offen lds\n"
"ds_read_b64_tr_b16 a[90:91], v30 offset:37376\n"
"v_mfma_f32_16x16x32_bf16 v[184:187], a[92:95], a[12:15], v[184:187]\n"
"v_mfma_f32_16x16x32_bf16 v[180:183], a[92:95], a[8:11], v[180:183]\n"
"s_xor_b32 s96, s14, 6144\n"
"v_mfma_f32_16x16x32_bf16 v[180:183], a[96:99], a[24:27], v[180:183]\n"
"s_add_u32 m0, s96, 81920\n"
"v_mfma_f32_16x16x32_bf16 v[176:179], a[96:99], a[20:23], v[176:179]\n"
"s_waitcnt lgkmcnt(5)\n"
"s_xor_b32 s94, s98, 49664\n"
"v_mfma_f32_16x16x32_bf16 v[176:179], a[92:95], a[4:7], v[176:179]\n"
"buffer_load_dwordx4 v16, s[44:47], s94 offen lds\n"
"ds_read_b64_tr_b16 a[92:93], v31 offset:32768\n"
"v_mfma_f32_16x16x32_bf16 v[192:195], a[68:71], a[4:7], v[192:195]\n"
"ds_read_b64_tr_b16 a[94:95], v31 offset:33280\n"
"v_mfma_f32_16x16x32_bf16 v[192:195], a[72:75], a[20:23], v[192:195]\n"
"ds_read_b64_tr_b16 a[96:97], v31 offset:36864\n"
"v_mfma_f32_16x16x32_bf16 v[196:199], a[72:75], a[24:27], v[196:199]\n"
"s_xor_b32 s96, s14, 7168\n"
"ds_read_b64_tr_b16 a[98:99], v31 offset:37376\n"
"v_mfma_f32_16x16x32_bf16 v[196:199], a[68:71], a[8:11], v[196:199]\n"
"s_add_u32 m0, s96, 81920\n"
"v_mfma_f32_16x16x32_bf16 v[200:203], a[68:71], a[12:15], v[200:203]\n"
"s_xor_b32 s94, s98, 57856\n"
"v_mfma_f32_16x16x32_bf16 v[200:203], a[72:75], a[28:31], v[200:203]\n"
"buffer_load_dwordx4 v17, s[44:47], s94 offen lds\n"
"v_mfma_f32_16x16x32_bf16 v[204:207], a[72:75], a[32:35], v[204:207]\n"
"v_mfma_f32_16x16x32_bf16 v[204:207], a[68:71], a[16:19], v[204:207]\n"
"v_mfma_f32_16x16x32_bf16 v[220:223], a[76:79], a[16:19], v[220:223]\n"
"s_waitcnt lgkmcnt(4)\n"
"v_mfma_f32_16x16x32_bf16 v[220:223], a[80:83], a[32:35], v[220:223]\n"
"v_mfma_f32_16x16x32_bf16 v[216:219], a[80:83], a[28:31], v[216:219]\n"
"v_mfma_f32_16x16x32_bf16 v[216:219], a[76:79], a[12:15], v[216:219]\n"
"s_waitcnt vmcnt(13) lgkmcnt(0)\n"
"ds_read_b128 a[36:39], v20 offset:0\n"
"v_mfma_f32_16x16x32_bf16 v[212:215], a[76:79], a[8:11], v[212:215]\n"
"s_barrier\n"
"v_mfma_f32_16x16x32_bf16 v[212:215], a[80:83], a[24:27], v[212:215]\n"
"s_add_u32 m0, s6, 65536\n"
"ds_read_b64_tr_b16 a[68:69], v24 offset:0\n"
"v_mfma_f32_16x16x32_bf16 v[208:211], a[80:83], a[20:23], v[208:211]\n"
"s_xor_b32 s94, s99, 2621440\n"
"ds_read_b64_tr_b16 a[70:71], v24 offset:512\n"
"v_mfma_f32_16x16x32_bf16 v[208:211], a[76:79], a[4:7], v[208:211]\n"
"buffer_load_dwordx4 v18, s[48:51], s94 offen lds\n"
"ds_read_b64_tr_b16 a[72:73], v24 offset:4096\n"
"v_mfma_f32_16x16x32_bf16 v[224:227], a[84:87], a[4:7], v[224:227]\n"
"s_add_u32 m0, s14, 131072\n"
"ds_read_b64_tr_b16 a[74:75], v24 offset:4608\n"
"v_mfma_f32_16x16x32_bf16 v[224:227], a[88:91], a[20:23], v[224:227]\n"
"s_xor_b32 s94, s98, 640\n"
"ds_read_b128 a[52:55], v21 offset:0\n"
"v_mfma_f32_16x16x32_bf16 v[228:231], a[88:91], a[24:27], v[228:231]\n"
"buffer_load_dwordx4 v16, s[44:47], s94 offen lds\n"
"ds_read_b128 a[40:43], v20 offset:2048\n"
"v_mfma_f32_16x16x32_bf16 v[228:231], a[84:87], a[8:11], v[228:231]\n"
"s_xor_b32 s96, s14, 1024\n"
"ds_read_b128 a[56:59], v21 offset:2048\n"
"v_mfma_f32_16x16x32_bf16 v[232:235], a[84:87], a[12:15], v[232:235]\n"
"s_add_u32 m0, s96, 131072\n"
"ds_read_b128 a[44:47], v20 offset:4096\n"
"v_mfma_f32_16x16x32_bf16 v[232:235], a[88:91], a[28:31], v[232:235]\n"
"s_xor_b32 s94, s98, 8832\n"
"ds_read_b128 a[60:63], v21 offset:4096\n"
"v_mfma_f32_16x16x32_bf16 v[236:239], a[88:91], a[32:35], v[236:239]\n"
"buffer_load_dwordx4 v17, s[44:47], s94 offen lds\n"
"ds_read_b128 a[48:51], v20 offset:6144\n"
"v_mfma_f32_16x16x32_bf16 v[236:239], a[84:87], a[16:19], v[236:239]\n"
"ds_read_b128 a[64:67], v21 offset:6144\n"
"v_mfma_f32_16x16x32_bf16 v[252:255], a[92:95], a[16:19], v[252:255]\n"
"ds_read_b64_tr_b16 a[76:77], v25 offset:0\n"
"v_mfma_f32_16x16x32_bf16 v[252:255], a[96:99], a[32:35], v[252:255]\n"
"s_xor_b32 s96, s6, 1024\n"
"ds_read_b64_tr_b16 a[78:79], v25 offset:512\n"
"v_mfma_f32_16x16x32_bf16 v[248:251], a[96:99], a[28:31], v[248:251]\n"
"s_add_u32 m0, s96, 65536\n"
"ds_read_b64_tr_b16 a[80:81], v25 offset:4096\n"
"v_mfma_f32_16x16x32_bf16 v[248:251], a[92:95], a[12:15], v[248:251]\n"
"s_xor_b32 s94, s99, 2686976\n"
"ds_read_b64_tr_b16 a[82:83], v25 offset:4608\n"
"v_mfma_f32_16x16x32_bf16 v[244:247], a[92:95], a[8:11], v[244:247]\n"
"buffer_load_dwordx4 v19, s[48:51], s94 offen lds\n"
"ds_read_b64_tr_b16 a[84:85], v26 offset:0\n"
"v_mfma_f32_16x16x32_bf16 v[244:247], a[96:99], a[24:27], v[244:247]\n"
"ds_read_b64_tr_b16 a[86:87], v26 offset:512\n"
"v_mfma_f32_16x16x32_bf16 v[240:243], a[96:99], a[20:23], v[240:243]\n"
"s_xor_b32 s96, s6, 2048\n"
"ds_read_b64_tr_b16 a[88:89], v26 offset:4096\n"
"v_mfma_f32_16x16x32_bf16 v[240:243], a[92:95], a[4:7], v[240:243]\n"
"s_waitcnt lgkmcnt(11)\n"
"s_add_u32 m0, s96, 65536\n"
"ds_read_b64_tr_b16 a[90:91], v26 offset:4608\n"
"v_mfma_f32_16x16x32_bf16 v[128:131], a[68:71], a[36:39], v[128:131]\n"
"s_xor_b32 s94, s99, 2752512\n"
"ds_read_b64_tr_b16 a[92:93], v27 offset:0\n"
"v_mfma_f32_16x16x32_bf16 v[128:131], a[72:75], a[52:55], v[128:131]\n"
"s_mul_i32 s8, s16, 1525\n"
"buffer_load_dwordx4 v18, s[48:51], s94 offen lds\n"
"v_mfma_f32_16x16x32_bf16 v[132:135], a[72:75], a[56:59], v[132:135]\n"
"s_lshr_b32 s8, s8, 16\n"
"ds_read_b64_tr_b16 a[94:95], v27 offset:512\n"
"v_mfma_f32_16x16x32_bf16 v[132:135], a[68:71], a[40:43], v[132:135]\n"
"s_waitcnt lgkmcnt(10)\n"
"s_mul_i32 s10, s8, 43\n"
"ds_read_b64_tr_b16 a[96:97], v27 offset:4096\n"
"v_mfma_f32_16x16x32_bf16 v[136:139], a[68:71], a[44:47], v[136:139]\n"
"s_sub_u32 s10, s16, s10\n"
"ds_read_b64_tr_b16 a[98:99], v27 offset:4608\n"
"v_mfma_f32_16x16x32_bf16 v[136:139], a[72:75], a[60:63], v[136:139]\n"
"s_xor_b32 s96, s6, 3072\n"
"v_mfma_f32_16x16x32_bf16 v[140:143], a[72:75], a[64:67], v[140:143]\n"
"s_add_u32 m0, s96, 65536\n"
"v_mfma_f32_16x16x32_bf16 v[140:143], a[68:71], a[48:51], v[140:143]\n"
"s_xor_b32 s94, s99, 2818048\n"
"v_mfma_f32_16x16x32_bf16 v[32:35], a[0:3], a[68:71], v[32:35]\n"
"s_mul_i32 s37, s10, 6\n"
"buffer_load_dwordx4 v19, s[48:51], s94 offen lds\n"
"v_mfma_f32_16x16x32_bf16 v[32:35], a[0:3], a[72:75], v[32:35]\n"
"s_waitcnt lgkmcnt(6)\n"
"s_add_u32 s37, s37, s8\n"
"ds_read_b64_tr_b16 a[68:69], v28 offset:0\n"
"v_mfma_f32_16x16x32_bf16 v[156:159], a[76:79], a[48:51], v[156:159]\n"
"s_lshl_b32 s37, s37, 10\n"
"ds_read_b64_tr_b16 a[70:71], v28 offset:512\n"
"v_mfma_f32_16x16x32_bf16 v[156:159], a[80:83], a[64:67], v[156:159]\n"
"s_add_u32 s37, s37, 1048576\n"
"ds_read_b64_tr_b16 a[72:73], v28 offset:4096\n"
"v_mfma_f32_16x16x32_bf16 v[152:155], a[80:83], a[60:63], v[152:155]\n"
"s_xor_b32 s96, s14, 2048\n"
"ds_read_b64_tr_b16 a[74:75], v28 offset:4608\n"
"v_mfma_f32_16x16x32_bf16 v[152:155], a[76:79], a[44:47], v[152:155]\n"
"s_add_u32 m0, s96, 131072\n"
"v_mfma_f32_16x16x32_bf16 v[148:151], a[76:79], a[40:43], v[148:151]\n"
"s_xor_b32 s94, s98, 17024\n"
"v_mfma_f32_16x16x32_bf16 v[148:151], a[80:83], a[56:59], v[148:151]\n"
"s_lshl_b32 s10, s10, 6\n"
"buffer_load_dwordx4 v16, s[44:47], s94 offen lds\n"
"v_mfma_f32_16x16x32_bf16 v[144:147], a[80:83], a[52:55], v[144:147]\n"
"s_mul_i32 s39, s8, 352256\n"
"v_mfma_f32_16x16x32_bf16 v[144:147], a[76:79], a[36:39], v[144:147]\n"
"s_add_u32 s10, s10, s39\n"
"v_mfma_f32_16x16x32_bf16 v[36:39], a[0:3], a[76:79], v[36:39]\n"
"s_xor_b32 s96, s14, 3072\n"
"v_mfma_f32_16x16x32_bf16 v[36:39], a[0:3], a[80:83], v[36:39]\n"
"s_add_u32 m0, s96, 131072\n"
"ds_read_b64_tr_b16 a[76:77], v29 offset:0\n"
"v_mfma_f32_16x16x32_bf16 v[160:163], a[84:87], a[36:39], v[160:163]\n"
"s_waitcnt lgkmcnt(5)\n"
"s_xor_b32 s94, s98, 25216\n"
"ds_read_b64_tr_b16 a[78:79], v29 offset:512\n"
"v_mfma_f32_16x16x32_bf16 v[160:163], a[88:91], a[52:55], v[160:163]\n"
"s_and_b32 s39, s17, 1\n"
"buffer_load_dwordx4 v17, s[44:47], s94 offen lds\n"
"v_mfma_f32_16x16x32_bf16 v[164:167], a[88:91], a[56:59], v[164:167]\n"
"s_mul_i32 s39, s39, 176128\n"
"ds_read_b64_tr_b16 a[80:81], v29 offset:4096\n"
"v_mfma_f32_16x16x32_bf16 v[164:167], a[84:87], a[40:43], v[164:167]\n"
"s_add_u32 s10, s10, s39\n"
"v_lshrrev_b32 v80, 6, v0\n"
"v_mul_u32_u24 v80, 2752, v80\n"
"v_mfma_f32_16x16x32_bf16 v[168:171], a[84:87], a[44:47], v[168:171]\n"
"s_xor_b32 s96, s14, 4096\n"
"v_and_b32 v86, 0x30, v0\n"
"v_add3_u32 v80, v80, v86, s10\n"
"v_mfma_f32_16x16x32_bf16 v[168:171], a[88:91], a[60:63], v[168:171]\n"
"s_add_u32 m0, s96, 131072\n"
"ds_read_b64_tr_b16 a[82:83], v29 offset:4608\n"
"v_mfma_f32_16x16x32_bf16 v[172:175], a[88:91], a[64:67], v[172:175]\n"
"s_xor_b32 s94, s98, 33408\n"
"v_mfma_f32_16x16x32_bf16 v[172:175], a[84:87], a[48:51], v[172:175]\n"
"s_cmp_lt_u32 s17, 2\n"
"s_cselect_b64 s[20:21], s[76:77], s[80:81]\n"
"buffer_load_dwordx4 v16, s[44:47], s94 offen lds\n"
"v_mfma_f32_16x16x32_bf16 v[188:191], a[92:95], a[48:51], v[188:191]\n"
"s_xor_b32 s96, s14, 5120\n"
"ds_read_b64_tr_b16 a[84:85], v30 offset:0\n"
"v_mfma_f32_16x16x32_bf16 v[188:191], a[96:99], a[64:67], v[188:191]\n"
"s_add_u32 m0, s96, 131072\n"
"ds_read_b64_tr_b16 a[86:87], v30 offset:512\n"
"v_mfma_f32_16x16x32_bf16 v[184:187], a[96:99], a[60:63], v[184:187]\n"
"s_xor_b32 s94, s98, 41600\n"
"ds_read_b64_tr_b16 a[88:89], v30 offset:4096\n"
"v_mfma_f32_16x16x32_bf16 v[184:187], a[92:95], a[44:47], v[184:187]\n"
"s_mov_b64 s[22:23], s[78:79]\n"
"buffer_load_dwordx4 v17, s[44:47], s94 offen lds\n"
"v_lshrrev_b32 v83, 4, v0\n"
"v_mfma_f32_16x16x32_bf16 v[180:183], a[92:95], a[40:43], v[180:183]\n"
"s_xor_b32 s96, s14, 6144\n"
"v_and_b32 v85, 1, v83\n"
"v_and_b32 v83, 0x30, v83\n"
"v_mfma_f32_16x16x32_bf16 v[180:183], a[96:99], a[56:59], v[180:183]\n"
"s_add_u32 m0, s96, 131072\n"
"ds_read_b64_tr_b16 a[90:91], v30 offset:4608\n"
"v_mfma_f32_16x16x32_bf16 v[176:179], a[96:99], a[52:55], v[176:179]\n"
"s_waitcnt lgkmcnt(6)\n"
"s_xor_b32 s94, s98, 49792\n"
"v_mfma_f32_16x16x32_bf16 v[176:179], a[92:95], a[36:39], v[176:179]\n"
"buffer_load_dwordx4 v16, s[44:47], s94 offen lds\n"
"ds_read_b64_tr_b16 a[92:93], v31 offset:0\n"
"v_mfma_f32_16x16x32_bf16 v[192:195], a[68:71], a[36:39], v[192:195]\n"
"s_lshr_b32 s10, s17, 1\n"
"ds_read_b64_tr_b16 a[94:95], v31 offset:512\n"
"v_mfma_f32_16x16x32_bf16 v[192:195], a[72:75], a[52:55], v[192:195]\n"
"s_lshl_b32 s39, s10, 13\n"
"ds_read_b64_tr_b16 a[96:97], v31 offset:4096\n"
"v_mfma_f32_16x16x32_bf16 v[196:199], a[72:75], a[56:59], v[196:199]\n"
"s_xor_b32 s96, s14, 7168\n"
"ds_read_b64_tr_b16 a[98:99], v31 offset:4608\n"
"v_mfma_f32_16x16x32_bf16 v[196:199], a[68:71], a[40:43], v[196:199]\n"
"s_add_u32 m0, s96, 131072\n"
"v_mfma_f32_16x16x32_bf16 v[200:203], a[68:71], a[44:47], v[200:203]\n"
"s_xor_b32 s94, s98, 57984\n"
"v_mfma_f32_16x16x32_bf16 v[200:203], a[72:75], a[60:63], v[200:203]\n"
"s_add_u32 s39, s39, 49152\n"
"buffer_load_dwordx4 v17, s[44:47], s94 offen lds\n"
"v_lshrrev_b32 v86, 1, v86\n"
"v_mfma_f32_16x16x32_bf16 v[204:207], a[72:75], a[64:67], v[204:207]\n"
"v_and_b32 v81, 0xc0, v0\n"
"v_lshl_add_u32 v81, v83, 5, v81\n"
"v_mfma_f32_16x16x32_bf16 v[204:207], a[68:71], a[48:51], v[204:207]\n"
"v_add3_u32 v81, v81, v86, s39\n"
"v_mfma_f32_16x16x32_bf16 v[220:223], a[76:79], a[48:51], v[220:223]\n"
"s_waitcnt lgkmcnt(5)\n"
"s_and_b32 s39, s17, 1\n"
"v_mfma_f32_16x16x32_bf16 v[220:223], a[80:83], a[64:67], v[220:223]\n"
"s_lshl_b32 s39, s39, 5\n"
"v_mfma_f32_16x16x32_bf16 v[216:219], a[80:83], a[60:63], v[216:219]\n"
"v_add_u32 v81, s39, v81\n"
"v_mfma_f32_16x16x32_bf16 v[216:219], a[76:79], a[44:47], v[216:219]\n"
"s_waitcnt vmcnt(12) lgkmcnt(0)\n"
"ds_read_b128 a[4:7], v22 offset:0\n"
"v_mfma_f32_16x16x32_bf16 v[212:215], a[76:79], a[40:43], v[212:215]\n"
".Lpg2:\n"
"s_cbranch_execz .Lpg6\n"
"s_nop 0\n"
"s_barrier\n"
"v_mfma_f32_16x16x32_bf16 v[212:215], a[80:83], a[56:59], v[212:215]\n"
"s_add_u32 m0, s6, 32768\n"
"ds_read_b64_tr_b16 a[68:69], v24 offset:16384\n"
"v_mfma_f32_16x16x32_bf16 v[208:211], a[80:83], a[52:55], v[208:211]\n"
"s_xor_b32 s94, s99, 3145728\n"
"ds_read_b64_tr_b16 a[70:71], v24 offset:16896\n"
"v_mfma_f32_16x16x32_bf16 v[208:211], a[76:79], a[36:39], v[208:211]\n"
"s_lshl_b32 s39, s39, 2\n"
"buffer_load_dwordx4 v18, s[48:51], s94 offen lds\n"
"v_mfma_f32_16x16x32_bf16 v[224:227], a[84:87], a[36:39], v[224:227]\n"
"s_add_u32 m0, s14, 0\n"
"ds_read_b64_tr_b16 a[72:73], v24 offset:20480\n"
"v_mfma_f32_16x16x32_bf16 v[224:227], a[88:91], a[52:55], v[224:227]\n"
"s_xor_b32 s94, s98, 768\n"
"ds_read_b64_tr_b16 a[74:75], v24 offset:20992\n"
"v_mfma_f32_16x16x32_bf16 v[228:231], a[88:91], a[56:59], v[228:231]\n"
"s_lshl_b32 s10, s10, 9\n"
"buffer_load_dwordx4 v16, s[44:47], s94 offen lds\n"
"v_mfma_f32_16x16x32_bf16 v[228:231], a[84:87], a[40:43], v[228:231]\n"
"s_add_u32 s10, s10, s39\n"
"ds_read_b128 a[20:23], v23 offset:0\n"
"v_mfma_f32_16x16x32_bf16 v[232:235], a[84:87], a[44:47], v[232:235]\n"
"s_lshr_b32 s39, s8, 2\n"
"ds_read_b128 a[8:11], v22 offset:2048\n"
"v_mfma_f32_16x16x32_bf16 v[232:235], a[88:91], a[60:63], v[232:235]\n"
"s_xor_b32 s96, s14, 1024\n"
"ds_read_b128 a[24:27], v23 offset:2048\n"
"v_mfma_f32_16x16x32_bf16 v[236:239], a[88:91], a[64:67], v[236:239]\n"
"s_add_u32 m0, s96, 0\n"
"ds_read_b128 a[12:15], v22 offset:4096\n"
"v_mfma_f32_16x16x32_bf16 v[236:239], a[84:87], a[48:51], v[236:239]\n"
"s_xor_b32 s94, s98, 8960\n"
"ds_read_b128 a[28:31], v23 offset:4096\n"
"v_mfma_f32_16x16x32_bf16 v[252:255], a[92:95], a[48:51], v[252:255]\n"
"s_lshl_b32 s39, s39, 30\n"
"buffer_load_dwordx4 v17, s[44:47], s94 offen lds\n"
"v_mfma_f32_16x16x32_bf16 v[252:255], a[96:99], a[64:67], v[252:255]\n"
"s_add_u32 s10, s10, s39\n"
"v_and_b32 v86, 0xc0, v0\n"
"v_lshlrev_b32 v86, 22, v86\n"
"v_mfma_f32_16x16x32_bf16 v[248:251], a[96:99], a[60:63], v[248:251]\n"
"s_xor_b32 s96, s6, 1024\n"
"ds_read_b128 a[16:19], v22 offset:6144\n"
"v_lshl_add_u32 v86, v85, 8, v86\n"
"v_mfma_f32_16x16x32_bf16 v[248:251], a[92:95], a[44:47], v[248:251]\n"
"s_add_u32 m0, s96, 32768\n"
"ds_read_b128 a[32:35], v23 offset:6144\n"
"v_lshl_add_u32 v86, v83, 1, v86\n"
"v_mfma_f32_16x16x32_bf16 v[244:247], a[92:95], a[40:43], v[244:247]\n"
"s_xor_b32 s94, s99, 3211264\n"
"ds_read_b64_tr_b16 a[76:77], v25 offset:16384\n"
"v_add_u32 v93, s10, v86\n"
"v_mfma_f32_16x16x32_bf16 v[244:247], a[96:99], a[56:59], v[244:247]\n"
"buffer_load_dwordx4 v19, s[48:51], s94 offen lds\n"
"ds_read_b64_tr_b16 a[78:79], v25 offset:16896\n"
"v_mfma_f32_16x16x32_bf16 v[240:243], a[96:99], a[52:55], v[240:243]\n"
"s_mov_b32 s24, 4096\n"
"ds_read_b64_tr_b16 a[80:81], v25 offset:20480\n"
"v_bfe_u32 v86, v0, 5, 1\n"
"v_mfma_f32_16x16x32_bf16 v[240:243], a[92:95], a[36:39], v[240:243]\n"
"s_waitcnt lgkmcnt(9)\n"
"buffer_load_dwordx2 v[44:45], v91, s[64:67], s24 offen offset:0 sc1\n"
"ds_read_b64_tr_b16 a[82:83], v25 offset:20992\n"
"v_mfma_f32_16x16x32_bf16 v[128:131], a[68:71], a[4:7], v[128:131]\n"
"s_xor_b32 s96, s6, 2048\n"
"ds_read_b64_tr_b16 a[84:85], v26 offset:16384\n"
"v_lshl_add_u32 v93, v86, 4, v93\n"
"v_mfma_f32_16x16x32_bf16 v[128:131], a[72:75], a[20:23], v[128:131]\n"
"s_waitcnt lgkmcnt(9)\n"
"s_add_u32 m0, s96, 32768\n"
"ds_read_b64_tr_b16 a[86:87], v26 offset:16896\n"
"v_mfma_f32_16x16x32_bf16 v[132:135], a[72:75], a[24:27], v[132:135]\n"
"s_xor_b32 s94, s99, 3276800\n"
"ds_read_b64_tr_b16 a[88:89], v26 offset:20480\n"
"v_mfma_f32_16x16x32_bf16 v[132:135], a[68:71], a[8:11], v[132:135]\n"
"s_waitcnt lgkmcnt(9)\n"
"s_lshl_b32 s8, s8, 8\n"
"buffer_load_dwordx4 v18, s[48:51], s94 offen lds\n"
"v_mfma_f32_16x16x32_bf16 v[136:139], a[68:71], a[12:15], v[136:139]\n"
"s_waitcnt lgkmcnt(7)\n"
"s_add_u32 s8, s8, s39\n"
"ds_read_b64_tr_b16 a[90:91], v26 offset:20992\n"
"v_mfma_f32_16x16x32_bf16 v[136:139], a[72:75], a[28:31], v[136:139]\n"
"s_xor_b32 s96, s6, 3072\n"
"buffer_load_dwordx2 v[46:47], v91, s[64:67], s24 offen offset:128 sc1\n"
"v_add_u32 v83, s8, v83\n"
"v_mfma_f32_16x16x32_bf16 v[140:143], a[72:75], a[32:35], v[140:143]\n"
"s_add_u32 m0, s96, 32768\n"
"ds_read_b64_tr_b16 a[92:93], v27 offset:16384\n"
"v_mfma_f32_16x16x32_bf16 v[140:143], a[68:71], a[16:19], v[140:143]\n"
"s_xor_b32 s94, s99, 3342336\n"
"ds_read_b64_tr_b16 a[94:95], v27 offset:16896\n"
"v_mfma_f32_16x16x32_bf16 v[32:35], a[0:3], a[68:71], v[32:35]\n"
"s_sub_u32 s39, 255, s16\n"
"buffer_load_dwordx4 v19, s[48:51], s94 offen lds\n"
"v_mfma_f32_16x16x32_bf16 v[32:35], a[0:3], a[72:75], v[32:35]\n"
"s_waitcnt lgkmcnt(8)\n"
"s_sub_u32 s8, s39, 84\n"
"ds_read_b64_tr_b16 a[96:97], v27 offset:20480\n"
"v_mfma_f32_16x16x32_bf16 v[156:159], a[76:79], a[16:19], v[156:159]\n"
"s_waitcnt lgkmcnt(7)\n"
"s_xor_b32 s10, s8, s17\n"
"ds_read_b64_tr_b16 a[98:99], v27 offset:20992\n"
"v_mfma_f32_16x16x32_bf16 v[156:159], a[80:83], a[32:35], v[156:159]\n"
"s_xor_b32 s96, s14, 2048\n"
"buffer_load_dwordx2 v[48:49], v91, s[64:67], s24 offen offset:256 sc1\n"
"v_mfma_f32_16x16x32_bf16 v[152:155], a[80:83], a[28:31], v[152:155]\n"
"s_add_u32 m0, s96, 0\n"
"ds_read_b64_tr_b16 a[68:69], v28 offset:16384\n"
"v_mfma_f32_16x16x32_bf16 v[152:155], a[76:79], a[12:15], v[152:155]\n"
"s_xor_b32 s94, s98, 17152\n"
"ds_read_b64_tr_b16 a[70:71], v28 offset:16896\n"
"v_mfma_f32_16x16x32_bf16 v[148:151], a[76:79], a[8:11], v[148:151]\n"
"s_and_b32 s10, s10, 3\n"
"buffer_load_dwordx4 v16, s[44:47], s94 offen lds\n"
"v_mfma_f32_16x16x32_bf16 v[148:151], a[80:83], a[24:27], v[148:151]\n"
"s_lshl_b32 s10, s10, 3\n"
"ds_read_b64_tr_b16 a[72:73], v28 offset:20480\n"
"v_mfma_f32_16x16x32_bf16 v[144:147], a[80:83], a[20:23], v[144:147]\n"
"s_add_u32 s8, s8, s10\n"
"ds_read_b64_tr_b16 a[74:75], v28 offset:20992\n"
"v_mfma_f32_16x16x32_bf16 v[144:147], a[76:79], a[4:7], v[144:147]\n"
"s_lshr_b32 s8, s8, 2\n"
"buffer_load_dwordx2 v[50:51], v91, s[64:67], s24 offen offset:384 sc1\n"
"v_mfma_f32_16x16x32_bf16 v[36:39], a[0:3], a[76:79], v[36:39]\n"
"s_lshl_b32 s24, s17, 12\n"
"v_mfma_f32_16x16x32_bf16 v[36:39], a[0:3], a[80:83], v[36:39]\n"
"s_waitcnt lgkmcnt(6)\n"
"s_add_u32 s24, s24, 32768\n"
"ds_read_b64_tr_b16 a[76:77], v29 offset:16384\n"
"v_mfma_f32_16x16x32_bf16 v[160:163], a[84:87], a[4:7], v[160:163]\n"
"s_xor_b32 s96, s14, 3072\n"
"ds_read_b64_tr_b16 a[78:79], v29 offset:16896\n"
"v_mfma_f32_16x16x32_bf16 v[160:163], a[88:91], a[20:23], v[160:163]\n"
"s_add_u32 m0, s96, 0\n"
"ds_read_b64_tr_b16 a[80:81], v29 offset:20480\n"
"v_mfma_f32_16x16x32_bf16 v[164:167], a[88:91], a[24:27], v[164:167]\n"
"s_xor_b32 s94, s98, 25344\n"
"ds_read_b64_tr_b16 a[82:83], v29 offset:20992\n"
"v_mfma_f32_16x16x32_bf16 v[164:167], a[84:87], a[8:11], v[164:167]\n"
"s_add_u32 s8, s8, 84\n"
"buffer_load_dwordx4 v[60:63], v0, s[64:67], s24 offen offset:0 sc1\n"
"v_mfma_f32_16x16x32_bf16 v[168:171], a[84:87], a[12:15], v[168:171]\n"
"s_cmp_ge_u32 s39, 84\n"
"s_cselect_b32 s39, s8, s39\n"
"buffer_load_dwordx4 v17, s[44:47], s94 offen lds\n"
"v_mfma_f32_16x16x32_bf16 v[168:171], a[88:91], a[28:31], v[168:171]\n"
"s_mul_i32 s8, s39, 1525\n"
"v_mfma_f32_16x16x32_bf16 v[172:175], a[88:91], a[32:35], v[172:175]\n"
"s_xor_b32 s96, s14, 4096\n"
"v_mfma_f32_16x16x32_bf16 v[172:175], a[84:87], a[16:19], v[172:175]\n"
"s_add_u32 m0, s96, 0\n"
"ds_read_b64_tr_b16 a[84:85], v30 offset:16384\n"
"v_mfma_f32_16x16x32_bf16 v[188:191], a[92:95], a[16:19], v[188:191]\n"
"s_waitcnt lgkmcnt(5)\n"
"s_xor_b32 s94, s98, 33536\n"
"ds_read_b64_tr_b16 a[86:87], v30 offset:16896\n"
"v_mfma_f32_16x16x32_bf16 v[188:191], a[96:99], a[32:35], v[188:191]\n"
"s_lshr_b32 s8, s8, 16\n"
"buffer_load_dwordx4 v[64:67], v0, s[64:67], s24 offen offset:1024 sc1\n"
"v_mfma_f32_16x16x32_bf16 v[184:187], a[96:99], a[28:31], v[184:187]\n"
"s_mul_i32 s10, s8, 43\n"
"buffer_load_dwordx4 v16, s[44:47], s94 offen lds\n"
"v_mfma_f32_16x16x32_bf16 v[184:187], a[92:95], a[12:15], v[184:187]\n"
"s_sub_u32 s10, s39, s10\n"
"ds_read_b64_tr_b16 a[88:89], v30 offset:20480\n"
"v_mfma_f32_16x16x32_bf16 v[180:183], a[92:95], a[8:11], v[180:183]\n"
"s_lshl_b32 s8, s8, 2\n"
"ds_read_b64_tr_b16 a[90:91], v30 offset:20992\n"
"v_mfma_f32_16x16x32_bf16 v[180:183], a[96:99], a[24:27], v[180:183]\n"
"s_xor_b32 s96, s14, 5120\n"
"v_mfma_f32_16x16x32_bf16 v[176:179], a[96:99], a[20:23], v[176:179]\n"
"s_add_u32 m0, s96, 0\n"
"v_mfma_f32_16x16x32_bf16 v[176:179], a[92:95], a[4:7], v[176:179]\n"
"s_xor_b32 s94, s98, 41728\n"
"buffer_load_dwordx4 v[68:71], v0, s[64:67], s24 offen offset:2048 sc1\n"
"v_mfma_f32_16x16x32_bf16 v[192:195], a[68:71], a[4:7], v[192:195]\n"
"s_add_u32 s8, s8, s17\n"
"buffer_load_dwordx4 v17, s[44:47], s94 offen lds\n"
"v_mfma_f32_16x16x32_bf16 v[192:195], a[72:75], a[20:23], v[192:195]\n"
"s_lshl_b32 s39, s10, 8\n"
"ds_read_b64_tr_b16 a[92:93], v31 offset:16384\n"
"v_mfma_f32_16x16x32_bf16 v[196:199], a[72:75], a[24:27], v[196:199]\n"
"s_add_u32 s39, s39, s8\n"
"ds_read_b64_tr_b16 a[94:95], v31 offset:16896\n"
"v_mfma_f32_16x16x32_bf16 v[196:199], a[68:71], a[8:11], v[196:199]\n"
"s_xor_b32 s96, s14, 6144\n"
"ds_read_b64_tr_b16 a[96:97], v31 offset:20480\n"
"v_mfma_f32_16x16x32_bf16 v[200:203], a[68:71], a[12:15], v[200:203]\n"
"s_add_u32 m0, s96, 0\n"
"ds_read_b64_tr_b16 a[98:99], v31 offset:20992\n"
"v_mfma_f32_16x16x32_bf16 v[200:203], a[72:75], a[28:31], v[200:203]\n"
"s_xor_b32 s94, s98, 49920\n"
"buffer_load_dwordx4 v[72:75], v0, s[64:67], s24 offen offset:3072 sc1\n"
"v_mfma_f32_16x16x32_bf16 v[204:207], a[72:75], a[32:35], v[204:207]\n"
"s_lshl_b32 s39, s39, 7\n"
"buffer_load_dwordx4 v16, s[44:47], s94 offen lds\n"
"v_mfma_f32_16x16x32_bf16 v[204:207], a[68:71], a[16:19], v[204:207]\n"
"s_waitcnt lgkmcnt(5)\n"
"s_lshr_b32 s27, s8, 3\n"
"v_mfma_f32_16x16x32_bf16 v[220:223], a[76:79], a[16:19], v[220:223]\n"
"s_lshl_b32 s27, s27, 28\n"
"v_mfma_f32_16x16x32_bf16 v[220:223], a[80:83], a[32:35], v[220:223]\n"
"s_add_u32 s39, s39, s27\n"
"v_mfma_f32_16x16x32_bf16 v[216:219], a[80:83], a[28:31], v[216:219]\n"
"s_xor_b32 s96, s14, 7168\n"
"v_add_u32 v94, s39, v13\n"
"v_mfma_f32_16x16x32_bf16 v[216:219], a[76:79], a[12:15], v[216:219]\n"
"s_add_u32 m0, s96, 0\n"
"v_mfma_f32_16x16x32_bf16 v[212:215], a[76:79], a[8:11], v[212:215]\n"
"s_xor_b32 s94, s98, 58112\n"
"v_mfma_f32_16x16x32_bf16 v[212:215], a[80:83], a[24:27], v[212:215]\n"
"s_lshl_b32 s27, s10, 3\n"
"buffer_load_dwordx4 v17, s[44:47], s94 offen lds\n"
"v_mfma_f32_16x16x32_bf16 v[208:211], a[80:83], a[20:23], v[208:211]\n"
"s_add_u32 s27, s27, s8\n"
"v_mfma_f32_16x16x32_bf16 v[208:211], a[76:79], a[4:7], v[208:211]\n"
"s_lshl_b32 s27, s27, 9\n"
"v_mfma_f32_16x16x32_bf16 v[224:227], a[84:87], a[4:7], v[224:227]\n"
"s_waitcnt vmcnt(20) lgkmcnt(0)\n"
"ds_read_b128 a[36:39], v22 offset:49152\n"
"v_mfma_f32_16x16x32_bf16 v[224:227], a[88:91], a[20:23], v[224:227]\n"
"s_barrier\n"
"v_mfma_f32_16x16x32_bf16 v[228:231], a[88:91], a[24:27], v[228:231]\n"
"s_add_u32 m0, s6, 49152\n"
"ds_read_b64_tr_b16 a[68:69], v24 offset:32768\n"
"v_mfma_f32_16x16x32_bf16 v[228:231], a[84:87], a[8:11], v[228:231]\n"
"s_xor_b32 s94, s99, 3670016\n"
"ds_read_b64_tr_b16 a[70:71], v24 offset:33280\n"
"v_mfma_f32_16x16x32_bf16 v[232:235], a[84:87], a[12:15], v[232:235]\n"
"s_add_u32 s27, s27, 1572864\n"
"buffer_load_dwordx4 v18, s[48:51], s94 offen lds\n"
"v_mfma_f32_16x16x32_bf16 v[232:235], a[88:91], a[28:31], v[232:235]\n"
"s_lshl_b32 s12, s8, 6\n"
"ds_read_b64_tr_b16 a[72:73], v24 offset:36864\n"
"v_mfma_f32_16x16x32_bf16 v[236:239], a[88:91], a[32:35], v[236:239]\n"
"s_add_u32 s12, s12, s10\n"
"ds_read_b64_tr_b16 a[74:75], v24 offset:37376\n"
"v_mfma_f32_16x16x32_bf16 v[236:239], a[84:87], a[16:19], v[236:239]\n"
"s_lshl_b32 s12, s12, 7\n"
"ds_read_b128 a[52:55], v23 offset:49152\n"
"v_mfma_f32_16x16x32_bf16 v[252:255], a[92:95], a[16:19], v[252:255]\n"
"s_add_u32 m0, s14, 81920\n"
"ds_read_b128 a[40:43], v22 offset:51200\n"
"v_mfma_f32_16x16x32_bf16 v[252:255], a[96:99], a[32:35], v[252:255]\n"
"s_xor_b32 s94, s98, 896\n"
"ds_read_b128 a[56:59], v23 offset:51200\n"
"v_mfma_f32_16x16x32_bf16 v[248:251], a[96:99], a[28:31], v[248:251]\n"
"s_add_u32 s12, s12, 262144\n"
"buffer_load_dwordx4 v16, s[44:47], s94 offen lds\n"
"v_mfma_f32_16x16x32_bf16 v[248:251], a[92:95], a[12:15], v[248:251]\n"
"s_xor_b32 s96, s14, 1024\n"
"ds_read_b128 a[44:47], v22 offset:53248\n"
"v_mfma_f32_16x16x32_bf16 v[244:247], a[92:95], a[8:11], v[244:247]\n"
"s_add_u32 m0, s96, 81920\n"
"ds_read_b128 a[60:63], v23 offset:53248\n"
"v_mfma_f32_16x16x32_bf16 v[244:247], a[96:99], a[24:27], v[244:247]\n"
"s_xor_b32 s94, s98, 9088\n"
"ds_read_b128 a[48:51], v22 offset:55296\n"
"v_mfma_f32_16x16x32_bf16 v[240:243], a[96:99], a[20:23], v[240:243]\n"
"buffer_load_dwordx4 v17, s[44:47], s94 offen lds\n"
"ds_read_b128 a[64:67], v23 offset:55296\n"
"v_mfma_f32_16x16x32_bf16 v[240:243], a[92:95], a[4:7], v[240:243]\n"
"s_waitcnt lgkmcnt(9)\n"
"s_xor_b32 s96, s6, 1024\n"
"ds_read_b64_tr_b16 a[76:77], v25 offset:32768\n"
"v_mfma_f32_16x16x32_bf16 v[128:131], a[68:71], a[36:39], v[128:131]\n"
"s_waitcnt lgkmcnt(7)\n"
"s_add_u32 m0, s96, 49152\n"
"ds_read_b64_tr_b16 a[78:79], v25 offset:33280\n"
"v_mfma_f32_16x16x32_bf16 v[128:131], a[72:75], a[52:55], v[128:131]\n"
"s_waitcnt lgkmcnt(6)\n"
"s_xor_b32 s94, s99, 3735552\n"
"ds_read_b64_tr_b16 a[80:81], v25 offset:36864\n"
"v_mfma_f32_16x16x32_bf16 v[132:135], a[72:75], a[56:59], v[132:135]\n"
"buffer_load_dwordx4 v19, s[48:51], s94 offen lds\n"
"ds_read_b64_tr_b16 a[82:83], v25 offset:37376\n"
"v_mfma_f32_16x16x32_bf16 v[132:135], a[68:71], a[40:43], v[132:135]\n"
"s_waitcnt lgkmcnt(7)\n"
"s_xor_b32 s96, s6, 2048\n"
"ds_read_b64_tr_b16 a[84:85], v26 offset:32768\n"
"v_mfma_f32_16x16x32_bf16 v[136:139], a[68:71], a[44:47], v[136:139]\n"
"s_waitcnt lgkmcnt(7)\n"
"s_add_u32 m0, s96, 49152\n"
"ds_read_b64_tr_b16 a[86:87], v26 offset:33280\n"
"v_mfma_f32_16x16x32_bf16 v[136:139], a[72:75], a[60:63], v[136:139]\n"
"s_waitcnt lgkmcnt(6)\n"
"s_xor_b32 s94, s99, 3801088\n"
"ds_read_b64_tr_b16 a[88:89], v26 offset:36864\n"
"v_mfma_f32_16x16x32_bf16 v[140:143], a[72:75], a[64:67], v[140:143]\n"
"buffer_load_dwordx4 v18, s[48:51], s94 offen lds\n"
"ds_read_b64_tr_b16 a[90:91], v26 offset:37376\n"
"v_mfma_f32_16x16x32_bf16 v[140:143], a[68:71], a[48:51], v[140:143]\n"
"s_xor_b32 s96, s6, 3072\n"
"ds_read_b64_tr_b16 a[92:93], v27 offset:32768\n"
"v_mfma_f32_16x16x32_bf16 v[32:35], a[0:3], a[68:71], v[32:35]\n"
"s_add_u32 m0, s96, 49152\n"
"ds_read_b64_tr_b16 a[94:95], v27 offset:33280\n"
"v_mfma_f32_16x16x32_bf16 v[32:35], a[0:3], a[72:75], v[32:35]\n"
"s_waitcnt lgkmcnt(8)\n"
"s_xor_b32 s94, s99, 3866624\n"
"ds_read_b64_tr_b16 a[96:97], v27 offset:36864\n"
"v_mfma_f32_16x16x32_bf16 v[156:159], a[76:79], a[48:51], v[156:159]\n"
"s_waitcnt lgkmcnt(7)\n"
"buffer_load_dwordx4 v19, s[48:51], s94 offen lds\n"
"ds_read_b64_tr_b16 a[98:99], v27 offset:37376\n"
"v_mfma_f32_16x16x32_bf16 v[156:159], a[80:83], a[64:67], v[156:159]\n"
"s_xor_b32 s96, s14, 2048\n"
"ds_read_b64_tr_b16 a[68:69], v28 offset:32768\n"
"v_mfma_f32_16x16x32_bf16 v[152:155], a[80:83], a[60:63], v[152:155]\n"
"s_add_u32 m0, s96, 81920\n"
"ds_read_b64_tr_b16 a[70:71], v28 offset:33280\n"
"v_mfma_f32_16x16x32_bf16 v[152:155], a[76:79], a[44:47], v[152:155]\n"
"s_xor_b32 s94, s98, 17280\n"
"ds_read_b64_tr_b16 a[72:73], v28 offset:36864\n"
"v_mfma_f32_16x16x32_bf16 v[148:151], a[76:79], a[40:43], v[148:151]\n"
"buffer_load_dwordx4 v16, s[44:47], s94 offen lds\n"
"ds_read_b64_tr_b16 a[74:75], v28 offset:37376\n"
"v_mfma_f32_16x16x32_bf16 v[148:151], a[80:83], a[56:59], v[148:151]\n"
"s_xor_b32 s96, s14, 3072\n"
"v_mfma_f32_16x16x32_bf16 v[144:147], a[80:83], a[52:55], v[144:147]\n"
"s_add_u32 m0, s96, 81920\n"
"v_mfma_f32_16x16x32_bf16 v[144:147], a[76:79], a[36:39], v[144:147]\n"
"s_xor_b32 s94, s98, 25472\n"
"v_mfma_f32_16x16x32_bf16 v[36:39], a[0:3], a[76:79], v[36:39]\n"
"s_sub_u32 s9, s43, 65536\n"
"buffer_load_dwordx4 v17, s[44:47], s94 offen lds\n"
"v_and_b32 v77, 24, v8\n"
"v_mfma_f32_16x16x32_bf16 v[36:39], a[0:3], a[80:83], v[36:39]\n"
"s_waitcnt lgkmcnt(7)\n"
"ds_read_b64_tr_b16 a[76:77], v29 offset:32768\n"
"v_sub_u32 v76, v8, v77\n"
"v_mfma_f32_16x16x32_bf16 v[160:163], a[84:87], a[36:39], v[160:163]\n"
"ds_read_b64_tr_b16 a[78:79], v29 offset:33280\n"
"v_lshlrev_b32 v77, 2, v77\n"
"v_mfma_f32_16x16x32_bf16 v[160:163], a[88:91], a[52:55], v[160:163]\n"
"s_xor_b32 s96, s14, 4096\n"
"ds_read_b64_tr_b16 a[80:81], v29 offset:36864\n"
"v_xor_b32 v76, v76, v77\n"
"v_mfma_f32_16x16x32_bf16 v[164:167], a[88:91], a[56:59], v[164:167]\n"
"s_add_u32 m0, s96, 81920\n"
"ds_read_b64_tr_b16 a[82:83], v29 offset:37376\n"
"v_add_u32 v76, s9, v76\n"
"v_mfma_f32_16x16x32_bf16 v[164:167], a[84:87], a[40:43], v[164:167]\n"
"s_xor_b32 s94, s98, 33664\n"
"v_mfma_f32_16x16x32_bf16 v[168:171], a[84:87], a[44:47], v[168:171]\n"
"s_bfe_u32 s9, s15, 0x50002\n"
"buffer_load_dwordx4 v16, s[44:47], s94 offen lds\n"
"v_mfma_f32_16x16x32_bf16 v[168:171], a[88:91], a[60:63], v[168:171]\n"
"s_and_b32 s11, s15, 3\n"
"v_mfma_f32_16x16x32_bf16 v[172:175], a[88:91], a[64:67], v[172:175]\n"
"s_lshl_b32 s11, s11, 5\n"
"v_mfma_f32_16x16x32_bf16 v[172:175], a[84:87], a[48:51], v[172:175]\n"
"s_waitcnt vmcnt(17) lgkmcnt(4)\n"
"s_xor_b32 s96, s14, 5120\n"
"ds_read_b64_tr_b16 a[84:85], v30 offset:32768\n"
"v_mfma_f32_16x16x32_bf16 v[188:191], a[92:95], a[48:51], v[188:191]\n"
"s_add_u32 m0, s96, 81920\n"
"v_xor_b32 v52, s92, v44\n"
"v_bitop3_b32 v52, v52, v46, s92 bitop3:0xf6\n"
"v_mfma_f32_16x16x32_bf16 v[188:191], a[96:99], a[64:67], v[188:191]\n"
"s_xor_b32 s94, s98, 41856\n"
"v_bitop3_b32 v52, v52, v48, s92 bitop3:0xf6\n"
"v_bitop3_b32 v52, v52, v50, s92 bitop3:0xf6\n"
"v_mfma_f32_16x16x32_bf16 v[184:187], a[96:99], a[60:63], v[184:187]\n"
"s_add_u32 s9, s9, s11\n"
"buffer_load_dwordx4 v17, s[44:47], s94 offen lds\n"
"v_bitop3_b32 v52, v52, v60, s92 bitop3:0xf6\n"
"v_mfma_f32_16x16x32_bf16 v[184:187], a[92:95], a[44:47], v[184:187]\n"
"s_waitcnt vmcnt(12)\n"
"s_and_b32 s11, s15, 0x380\n"
"v_bitop3_b32 v52, v52, v62, s92 bitop3:0xf6\n"
"v_bitop3_b32 v52, v52, v64, s92 bitop3:0xf6\n"
"v_mfma_f32_16x16x32_bf16 v[180:183], a[92:95], a[40:43], v[180:183]\n"
"s_xor_b32 s96, s14, 6144\n"
"v_bitop3_b32 v52, v52, v66, s92 bitop3:0xf6\n"
"v_bitop3_b32 v52, v52, v68, s92 bitop3:0xf6\n"
"v_mfma_f32_16x16x32_bf16 v[180:183], a[96:99], a[56:59], v[180:183]\n"
"s_add_u32 m0, s96, 81920\n"
"v_bitop3_b32 v52, v52, v70, s92 bitop3:0xf6\n"
"v_bitop3_b32 v52, v52, v72, s92 bitop3:0xf6\n"
"v_mfma_f32_16x16x32_bf16 v[176:179], a[96:99], a[52:55], v[176:179]\n"
"s_xor_b32 s94, s98, 50048\n"
"v_bitop3_b32 v52, v52, v74, s92 bitop3:0xf6\n"
"v_cmp_ne_u32 vcc, 0, v52\n"
"v_mfma_f32_16x16x32_bf16 v[176:179], a[92:95], a[36:39], v[176:179]\n"
"buffer_load_dwordx4 v16, s[44:47], s94 offen lds\n"
"ds_read_b64_tr_b16 a[86:87], v30 offset:33280\n"
"v_mfma_f32_16x16x32_bf16 v[192:195], a[68:71], a[36:39], v[192:195]\n"
"s_cbranch_vccnz .Lrt0\n"
".Lbk0:\n"
"v_mfma_f32_16x16x32_bf16 v[192:195], a[72:75], a[52:55], v[192:195]\n"
"s_add_u32 s9, s9, s11\n"
"ds_read_b64_tr_b16 a[88:89], v30 offset:36864\n"
"v_max_u32 v53, v61, v63\n"
"v_mfma_f32_16x16x32_bf16 v[196:199], a[72:75], a[56:59], v[196:199]\n"
"s_xor_b32 s96, s14, 7168\n"
"ds_read_b64_tr_b16 a[90:91], v30 offset:37376\n"
"v_min_u32 v54, v61, v63\n"
"v_mfma_f32_16x16x32_bf16 v[196:199], a[68:71], a[40:43], v[196:199]\n"
"s_add_u32 m0, s96, 81920\n"
"ds_read_b64_tr_b16 a[92:93], v31 offset:32768\n"
"v_med3_u32 v54, v53, v54, v65\n"
"v_mfma_f32_16x16x32_bf16 v[200:203], a[68:71], a[44:47], v[200:203]\n"
"s_xor_b32 s94, s98, 58240\n"
"ds_read_b64_tr_b16 a[94:95], v31 offset:33280\n"
"v_max_u32 v53, v53, v65\n"
"v_mfma_f32_16x16x32_bf16 v[200:203], a[72:75], a[60:63], v[200:203]\n"
"buffer_load_dwordx4 v17, s[44:47], s94 offen lds\n"
"ds_read_b64_tr_b16 a[96:97], v31 offset:36864\n"
"v_mfma_f32_16x16x32_bf16 v[204:207], a[72:75], a[64:67], v[204:207]\n"
"s_lshl_b32 s9, s9, 12\n"
"ds_read_b64_tr_b16 a[98:99], v31 offset:37376\n"
"v_med3_u32 v54, v53, v54, v67\n"
"v_mfma_f32_16x16x32_bf16 v[204:207], a[68:71], a[48:51], v[204:207]\n"
"s_waitcnt lgkmcnt(7)\n"
"s_add_u32 s9, s9, 4194304\n"
"v_max_u32 v53, v53, v67\n"
"v_med3_u32 v54, v53, v54, v69\n"
"v_mfma_f32_16x16x32_bf16 v[220:223], a[76:79], a[48:51], v[220:223]\n"
"s_add_u32 m0, s43, 0\n"
"v_max_u32 v53, v53, v69\n"
"v_med3_u32 v54, v53, v54, v71\n"
"v_mfma_f32_16x16x32_bf16 v[220:223], a[80:83], a[64:67], v[220:223]\n"
"buffer_load_dwordx4 v92, s[60:63], 0 offen offset:0 lds\n"
"v_max_u32 v53, v53, v71\n"
"v_mfma_f32_16x16x32_bf16 v[216:219], a[80:83], a[60:63], v[216:219]\n"
"v_med3_u32 v54, v53, v54, v73\n"
"v_max_u32 v53, v53, v73\n"
"v_mfma_f32_16x16x32_bf16 v[216:219], a[76:79], a[44:47], v[216:219]\n"
"v_med3_u32 v54, v53, v54, v75\n"
"v_max_u32 v53, v53, v75\n"
"v_mfma_f32_16x16x32_bf16 v[212:215], a[76:79], a[40:43], v[212:215]\n"
"v_mov_b32 v56, v53\n"
"v_mfma_f32_16x16x32_bf16 v[212:215], a[80:83], a[56:59], v[212:215]\n"
"s_mov_b32 s94, 7168\n"
"v_max_u32_dpp v56, v56, v56 quad_perm:[1,0,3,2] row_mask:0xf bank_mask:0xf\n"
"v_mfma_f32_16x16x32_bf16 v[208:211], a[80:83], a[52:55], v[208:211]\n"
"buffer_load_dwordx4 v92, s[60:63], s94 offen offset:1024 lds\n"
"v_max_u32_dpp v56, v56, v56 quad_perm:[2,3,0,1] row_mask:0xf bank_mask:0xf\n"
"v_mfma_f32_16x16x32_bf16 v[208:211], a[76:79], a[36:39], v[208:211]\n"
"s_waitcnt vmcnt(14) lgkmcnt(0)\n"
"ds_read_b128 a[4:7], v20 offset:0\n"
"v_max_u32_dpp v56, v56, v56 row_half_mirror row_mask:0xf bank_mask:0xf\n"
"v_mfma_f32_16x16x32_bf16 v[224:227], a[84:87], a[36:39], v[224:227]\n"
"s_barrier\n"
"v_max_u32_dpp v56, v56, v56 row_mirror row_mask:0xf bank_mask:0xf\n"
"v_mov_b32 v55, v56\n"
"v_mfma_f32_16x16x32_bf16 v[224:227], a[88:91], a[52:55], v[224:227]\n"
"ds_read_b64_tr_b16 a[68:69], v24 offset:0\n"
"v_permlane16_swap_b32 v56, v55\n"
"v_mfma_f32_16x16x32_bf16 v[228:231], a[88:91], a[56:59], v[228:231]\n"
"s_mov_b32 s95, 14336\n"
"ds_read_b64_tr_b16 a[70:71], v24 offset:512\n"
"v_max_u32 v56, v56, v55\n"
"v_mfma_f32_16x16x32_bf16 v[228:231], a[84:87], a[40:43], v[228:231]\n"
"ds_read_b64_tr_b16 a[72:73], v24 offset:4096\n"
"buffer_load_dwordx4 v92, s[60:63], s95 offen offset:2048 lds\n"
"v_mfma_f32_16x16x32_bf16 v[232:235], a[84:87], a[44:47], v[232:235]\n"
"ds_read_b64_tr_b16 a[74:75], v24 offset:4608\n"
"v_mov_b32 v55, v56\n"
"v_mfma_f32_16x16x32_bf16 v[232:235], a[88:91], a[60:63], v[232:235]\n"
"ds_read_b128 a[20:23], v21 offset:0\n"
"v_permlane32_swap_b32 v56, v55\n"
"v_mfma_f32_16x16x32_bf16 v[236:239], a[88:91], a[64:67], v[236:239]\n"
"ds_read_b128 a[8:11], v20 offset:2048\n"
"v_max_u32 v56, v56, v55\n"
"v_mfma_f32_16x16x32_bf16 v[236:239], a[84:87], a[48:51], v[236:239]\n"
"s_mov_b32 s96, 21504\n"
"ds_read_b128 a[24:27], v21 offset:2048\n"
"v_cmp_eq_u32 vcc, v53, v56\n"
"v_mfma_f32_16x16x32_bf16 v[252:255], a[92:95], a[48:51], v[252:255]\n"
"ds_read_b128 a[12:15], v20 offset:4096\n"
"buffer_load_dwordx4 v92, s[60:63], s96 offen offset:3072 lds\n"
"v_mfma_f32_16x16x32_bf16 v[252:255], a[96:99], a[64:67], v[252:255]\n"
"ds_read_b128 a[28:31], v21 offset:4096\n"
"v_cndmask_b32 v57, v53, v54, vcc\n"
"v_mfma_f32_16x16x32_bf16 v[248:251], a[96:99], a[60:63], v[248:251]\n"
"ds_read_b128 a[16:19], v20 offset:6144\n"
"v_max_u32_dpp v57, v57, v57 quad_perm:[1,0,3,2] row_mask:0xf bank_mask:0xf\n"
"v_mfma_f32_16x16x32_bf16 v[248:251], a[92:95], a[44:47], v[248:251]\n"
"ds_read_b128 a[32:35], v21 offset:6144\n"
"v_max_u32_dpp v57, v57, v57 quad_perm:[2,3,0,1] row_mask:0xf bank_mask:0xf\n"
"v_mfma_f32_16x16x32_bf16 v[244:247], a[92:95], a[40:43], v[244:247]\n"
"ds_read_b64_tr_b16 a[76:77], v25 offset:0\n"
"v_max_u32_dpp v57, v57, v57 row_half_mirror row_mask:0xf bank_mask:0xf\n"
"v_mfma_f32_16x16x32_bf16 v[244:247], a[96:99], a[56:59], v[244:247]\n"
"s_waitcnt lgkmcnt(10)\n"
"ds_read_b64_tr_b16 a[78:79], v25 offset:512\n"
"v_max_u32_dpp v57, v57, v57 row_mirror row_mask:0xf bank_mask:0xf\n"
"v_mfma_f32_16x16x32_bf16 v[240:243], a[96:99], a[52:55], v[240:243]\n"
"s_add_u32 m0, s41, 131072\n"
"ds_read_b64_tr_b16 a[80:81], v25 offset:4096\n"
"v_mov_b32 v55, v57\n"
"v_mfma_f32_16x16x32_bf16 v[240:243], a[92:95], a[36:39], v[240:243]\n"
"ds_read_b64_tr_b16 a[82:83], v25 offset:4608\n"
"buffer_load_dwordx4 v1, s[52:55], 0 offen offset:0 lds\n"
"v_mfma_f32_16x16x32_bf16 v[128:131], a[68:71], a[4:7], v[128:131]\n"
"s_waitcnt lgkmcnt(10)\n"
"v_permlane16_swap_b32 v57, v55\n"
"v_max_u32 v57, v57, v55\n"
"v_mfma_f32_16x16x32_bf16 v[128:131], a[72:75], a[20:23], v[128:131]\n"
"s_waitcnt lgkmcnt(8)\n"
"ds_read_b64_tr_b16 a[84:85], v26 offset:0\n"
"v_mov_b32 v55, v57\n"
"v_mfma_f32_16x16x32_bf16 v[132:135], a[72:75], a[24:27], v[132:135]\n"
"s_waitcnt lgkmcnt(7)\n"
"ds_read_b64_tr_b16 a[86:87], v26 offset:512\n"
"v_permlane32_swap_b32 v57, v55\n"
"v_mfma_f32_16x16x32_bf16 v[132:135], a[68:71], a[8:11], v[132:135]\n"
"s_mov_b32 s94, 1024\n"
"ds_read_b64_tr_b16 a[88:89], v26 offset:4096\n"
"v_max_u32 v57, v57, v55\n"
"v_mfma_f32_16x16x32_bf16 v[136:139], a[68:71], a[12:15], v[136:139]\n"
"buffer_load_dwordx4 v2, s[52:55], s94 offen offset:1024 lds\n"
"ds_read_b64_tr_b16 a[90:91], v26 offset:4608\n"
"v_mfma_f32_16x16x32_bf16 v[136:139], a[72:75], a[28:31], v[136:139]\n"
"s_waitcnt lgkmcnt(8)\n"
"s_lshl_b32 s24, s17, 13\n"
"ds_read_b64_tr_b16 a[92:93], v27 offset:0\n"
"v_mfma_f32_16x16x32_bf16 v[140:143], a[72:75], a[32:35], v[140:143]\n"
"s_add_u32 s24, s24, 4096\n"
"ds_read_b64_tr_b16 a[94:95], v27 offset:512\n"
"v_lshrrev_b32 v55, 1, v0\n"
"v_mfma_f32_16x16x32_bf16 v[140:143], a[68:71], a[16:19], v[140:143]\n"
"ds_read_b64_tr_b16 a[96:97], v27 offset:4096\n"
"v_add_u32 v55, s24, v55\n"
"v_mfma_f32_16x16x32_bf16 v[32:35], a[0:3], a[68:71], v[32:35]\n"
"s_waitcnt lgkmcnt(7)\n"
"ds_read_b64_tr_b16 a[98:99], v27 offset:4608\n"
"v_lshlrev_b32 v44, 16, v45\n"
"v_mfma_f32_16x16x32_bf16 v[32:35], a[0:3], a[72:75], v[32:35]\n"
"s_mov_b32 s95, 2048\n"
"ds_read_b64_tr_b16 a[68:69], v28 offset:0\n"
"v_and_b32 v45, 0xffff0000, v45\n"
"v_mfma_f32_16x16x32_bf16 v[156:159], a[76:79], a[16:19], v[156:159]\n"
"buffer_load_dwordx4 v1, s[52:55], s95 offen offset:2048 lds\n"
"ds_read_b64_tr_b16 a[70:71], v28 offset:512\n"
"v_mfma_f32_16x16x32_bf16 v[156:159], a[80:83], a[32:35], v[156:159]\n"
"v_rcp_f32 v45, v45\n"
"v_mfma_f32_16x16x32_bf16 v[152:155], a[80:83], a[28:31], v[152:155]\n"
"v_xor_b32 v44, 0x80000000, v44\n"
"v_mul_f32 v45, 0x3e0293ee, v45\n"
"v_mfma_f32_16x16x32_bf16 v[152:155], a[76:79], a[12:15], v[152:155]\n"
"v_lshlrev_b32 v46, 16, v47\n"
"v_and_b32 v47, 0xffff0000, v47\n"
"v_mfma_f32_16x16x32_bf16 v[148:151], a[76:79], a[8:11], v[148:151]\n"
"v_rcp_f32 v47, v47\n"
"v_mfma_f32_16x16x32_bf16 v[148:151], a[80:83], a[24:27], v[148:151]\n"
"s_mov_b32 s96, 3072\n"
"v_xor_b32 v46, 0x80000000, v46\n"
"v_mul_f32 v47, 0x3e0293ee, v47\n"
"v_mfma_f32_16x16x32_bf16 v[144:147], a[80:83], a[20:23], v[144:147]\n"
"buffer_load_dwordx4 v2, s[52:55], s96 offen offset:3072 lds\n"
"v_lshlrev_b32 v48, 16, v49\n"
"v_mfma_f32_16x16x32_bf16 v[144:147], a[76:79], a[4:7], v[144:147]\n"
"ds_read_b64_tr_b16 a[72:73], v28 offset:4096\n"
"v_and_b32 v49, 0xffff0000, v49\n"
"v_mfma_f32_16x16x32_bf16 v[36:39], a[0:3], a[76:79], v[36:39]\n"
"v_rcp_f32 v49, v49\n"
"v_mfma_f32_16x16x32_bf16 v[36:39], a[0:3], a[80:83], v[36:39]\n"
"s_waitcnt lgkmcnt(4)\n"
"v_xor_b32 v48, 0x80000000, v48\n"
"v_mul_f32 v49, 0x3e0293ee, v49\n"
"v_mfma_f32_16x16x32_bf16 v[160:163], a[84:87], a[4:7], v[160:163]\n"
"v_lshlrev_b32 v50, 16, v51\n"
"v_and_b32 v51, 0xffff0000, v51\n"
"v_mfma_f32_16x16x32_bf16 v[160:163], a[88:91], a[20:23], v[160:163]\n"
"s_add_u32 m0, s42, 65536\n"
"v_rcp_f32 v51, v51\n"
"v_mfma_f32_16x16x32_bf16 v[164:167], a[88:91], a[24:27], v[164:167]\n"
".Lpg3:\n"
"s_cbranch_execz .Lpg7\n"
"v_xor_b32 v50, 0x80000000, v50\n"
"v_mul_f32 v51, 0x3e0293ee, v51\n"
"v_mfma_f32_16x16x32_bf16 v[164:167], a[84:87], a[8:11], v[164:167]\n"
"ds_write_b64 v55, v[56:57]\n"
"buffer_load_dwordx4 v3, s[56:59], 0 offen offset:0 lds\n"
"v_mfma_f32_16x16x32_bf16 v[168:171], a[84:87], a[12:15], v[168:171]\n"
"ds_read_b64_tr_b16 a[74:75], v28 offset:4608\n"
"v_mov_b32 v55, 4096\n"
"v_mfma_f32_16x16x32_bf16 v[168:171], a[88:91], a[28:31], v[168:171]\n"
"ds_read_b64_tr_b16 a[76:77], v29 offset:0\n"
"v_mfma_f32_16x16x32_bf16 v[172:175], a[88:91], a[32:35], v[172:175]\n"
"s_mov_b32 s94, 1024\n"
"ds_read_b64_tr_b16 a[78:79], v29 offset:512\n"
"v_mfma_f32_16x16x32_bf16 v[172:175], a[84:87], a[16:19], v[172:175]\n"
"buffer_load_dwordx4 v3, s[56:59], s94 offen offset:1024 lds\n"
"ds_read_b64_tr_b16 a[80:81], v29 offset:4096\n"
"v_mfma_f32_16x16x32_bf16 v[188:191], a[92:95], a[16:19], v[188:191]\n"
"s_waitcnt lgkmcnt(6)\n"
"ds_read_b64_tr_b16 a[82:83], v29 offset:4608\n"
"v_mfma_f32_16x16x32_bf16 v[188:191], a[96:99], a[32:35], v[188:191]\n"
"ds_read_b64_tr_b16 a[84:85], v30 offset:0\n"
"v_mfma_f32_16x16x32_bf16 v[184:187], a[96:99], a[28:31], v[184:187]\n"
"ds_read_b64_tr_b16 a[86:87], v30 offset:512\n"
"v_mfma_f32_16x16x32_bf16 v[184:187], a[92:95], a[12:15], v[184:187]\n"
"ds_read_b64_tr_b16 a[88:89], v30 offset:4096\n"
"v_mfma_f32_16x16x32_bf16 v[180:183], a[92:95], a[8:11], v[180:183]\n"
"s_add_u32 m0, s41, 147456\n"
"ds_read_b64_tr_b16 a[90:91], v30 offset:4608\n"
"v_mfma_f32_16x16x32_bf16 v[180:183], a[96:99], a[24:27], v[180:183]\n"
"s_mov_b32 s95, 16384\n"
"v_mfma_f32_16x16x32_bf16 v[176:179], a[96:99], a[20:23], v[176:179]\n"
"buffer_load_dwordx4 v1, s[52:55], s95 offen offset:0 lds\n"
"v_mfma_f32_16x16x32_bf16 v[176:179], a[92:95], a[4:7], v[176:179]\n"
"ds_read_b64_tr_b16 a[92:93], v31 offset:0\n"
"v_mfma_f32_16x16x32_bf16 v[192:195], a[68:71], a[4:7], v[192:195]\n"
"s_waitcnt lgkmcnt(9)\n"
"ds_read_b64_tr_b16 a[94:95], v31 offset:512\n"
"v_mfma_f32_16x16x32_bf16 v[192:195], a[72:75], a[20:23], v[192:195]\n"
"ds_read_b64_tr_b16 a[96:97], v31 offset:4096\n"
"v_mfma_f32_16x16x32_bf16 v[196:199], a[72:75], a[24:27], v[196:199]\n"
"s_mov_b32 s96, 17408\n"
"ds_read_b64_tr_b16 a[98:99], v31 offset:4608\n"
"v_mfma_f32_16x16x32_bf16 v[196:199], a[68:71], a[8:11], v[196:199]\n"
"buffer_load_dwordx4 v2, s[52:55], s96 offen offset:1024 lds\n"
"v_mfma_f32_16x16x32_bf16 v[200:203], a[68:71], a[12:15], v[200:203]\n"
"v_mfma_f32_16x16x32_bf16 v[200:203], a[72:75], a[28:31], v[200:203]\n"
"v_mfma_f32_16x16x32_bf16 v[204:207], a[72:75], a[32:35], v[204:207]\n"
"v_mfma_f32_16x16x32_bf16 v[204:207], a[68:71], a[16:19], v[204:207]\n"
"s_waitcnt lgkmcnt(5)\n"
"v_mfma_f32_16x16x32_bf16 v[220:223], a[76:79], a[16:19], v[220:223]\n"
"s_mov_b32 s94, 18432\n"
"v_mfma_f32_16x16x32_bf16 v[220:223], a[80:83], a[32:35], v[220:223]\n"
"buffer_load_dwordx4 v1, s[52:55], s94 offen offset:2048 lds\n"
"v_mfma_f32_16x16x32_bf16 v[216:219], a[80:83], a[28:31], v[216:219]\n"
"v_mfma_f32_16x16x32_bf16 v[216:219], a[76:79], a[12:15], v[216:219]\n"
"s_waitcnt vmcnt(13) lgkmcnt(0)\n"
"ds_read_b128 a[36:39], v22 offset:0\n"
"v_mfma_f32_16x16x32_bf16 v[212:215], a[76:79], a[8:11], v[212:215]\n"
"s_waitcnt vmcnt(9)\n"
"ds_read_b128 a[52:55], v23 offset:0\n"
"v_mfma_f32_16x16x32_bf16 v[212:215], a[80:83], a[24:27], v[212:215]\n"
"s_barrier\n"
"v_mfma_f32_16x16x32_bf16 v[208:211], a[80:83], a[20:23], v[208:211]\n"
"s_mov_b32 s95, 19456\n"
"ds_read_b64_tr_b16 a[68:69], v24 offset:16384\n"
"v_mfma_f32_16x16x32_bf16 v[208:211], a[76:79], a[4:7], v[208:211]\n"
"buffer_load_dwordx4 v2, s[52:55], s95 offen offset:3072 lds\n"
"ds_read_b64_tr_b16 a[116:117], v76 offset:0\n"
"v_mfma_f32_16x16x32_bf16 v[224:227], a[84:87], a[4:7], v[224:227]\n"
"ds_read_b64_tr_b16 a[70:71], v24 offset:16896\n"
"v_mfma_f32_16x16x32_bf16 v[224:227], a[88:91], a[20:23], v[224:227]\n"
"ds_read_b64_tr_b16 a[72:73], v24 offset:20480\n"
"v_mfma_f32_16x16x32_bf16 v[228:231], a[88:91], a[24:27], v[228:231]\n"
"ds_read_b64_tr_b16 a[74:75], v24 offset:20992\n"
"v_mfma_f32_16x16x32_bf16 v[228:231], a[84:87], a[8:11], v[228:231]\n"
"s_add_u32 m0, s42, 73728\n"
"ds_read_b128 a[40:43], v22 offset:2048\n"
"v_mfma_f32_16x16x32_bf16 v[232:235], a[84:87], a[12:15], v[232:235]\n"
"s_mov_b32 s96, 16384\n"
"ds_read_b64_tr_b16 a[118:119], v76 offset:2048\n"
"v_mfma_f32_16x16x32_bf16 v[232:235], a[88:91], a[28:31], v[232:235]\n"
"buffer_load_dwordx4 v3, s[56:59], s96 offen offset:0 lds\n"
"ds_read_b128 a[56:59], v23 offset:2048\n"
"v_mfma_f32_16x16x32_bf16 v[236:239], a[88:91], a[32:35], v[236:239]\n"
"ds_read_b64 v[60:61], v55 offset:0\n"
"v_mfma_f32_16x16x32_bf16 v[236:239], a[84:87], a[16:19], v[236:239]\n"
"ds_read_b64 v[62:63], v55 offset:8192\n"
"v_mfma_f32_16x16x32_bf16 v[252:255], a[92:95], a[16:19], v[252:255]\n"
"ds_read_b64 v[64:65], v55 offset:16384\n"
"v_mfma_f32_16x16x32_bf16 v[252:255], a[96:99], a[32:35], v[252:255]\n"
"s_mov_b32 s94, 17408\n"
"ds_read_b64 v[66:67], v55 offset:24576\n"
"v_mfma_f32_16x16x32_bf16 v[248:251], a[96:99], a[28:31], v[248:251]\n"
"ds_read_b64_tr_b16 a[120:121], v76 offset:8\n"
"buffer_load_dwordx4 v3, s[56:59], s94 offen offset:1024 lds\n"
"v_mfma_f32_16x16x32_bf16 v[248:251], a[92:95], a[12:15], v[248:251]\n"
"ds_read_b128 a[44:47], v22 offset:4096\n"
"v_mfma_f32_16x16x32_bf16 v[244:247], a[92:95], a[8:11], v[244:247]\n"
"ds_read_b128 a[60:63], v23 offset:4096\n"
"v_mfma_f32_16x16x32_bf16 v[244:247], a[96:99], a[24:27], v[244:247]\n"
"ds_read_b64_tr_b16 a[122:123], v76 offset:2056\n"
"v_mfma_f32_16x16x32_bf16 v[240:243], a[96:99], a[20:23], v[240:243]\n"
"ds_read_b128 a[48:51], v22 offset:6144\n"
"v_mfma_f32_16x16x32_bf16 v[240:243], a[92:95], a[4:7], v[240:243]\n"
"s_waitcnt lgkmcnt(11)\n"
"ds_read_b64_tr_b16 a[124:125], v76 offset:16\n"
"v_mfma_f32_16x16x32_bf16 v[128:131], a[68:71], a[36:39], v[128:131]\n"
"ds_read_b128 a[64:67], v23 offset:6144\n"
"v_mfma_f32_16x16x32_bf16 v[128:131], a[72:75], a[52:55], v[128:131]\n"
"s_waitcnt lgkmcnt(10)\n"
"ds_read_b64_tr_b16 a[76:77], v25 offset:16384\n"
"v_max_u32 v53, v60, v61\n"
"v_mfma_f32_16x16x32_bf16 v[132:135], a[72:75], a[56:59], v[132:135]\n"
"ds_read_b64_tr_b16 a[78:79], v25 offset:16896\n"
"v_min_u32 v54, v60, v61\n"
"v_mfma_f32_16x16x32_bf16 v[132:135], a[68:71], a[40:43], v[132:135]\n"
"s_waitcnt lgkmcnt(7)\n"
"ds_read_b64_tr_b16 a[80:81], v25 offset:20480\n"
"v_med3_u32 v54, v53, v54, v62\n"
"v_mfma_f32_16x16x32_bf16 v[136:139], a[68:71], a[44:47], v[136:139]\n"
"s_waitcnt lgkmcnt(7)\n"
"ds_read_b64_tr_b16 a[82:83], v25 offset:20992\n"
"v_max_u32 v53, v53, v62\n"
"v_mfma_f32_16x16x32_bf16 v[136:139], a[72:75], a[60:63], v[136:139]\n"
"s_waitcnt lgkmcnt(4)\n"
"ds_read_b64_tr_b16 a[126:127], v76 offset:2064\n"
"v_med3_u32 v54, v53, v54, v63\n"
"v_mfma_f32_16x16x32_bf16 v[140:143], a[72:75], a[64:67], v[140:143]\n"
"ds_read_b64_tr_b16 a[128:129], v76 offset:24\n"
"v_max_u32 v53, v53, v63\n"
"v_mfma_f32_16x16x32_bf16 v[140:143], a[68:71], a[48:51], v[140:143]\n"
"ds_read_b64_tr_b16 a[130:131], v76 offset:2072\n"
"v_med3_u32 v54, v53, v54, v64\n"
"v_mfma_f32_16x16x32_bf16 v[32:35], a[0:3], a[68:71], v[32:35]\n"
"ds_read_b64_tr_b16 a[84:85], v26 offset:16384\n"
"v_max_u32 v53, v53, v64\n"
"v_mfma_f32_16x16x32_bf16 v[32:35], a[0:3], a[72:75], v[32:35]\n"
"s_waitcnt lgkmcnt(6)\n"
"ds_read_b64_tr_b16 a[86:87], v26 offset:16896\n"
"v_med3_u32 v54, v53, v54, v65\n"
"v_mfma_f32_16x16x32_bf16 v[156:159], a[76:79], a[48:51], v[156:159]\n"
"s_waitcnt lgkmcnt(5)\n"
"ds_read_b64_tr_b16 a[88:89], v26 offset:20480\n"
"v_max_u32 v53, v53, v65\n"
"v_mfma_f32_16x16x32_bf16 v[156:159], a[80:83], a[64:67], v[156:159]\n"
"ds_read_b64_tr_b16 a[90:91], v26 offset:20992\n"
"v_med3_u32 v54, v53, v54, v66\n"
"v_mfma_f32_16x16x32_bf16 v[152:155], a[80:83], a[60:63], v[152:155]\n"
"v_max_u32 v53, v53, v66\n"
"v_med3_u32 v54, v53, v54, v67\n"
"v_mfma_f32_16x16x32_bf16 v[152:155], a[76:79], a[44:47], v[152:155]\n"
"buffer_store_dwordx4 a[116:119], v0, s[64:67], s9 offen offset:0 sc1\n"
"ds_read_b64_tr_b16 a[92:93], v27 offset:16384\n"
"v_mfma_f32_16x16x32_bf16 v[148:151], a[76:79], a[40:43], v[148:151]\n"
"ds_read_b64_tr_b16 a[94:95], v27 offset:16896\n"
"v_max_u32 v53, v53, v67\n"
"v_mfma_f32_16x16x32_bf16 v[148:151], a[80:83], a[56:59], v[148:151]\n"
"ds_read_b64_tr_b16 a[96:97], v27 offset:20480\n"
"v_readfirstlane_b32 s13, v53\n"
"v_mfma_f32_16x16x32_bf16 v[144:147], a[80:83], a[52:55], v[144:147]\n"
"ds_read_b64_tr_b16 a[98:99], v27 offset:20992\n"
"v_readfirstlane_b32 s14, v54\n"
"v_mfma_f32_16x16x32_bf16 v[144:147], a[76:79], a[36:39], v[144:147]\n"
"s_andn2_b32 s10, 0x7ff, s13\n"
"ds_read_b64_tr_b16 a[68:69], v28 offset:16384\n"
"v_mfma_f32_16x16x32_bf16 v[36:39], a[0:3], a[76:79], v[36:39]\n"
"s_andn2_b32 s39, 0x7ff, s14\n"
"ds_read_b64_tr_b16 a[70:71], v28 offset:16896\n"
"v_mfma_f32_16x16x32_bf16 v[36:39], a[0:3], a[80:83], v[36:39]\n"
"s_waitcnt lgkmcnt(8)\n"
"s_sub_u32 s39, s39, s10\n"
"ds_read_b64_tr_b16 a[72:73], v28 offset:20480\n"
"v_mfma_f32_16x16x32_bf16 v[160:163], a[84:87], a[36:39], v[160:163]\n"
"s_waitcnt lgkmcnt(7)\n"
"ds_read_b64_tr_b16 a[74:75], v28 offset:20992\n"
"v_mul_i32_i24 v84, s39, v85\n"
"v_mfma_f32_16x16x32_bf16 v[160:163], a[88:91], a[52:55], v[160:163]\n"
"ds_read_b64_tr_b16 a[76:77], v29 offset:16384\n"
"v_add_u32 v84, s10, v84\n"
"v_mfma_f32_16x16x32_bf16 v[164:167], a[88:91], a[56:59], v[164:167]\n"
"ds_read_b64_tr_b16 a[78:79], v29 offset:16896\n"
"v_lshl_add_u32 v83, v84, 10, v83\n"
"v_mfma_f32_16x16x32_bf16 v[164:167], a[84:87], a[40:43], v[164:167]\n"
"buffer_store_dwordx4 a[120:123], v0, s[64:67], s9 offen offset:1024 sc1\n"
"ds_read_b64_tr_b16 a[80:81], v29 offset:20480\n"
"v_mfma_f32_16x16x32_bf16 v[168:171], a[84:87], a[44:47], v[168:171]\n"
"ds_read_b64_tr_b16 a[82:83], v29 offset:20992\n"
"v_lshlrev_b32 v84, 3, v84\n"
"v_mfma_f32_16x16x32_bf16 v[168:171], a[88:91], a[60:63], v[168:171]\n"
"s_mov_b32 s8, 4096\n"
"ds_write_b32 v41, v32 offset:0\n"
"v_mfma_f32_16x16x32_bf16 v[172:175], a[88:91], a[64:67], v[172:175]\n"
"ds_write_b32 v41, v36 offset:256\n"
"v_xor_b32 v40, 512, v43\n"
"v_mfma_f32_16x16x32_bf16 v[172:175], a[84:87], a[48:51], v[172:175]\n"
"s_waitcnt lgkmcnt(10)\n"
"v_xor_b32 v41, 1024, v43\n"
"v_xor_b32 v42, 1536, v43\n"
"v_mfma_f32_16x16x32_bf16 v[188:191], a[92:95], a[48:51], v[188:191]\n"
"buffer_load_dwordx2 v[88:89], v84, s[64:67], s8 offen sc1\n"
"ds_read_b64_tr_b16 a[84:85], v30 offset:16384\n"
"v_mfma_f32_16x16x32_bf16 v[188:191], a[96:99], a[64:67], v[188:191]\n"
"ds_read_b64_tr_b16 a[86:87], v30 offset:16896\n"
"v_mfma_f32_16x16x32_bf16 v[184:187], a[96:99], a[60:63], v[184:187]\n"
"ds_read_b64_tr_b16 a[88:89], v30 offset:20480\n"
"v_mfma_f32_16x16x32_bf16 v[184:187], a[92:95], a[44:47], v[184:187]\n"
"ds_read_b64_tr_b16 a[90:91], v30 offset:20992\n"
"buffer_load_dwordx4 a[228:231], v83, s[44:47], 0 offen offset:0\n"
"v_mfma_f32_16x16x32_bf16 v[180:183], a[92:95], a[40:43], v[180:183]\n"
"v_mfma_f32_16x16x32_bf16 v[180:183], a[96:99], a[56:59], v[180:183]\n"
"buffer_store_dwordx4 a[124:127], v0, s[64:67], s9 offen offset:2048 sc1\n"
"v_mfma_f32_16x16x32_bf16 v[176:179], a[96:99], a[52:55], v[176:179]\n"
"buffer_load_dwordx4 a[232:235], v83, s[44:47], 0 offen offset:64\n"
"v_mfma_f32_16x16x32_bf16 v[176:179], a[92:95], a[36:39], v[176:179]\n"
"s_waitcnt lgkmcnt(8)\n"
"ds_read_b64_tr_b16 a[92:93], v31 offset:16384\n"
"v_mfma_f32_16x16x32_bf16 v[192:195], a[68:71], a[36:39], v[192:195]\n"
"ds_read_b64_tr_b16 a[94:95], v31 offset:16896\n"
"v_mfma_f32_16x16x32_bf16 v[192:195], a[72:75], a[52:55], v[192:195]\n"
"ds_read_b64_tr_b16 a[96:97], v31 offset:20480\n"
"buffer_load_dwordx4 a[236:239], v83, s[44:47], 0 offen offset:128\n"
"v_mfma_f32_16x16x32_bf16 v[196:199], a[72:75], a[56:59], v[196:199]\n"
"ds_read_b64_tr_b16 a[98:99], v31 offset:20992\n"
"v_mfma_f32_16x16x32_bf16 v[196:199], a[68:71], a[40:43], v[196:199]\n"
"v_mfma_f32_16x16x32_bf16 v[200:203], a[68:71], a[44:47], v[200:203]\n"
"buffer_load_dwordx4 a[240:243], v83, s[44:47], 0 offen offset:192\n"
"v_mfma_f32_16x16x32_bf16 v[200:203], a[72:75], a[60:63], v[200:203]\n"
"v_mfma_f32_16x16x32_bf16 v[204:207], a[72:75], a[64:67], v[204:207]\n"
"v_mfma_f32_16x16x32_bf16 v[204:207], a[68:71], a[48:51], v[204:207]\n"
"s_waitcnt vmcnt(14) lgkmcnt(0)\n"
"buffer_store_dwordx4 a[128:131], v0, s[64:67], s9 offen offset:3072 sc1\n"
"v_mfma_f32_16x16x32_bf16 v[220:223], a[76:79], a[48:51], v[220:223]\n"
"s_barrier\n"
"v_mfma_f32_16x16x32_bf16 v[220:223], a[80:83], a[64:67], v[220:223]\n"
"ds_read_b128 v[96:99], v43 offset:0\n"
"v_mfma_f32_16x16x32_bf16 v[216:219], a[80:83], a[60:63], v[216:219]\n"
"ds_read_b128 v[100:103], v43 offset:256\n"
"v_mfma_f32_16x16x32_bf16 v[216:219], a[76:79], a[44:47], v[216:219]\n"
"s_add_u32 m0, s41, 98304\n"
"ds_read_b128 v[104:107], v40 offset:0\n"
"v_mfma_f32_16x16x32_bf16 v[212:215], a[76:79], a[40:43], v[212:215]\n"
"s_mov_b32 s95, 32768\n"
"ds_read_b128 v[108:111], v40 offset:256\n"
"v_mfma_f32_16x16x32_bf16 v[212:215], a[80:83], a[56:59], v[212:215]\n"
"buffer_load_dwordx4 v1, s[52:55], s95 offen offset:0 lds\n"
"ds_read_b128 v[112:115], v41 offset:0\n"
"v_mfma_f32_16x16x32_bf16 v[208:211], a[80:83], a[52:55], v[208:211]\n"
"s_mov_b32 s96, 33792\n"
"ds_read_b128 v[116:119], v41 offset:256\n"
"v_mfma_f32_16x16x32_bf16 v[208:211], a[76:79], a[36:39], v[208:211]\n"
"buffer_load_dwordx4 v2, s[52:55], s96 offen offset:1024 lds\n"
"ds_read_b128 v[120:123], v42 offset:0\n"
"v_mfma_f32_16x16x32_bf16 v[224:227], a[84:87], a[36:39], v[224:227]\n"
"ds_read_b128 v[124:127], v42 offset:256\n"
"v_mfma_f32_16x16x32_bf16 v[224:227], a[88:91], a[52:55], v[224:227]\n"
"s_mov_b32 s94, 34816\n"
"ds_read_b128 a[148:151], v4 offset:32768\n"
"v_mfma_f32_16x16x32_bf16 v[228:231], a[88:91], a[56:59], v[228:231]\n"
"buffer_load_dwordx4 v1, s[52:55], s94 offen offset:2048 lds\n"
"ds_read_b128 a[152:155], v5 offset:32768\n"
"v_mfma_f32_16x16x32_bf16 v[228:231], a[84:87], a[40:43], v[228:231]\n"
"ds_read_b128 a[156:159], v6 offset:32768\n"
"v_mfma_f32_16x16x32_bf16 v[232:235], a[84:87], a[44:47], v[232:235]\n"
"s_mov_b32 s95, 35840\n"
"ds_read_b128 a[160:163], v7 offset:32768\n"
"v_mfma_f32_16x16x32_bf16 v[232:235], a[88:91], a[60:63], v[232:235]\n"
"buffer_load_dwordx4 v2, s[52:55], s95 offen offset:3072 lds\n"
"ds_read_b128 a[164:167], v4 offset:34816\n"
"v_mfma_f32_16x16x32_bf16 v[236:239], a[88:91], a[64:67], v[236:239]\n"
"ds_read_b128 a[168:171], v5 offset:34816\n"
"v_mfma_f32_16x16x32_bf16 v[236:239], a[84:87], a[48:51], v[236:239]\n"
"s_add_u32 m0, s42, 81920\n"
"ds_read_b128 a[172:175], v6 offset:34816\n"
"v_mfma_f32_16x16x32_bf16 v[252:255], a[92:95], a[48:51], v[252:255]\n"
"s_mov_b32 s96, 32768\n"
"ds_read_b128 a[176:179], v7 offset:34816\n"
"v_mfma_f32_16x16x32_bf16 v[252:255], a[96:99], a[64:67], v[252:255]\n"
"buffer_load_dwordx4 v3, s[56:59], s96 offen offset:0 lds\n"
"v_mfma_f32_16x16x32_bf16 v[248:251], a[96:99], a[60:63], v[248:251]\n"
"s_mov_b32 s94, 33792\n"
"v_mfma_f32_16x16x32_bf16 v[248:251], a[92:95], a[44:47], v[248:251]\n"
"buffer_load_dwordx4 v3, s[56:59], s94 offen offset:1024 lds\n"
"v_mfma_f32_16x16x32_bf16 v[244:247], a[92:95], a[40:43], v[244:247]\n"
"v_mfma_f32_16x16x32_bf16 v[244:247], a[96:99], a[56:59], v[244:247]\n"
"s_add_u32 s97, s7, 49152\n"
"v_mfma_f32_16x16x32_bf16 v[240:243], a[96:99], a[52:55], v[240:243]\n"
"buffer_load_dwordx4 a[144:147], v0, s[72:75], s97 offen\n"
"v_mfma_f32_16x16x32_bf16 v[240:243], a[92:95], a[36:39], v[240:243]\n"
"s_waitcnt lgkmcnt(6)\n"
"v_pk_fma_f32 v[128:129], v[96:97], v[44:45], v[128:129] op_sel_hi:[1,0,1]\n"
"v_pk_fma_f32 v[130:131], v[98:99], v[44:45], v[130:131] op_sel_hi:[1,0,1]\n"
"v_pk_fma_f32 v[144:145], v[100:101], v[44:45], v[144:145] op_sel_hi:[1,0,1]\n"
"v_pk_fma_f32 v[146:147], v[102:103], v[44:45], v[146:147] op_sel_hi:[1,0,1]\n"
"v_pk_fma_f32 v[160:161], v[104:105], v[44:45], v[160:161] op_sel_hi:[1,0,1]\n"
"v_pk_fma_f32 v[162:163], v[106:107], v[44:45], v[162:163] op_sel_hi:[1,0,1]\n"
"v_pk_fma_f32 v[176:177], v[108:109], v[44:45], v[176:177] op_sel_hi:[1,0,1]\n"
"v_pk_fma_f32 v[178:179], v[110:111], v[44:45], v[178:179] op_sel_hi:[1,0,1]\n"
"v_pk_fma_f32 v[192:193], v[112:113], v[44:45], v[192:193] op_sel_hi:[1,0,1]\n"
"v_pk_fma_f32 v[194:195], v[114:115], v[44:45], v[194:195] op_sel_hi:[1,0,1]\n"
"v_pk_fma_f32 v[208:209], v[116:117], v[44:45], v[208:209] op_sel_hi:[1,0,1]\n"
"v_pk_fma_f32 v[210:211], v[118:119], v[44:45], v[210:211] op_sel_hi:[1,0,1]\n"
"v_pk_fma_f32 v[224:225], v[120:121], v[44:45], v[224:225] op_sel_hi:[1,0,1]\n"
"v_pk_fma_f32 v[226:227], v[122:123], v[44:45], v[226:227] op_sel_hi:[1,0,1]\n"
"v_pk_fma_f32 v[240:241], v[124:125], v[44:45], v[240:241] op_sel_hi:[1,0,1]\n"
"v_pk_fma_f32 v[242:243], v[126:127], v[44:45], v[242:243] op_sel_hi:[1,0,1]\n"
"v_pk_mul_f32 v[128:129], v[128:129], v[44:45] op_sel:[0,1] op_sel_hi:[1,1]\n"
"v_pk_mul_f32 v[130:131], v[130:131], v[44:45] op_sel:[0,1] op_sel_hi:[1,1]\n"
"v_pk_mul_f32 v[144:145], v[144:145], v[44:45] op_sel:[0,1] op_sel_hi:[1,1]\n"
"v_pk_mul_f32 v[146:147], v[146:147], v[44:45] op_sel:[0,1] op_sel_hi:[1,1]\n"
"v_pk_mul_f32 v[160:161], v[160:161], v[44:45] op_sel:[0,1] op_sel_hi:[1,1]\n"
"v_pk_mul_f32 v[162:163], v[162:163], v[44:45] op_sel:[0,1] op_sel_hi:[1,1]\n"
"v_pk_mul_f32 v[176:177], v[176:177], v[44:45] op_sel:[0,1] op_sel_hi:[1,1]\n"
"v_pk_mul_f32 v[178:179], v[178:179], v[44:45] op_sel:[0,1] op_sel_hi:[1,1]\n"
"v_pk_mul_f32 v[192:193], v[192:193], v[44:45] op_sel:[0,1] op_sel_hi:[1,1]\n"
"v_pk_mul_f32 v[194:195], v[194:195], v[44:45] op_sel:[0,1] op_sel_hi:[1,1]\n"
"v_pk_mul_f32 v[208:209], v[208:209], v[44:45] op_sel:[0,1] op_sel_hi:[1,1]\n"
"v_pk_mul_f32 v[210:211], v[210:211], v[44:45] op_sel:[0,1] op_sel_hi:[1,1]\n"
"v_pk_mul_f32 v[224:225], v[224:225], v[44:45] op_sel:[0,1] op_sel_hi:[1,1]\n"
"v_pk_mul_f32 v[226:227], v[226:227], v[44:45] op_sel:[0,1] op_sel_hi:[1,1]\n"
"v_pk_mul_f32 v[240:241], v[240:241], v[44:45] op_sel:[0,1] op_sel_hi:[1,1]\n"
"v_pk_mul_f32 v[242:243], v[242:243], v[44:45] op_sel:[0,1] op_sel_hi:[1,1]\n"
"v_cvt_pk_bf16_f32 v60, v128, v129\n"
"v_cvt_pk_bf16_f32 v61, v130, v131\n"
"v_cvt_pk_bf16_f32 v62, v144, v145\n"
"v_cvt_pk_bf16_f32 v63, v146, v147\n"
"v_cvt_pk_bf16_f32 v64, v160, v161\n"
"v_cvt_pk_bf16_f32 v65, v162, v163\n"
"v_cvt_pk_bf16_f32 v66, v176, v177\n"
"v_cvt_pk_bf16_f32 v67, v178, v179\n"
"v_cvt_pk_bf16_f32 v68, v192, v193\n"
"v_cvt_pk_bf16_f32 v69, v194, v195\n"
"v_cvt_pk_bf16_f32 v70, v208, v209\n"
"v_cvt_pk_bf16_f32 v71, v210, v211\n"
"v_cvt_pk_bf16_f32 v72, v224, v225\n"
"v_cvt_pk_bf16_f32 v73, v226, v227\n"
"v_cvt_pk_bf16_f32 v74, v240, v241\n"
"v_cvt_pk_bf16_f32 v75, v242, v243\n"
"v_permlane32_swap_b32 v60, v62\n"
"v_permlane32_swap_b32 v61, v63\n"
"v_permlane32_swap_b32 v64, v66\n"
"v_permlane32_swap_b32 v65, v67\n"
"v_permlane32_swap_b32 v68, v70\n"
"v_permlane32_swap_b32 v69, v71\n"
"v_permlane32_swap_b32 v72, v74\n"
"v_permlane32_swap_b32 v73, v75\n"
"v_accvgpr_write_b32 a52, v60\n"
"v_accvgpr_write_b32 a53, v61\n"
"v_accvgpr_write_b32 a54, v62\n"
"v_accvgpr_write_b32 a55, v63\n"
"v_accvgpr_write_b32 a56, v64\n"
"v_accvgpr_write_b32 a57, v65\n"
"v_accvgpr_write_b32 a58, v66\n"
"v_accvgpr_write_b32 a59, v67\n"
"v_accvgpr_write_b32 a60, v68\n"
"v_accvgpr_write_b32 a61, v69\n"
"v_accvgpr_write_b32 a62, v70\n"
"v_accvgpr_write_b32 a63, v71\n"
"v_accvgpr_write_b32 a64, v72\n"
"v_accvgpr_write_b32 a65, v73\n"
"v_accvgpr_write_b32 a66, v74\n"
"v_accvgpr_write_b32 a67, v75\n"
"s_add_u32 m0, s43, -65536\n"
"s_nop 0\n"
"buffer_load_dwordx4 v80, s[20:23], 0 offen offset:0 lds\n"
"s_nop 0\n"
"v_pk_fma_f32 v[132:133], v[96:97], v[46:47], v[132:133] op_sel_hi:[1,0,1]\n"
"v_pk_fma_f32 v[134:135], v[98:99], v[46:47], v[134:135] op_sel_hi:[1,0,1]\n"
"v_pk_fma_f32 v[148:149], v[100:101], v[46:47], v[148:149] op_sel_hi:[1,0,1]\n"
"v_pk_fma_f32 v[150:151], v[102:103], v[46:47], v[150:151] op_sel_hi:[1,0,1]\n"
"v_pk_fma_f32 v[164:165], v[104:105], v[46:47], v[164:165] op_sel_hi:[1,0,1]\n"
"v_pk_fma_f32 v[166:167], v[106:107], v[46:47], v[166:167] op_sel_hi:[1,0,1]\n"
"v_pk_fma_f32 v[180:181], v[108:109], v[46:47], v[180:181] op_sel_hi:[1,0,1]\n"
"v_pk_fma_f32 v[182:183], v[110:111], v[46:47], v[182:183] op_sel_hi:[1,0,1]\n"
"v_pk_fma_f32 v[196:197], v[112:113], v[46:47], v[196:197] op_sel_hi:[1,0,1]\n"
"v_pk_fma_f32 v[198:199], v[114:115], v[46:47], v[198:199] op_sel_hi:[1,0,1]\n"
"v_pk_fma_f32 v[212:213], v[116:117], v[46:47], v[212:213] op_sel_hi:[1,0,1]\n"
"v_pk_fma_f32 v[214:215], v[118:119], v[46:47], v[214:215] op_sel_hi:[1,0,1]\n"
"v_pk_fma_f32 v[228:229], v[120:121], v[46:47], v[228:229] op_sel_hi:[1,0,1]\n"
"v_pk_fma_f32 v[230:231], v[122:123], v[46:47], v[230:231] op_sel_hi:[1,0,1]\n"
"v_pk_fma_f32 v[244:245], v[124:125], v[46:47], v[244:245] op_sel_hi:[1,0,1]\n"
"v_pk_fma_f32 v[246:247], v[126:127], v[46:47], v[246:247] op_sel_hi:[1,0,1]\n"
"v_pk_mul_f32 v[132:133], v[132:133], v[46:47] op_sel:[0,1] op_sel_hi:[1,1]\n"
"v_pk_mul_f32 v[134:135], v[134:135], v[46:47] op_sel:[0,1] op_sel_hi:[1,1]\n"
"v_pk_mul_f32 v[148:149], v[148:149], v[46:47] op_sel:[0,1] op_sel_hi:[1,1]\n"
"v_pk_mul_f32 v[150:151], v[150:151], v[46:47] op_sel:[0,1] op_sel_hi:[1,1]\n"
"v_pk_mul_f32 v[164:165], v[164:165], v[46:47] op_sel:[0,1] op_sel_hi:[1,1]\n"
"v_pk_mul_f32 v[166:167], v[166:167], v[46:47] op_sel:[0,1] op_sel_hi:[1,1]\n"
"v_pk_mul_f32 v[180:181], v[180:181], v[46:47] op_sel:[0,1] op_sel_hi:[1,1]\n"
"v_pk_mul_f32 v[182:183], v[182:183], v[46:47] op_sel:[0,1] op_sel_hi:[1,1]\n"
"v_pk_mul_f32 v[196:197], v[196:197], v[46:47] op_sel:[0,1] op_sel_hi:[1,1]\n"
"v_pk_mul_f32 v[198:199], v[198:199], v[46:47] op_sel:[0,1] op_sel_hi:[1,1]\n"
"v_pk_mul_f32 v[212:213], v[212:213], v[46:47] op_sel:[0,1] op_sel_hi:[1,1]\n"
"v_pk_mul_f32 v[214:215], v[214:215], v[46:47] op_sel:[0,1] op_sel_hi:[1,1]\n"
"v_pk_mul_f32 v[228:229], v[228:229], v[46:47] op_sel:[0,1] op_sel_hi:[1,1]\n"
"v_pk_mul_f32 v[230:231], v[230:231], v[46:47] op_sel:[0,1] op_sel_hi:[1,1]\n"
"v_pk_mul_f32 v[244:245], v[244:245], v[46:47] op_sel:[0,1] op_sel_hi:[1,1]\n"
"v_pk_mul_f32 v[246:247], v[246:247], v[46:47] op_sel:[0,1] op_sel_hi:[1,1]\n"
"v_cvt_pk_bf16_f32 v60, v132, v133\n"
"v_cvt_pk_bf16_f32 v61, v134, v135\n"
"v_cvt_pk_bf16_f32 v62, v148, v149\n"
"v_cvt_pk_bf16_f32 v63, v150, v151\n"
"v_cvt_pk_bf16_f32 v64, v164, v165\n"
"v_cvt_pk_bf16_f32 v65, v166, v167\n"
"v_cvt_pk_bf16_f32 v66, v180, v181\n"
"v_cvt_pk_bf16_f32 v67, v182, v183\n"
"v_cvt_pk_bf16_f32 v68, v196, v197\n"
"v_cvt_pk_bf16_f32 v69, v198, v199\n"
"v_cvt_pk_bf16_f32 v70, v212, v213\n"
"v_cvt_pk_bf16_f32 v71, v214, v215\n"
"v_cvt_pk_bf16_f32 v72, v228, v229\n"
"v_cvt_pk_bf16_f32 v73, v230, v231\n"
"v_cvt_pk_bf16_f32 v74, v244, v245\n"
"v_cvt_pk_bf16_f32 v75, v246, v247\n"
"v_permlane32_swap_b32 v60, v62\n"
"v_permlane32_swap_b32 v61, v63\n"
"v_permlane32_swap_b32 v64, v66\n"
"v_permlane32_swap_b32 v65, v67\n"
"v_permlane32_swap_b32 v68, v70\n"
"v_permlane32_swap_b32 v69, v71\n"
"v_permlane32_swap_b32 v72, v74\n"
"v_permlane32_swap_b32 v73, v75\n"
"v_accvgpr_write_b32 a68, v60\n"
"v_accvgpr_write_b32 a69, v61\n"
"v_accvgpr_write_b32 a70, v62\n"
"v_accvgpr_write_b32 a71, v63\n"
"v_accvgpr_write_b32 a72, v64\n"
"v_accvgpr_write_b32 a73, v65\n"
"v_accvgpr_write_b32 a74, v66\n"
"v_accvgpr_write_b32 a75, v67\n"
"v_accvgpr_write_b32 a76, v68\n"
"v_accvgpr_write_b32 a77, v69\n"
"v_accvgpr_write_b32 a78, v70\n"
"v_accvgpr_write_b32 a79, v71\n"
"v_accvgpr_write_b32 a80, v72\n"
"v_accvgpr_write_b32 a81, v73\n"
"v_accvgpr_write_b32 a82, v74\n"
"v_accvgpr_write_b32 a83, v75\n"
"s_mov_b32 s95, 43008\n"
"buffer_load_dwordx4 v80, s[20:23], s95 offen offset:1024 lds\n"
"v_pk_fma_f32 v[136:137], v[96:97], v[48:49], v[136:137] op_sel_hi:[1,0,1]\n"
"v_pk_fma_f32 v[138:139], v[98:99], v[48:49], v[138:139] op_sel_hi:[1,0,1]\n"
"v_pk_fma_f32 v[152:153], v[100:101], v[48:49], v[152:153] op_sel_hi:[1,0,1]\n"
"v_pk_fma_f32 v[154:155], v[102:103], v[48:49], v[154:155] op_sel_hi:[1,0,1]\n"
"v_pk_fma_f32 v[168:169], v[104:105], v[48:49], v[168:169] op_sel_hi:[1,0,1]\n"
"v_pk_fma_f32 v[170:171], v[106:107], v[48:49], v[170:171] op_sel_hi:[1,0,1]\n"
"v_pk_fma_f32 v[184:185], v[108:109], v[48:49], v[184:185] op_sel_hi:[1,0,1]\n"
"v_pk_fma_f32 v[186:187], v[110:111], v[48:49], v[186:187] op_sel_hi:[1,0,1]\n"
"v_pk_fma_f32 v[200:201], v[112:113], v[48:49], v[200:201] op_sel_hi:[1,0,1]\n"
"v_pk_fma_f32 v[202:203], v[114:115], v[48:49], v[202:203] op_sel_hi:[1,0,1]\n"
"v_pk_fma_f32 v[216:217], v[116:117], v[48:49], v[216:217] op_sel_hi:[1,0,1]\n"
"v_pk_fma_f32 v[218:219], v[118:119], v[48:49], v[218:219] op_sel_hi:[1,0,1]\n"
"v_pk_fma_f32 v[232:233], v[120:121], v[48:49], v[232:233] op_sel_hi:[1,0,1]\n"
"v_pk_fma_f32 v[234:235], v[122:123], v[48:49], v[234:235] op_sel_hi:[1,0,1]\n"
"v_pk_fma_f32 v[248:249], v[124:125], v[48:49], v[248:249] op_sel_hi:[1,0,1]\n"
"v_pk_fma_f32 v[250:251], v[126:127], v[48:49], v[250:251] op_sel_hi:[1,0,1]\n"
"v_pk_mul_f32 v[136:137], v[136:137], v[48:49] op_sel:[0,1] op_sel_hi:[1,1]\n"
"v_pk_mul_f32 v[138:139], v[138:139], v[48:49] op_sel:[0,1] op_sel_hi:[1,1]\n"
"v_pk_mul_f32 v[152:153], v[152:153], v[48:49] op_sel:[0,1] op_sel_hi:[1,1]\n"
"v_pk_mul_f32 v[154:155], v[154:155], v[48:49] op_sel:[0,1] op_sel_hi:[1,1]\n"
"v_pk_mul_f32 v[168:169], v[168:169], v[48:49] op_sel:[0,1] op_sel_hi:[1,1]\n"
"v_pk_mul_f32 v[170:171], v[170:171], v[48:49] op_sel:[0,1] op_sel_hi:[1,1]\n"
"v_pk_mul_f32 v[184:185], v[184:185], v[48:49] op_sel:[0,1] op_sel_hi:[1,1]\n"
"v_pk_mul_f32 v[186:187], v[186:187], v[48:49] op_sel:[0,1] op_sel_hi:[1,1]\n"
"v_pk_mul_f32 v[200:201], v[200:201], v[48:49] op_sel:[0,1] op_sel_hi:[1,1]\n"
"v_pk_mul_f32 v[202:203], v[202:203], v[48:49] op_sel:[0,1] op_sel_hi:[1,1]\n"
"v_pk_mul_f32 v[216:217], v[216:217], v[48:49] op_sel:[0,1] op_sel_hi:[1,1]\n"
"v_pk_mul_f32 v[218:219], v[218:219], v[48:49] op_sel:[0,1] op_sel_hi:[1,1]\n"
"v_pk_mul_f32 v[232:233], v[232:233], v[48:49] op_sel:[0,1] op_sel_hi:[1,1]\n"
"v_pk_mul_f32 v[234:235], v[234:235], v[48:49] op_sel:[0,1] op_sel_hi:[1,1]\n"
"v_pk_mul_f32 v[248:249], v[248:249], v[48:49] op_sel:[0,1] op_sel_hi:[1,1]\n"
"v_pk_mul_f32 v[250:251], v[250:251], v[48:49] op_sel:[0,1] op_sel_hi:[1,1]\n"
"v_cvt_pk_bf16_f32 v60, v136, v137\n"
"v_cvt_pk_bf16_f32 v61, v138, v139\n"
"v_cvt_pk_bf16_f32 v62, v152, v153\n"
"v_cvt_pk_bf16_f32 v63, v154, v155\n"
"v_cvt_pk_bf16_f32 v64, v168, v169\n"
"v_cvt_pk_bf16_f32 v65, v170, v171\n"
"v_cvt_pk_bf16_f32 v66, v184, v185\n"
"v_cvt_pk_bf16_f32 v67, v186, v187\n"
"v_cvt_pk_bf16_f32 v68, v200, v201\n"
"v_cvt_pk_bf16_f32 v69, v202, v203\n"
"v_cvt_pk_bf16_f32 v70, v216, v217\n"
"v_cvt_pk_bf16_f32 v71, v218, v219\n"
"v_cvt_pk_bf16_f32 v72, v232, v233\n"
"v_cvt_pk_bf16_f32 v73, v234, v235\n"
"v_cvt_pk_bf16_f32 v74, v248, v249\n"
"v_cvt_pk_bf16_f32 v75, v250, v251\n"
"v_permlane32_swap_b32 v60, v62\n"
"v_permlane32_swap_b32 v61, v63\n"
"v_permlane32_swap_b32 v64, v66\n"
"v_permlane32_swap_b32 v65, v67\n"
"v_permlane32_swap_b32 v68, v70\n"
"v_permlane32_swap_b32 v69, v71\n"
"v_permlane32_swap_b32 v72, v74\n"
"v_permlane32_swap_b32 v73, v75\n"
"v_accvgpr_write_b32 a84, v60\n"
"v_accvgpr_write_b32 a85, v61\n"
"v_accvgpr_write_b32 a86, v62\n"
"v_accvgpr_write_b32 a87, v63\n"
"v_accvgpr_write_b32 a88, v64\n"
"v_accvgpr_write_b32 a89, v65\n"
"v_accvgpr_write_b32 a90, v66\n"
"v_accvgpr_write_b32 a91, v67\n"
"v_accvgpr_write_b32 a92, v68\n"
"v_accvgpr_write_b32 a93, v69\n"
"v_accvgpr_write_b32 a94, v70\n"
"v_accvgpr_write_b32 a95, v71\n"
"v_accvgpr_write_b32 a96, v72\n"
"v_accvgpr_write_b32 a97, v73\n"
"v_accvgpr_write_b32 a98, v74\n"
"v_accvgpr_write_b32 a99, v75\n"
"s_mov_b32 s96, 86016\n"
"buffer_load_dwordx4 v80, s[20:23], s96 offen offset:2048 lds\n"
"v_pk_fma_f32 v[140:141], v[96:97], v[50:51], v[140:141] op_sel_hi:[1,0,1]\n"
"v_pk_fma_f32 v[142:143], v[98:99], v[50:51], v[142:143] op_sel_hi:[1,0,1]\n"
"v_pk_fma_f32 v[156:157], v[100:101], v[50:51], v[156:157] op_sel_hi:[1,0,1]\n"
"v_pk_fma_f32 v[158:159], v[102:103], v[50:51], v[158:159] op_sel_hi:[1,0,1]\n"
"v_pk_fma_f32 v[172:173], v[104:105], v[50:51], v[172:173] op_sel_hi:[1,0,1]\n"
"v_pk_fma_f32 v[174:175], v[106:107], v[50:51], v[174:175] op_sel_hi:[1,0,1]\n"
"v_pk_fma_f32 v[188:189], v[108:109], v[50:51], v[188:189] op_sel_hi:[1,0,1]\n"
"v_pk_fma_f32 v[190:191], v[110:111], v[50:51], v[190:191] op_sel_hi:[1,0,1]\n"
"v_pk_fma_f32 v[204:205], v[112:113], v[50:51], v[204:205] op_sel_hi:[1,0,1]\n"
"v_pk_fma_f32 v[206:207], v[114:115], v[50:51], v[206:207] op_sel_hi:[1,0,1]\n"
"v_pk_fma_f32 v[220:221], v[116:117], v[50:51], v[220:221] op_sel_hi:[1,0,1]\n"
"v_pk_fma_f32 v[222:223], v[118:119], v[50:51], v[222:223] op_sel_hi:[1,0,1]\n"
"v_pk_fma_f32 v[236:237], v[120:121], v[50:51], v[236:237] op_sel_hi:[1,0,1]\n"
"v_pk_fma_f32 v[238:239], v[122:123], v[50:51], v[238:239] op_sel_hi:[1,0,1]\n"
"v_pk_fma_f32 v[252:253], v[124:125], v[50:51], v[252:253] op_sel_hi:[1,0,1]\n"
"v_pk_fma_f32 v[254:255], v[126:127], v[50:51], v[254:255] op_sel_hi:[1,0,1]\n"
"v_pk_mul_f32 v[140:141], v[140:141], v[50:51] op_sel:[0,1] op_sel_hi:[1,1]\n"
"v_pk_mul_f32 v[142:143], v[142:143], v[50:51] op_sel:[0,1] op_sel_hi:[1,1]\n"
"v_pk_mul_f32 v[156:157], v[156:157], v[50:51] op_sel:[0,1] op_sel_hi:[1,1]\n"
"v_pk_mul_f32 v[158:159], v[158:159], v[50:51] op_sel:[0,1] op_sel_hi:[1,1]\n"
"v_pk_mul_f32 v[172:173], v[172:173], v[50:51] op_sel:[0,1] op_sel_hi:[1,1]\n"
"v_pk_mul_f32 v[174:175], v[174:175], v[50:51] op_sel:[0,1] op_sel_hi:[1,1]\n"
"v_pk_mul_f32 v[188:189], v[188:189], v[50:51] op_sel:[0,1] op_sel_hi:[1,1]\n"
"v_pk_mul_f32 v[190:191], v[190:191], v[50:51] op_sel:[0,1] op_sel_hi:[1,1]\n"
"v_pk_mul_f32 v[204:205], v[204:205], v[50:51] op_sel:[0,1] op_sel_hi:[1,1]\n"
"v_pk_mul_f32 v[206:207], v[206:207], v[50:51] op_sel:[0,1] op_sel_hi:[1,1]\n"
"v_pk_mul_f32 v[220:221], v[220:221], v[50:51] op_sel:[0,1] op_sel_hi:[1,1]\n"
"v_pk_mul_f32 v[222:223], v[222:223], v[50:51] op_sel:[0,1] op_sel_hi:[1,1]\n"
"v_pk_mul_f32 v[236:237], v[236:237], v[50:51] op_sel:[0,1] op_sel_hi:[1,1]\n"
"v_pk_mul_f32 v[238:239], v[238:239], v[50:51] op_sel:[0,1] op_sel_hi:[1,1]\n"
"v_pk_mul_f32 v[252:253], v[252:253], v[50:51] op_sel:[0,1] op_sel_hi:[1,1]\n"
".Lpg4:\n"
"s_cbranch_execz .Lpg8\n"
"s_nop 0\n"
"v_pk_mul_f32 v[254:255], v[254:255], v[50:51] op_sel:[0,1] op_sel_hi:[1,1]\n"
"v_cvt_pk_bf16_f32 v60, v140, v141\n"
"v_cvt_pk_bf16_f32 v61, v142, v143\n"
"v_cvt_pk_bf16_f32 v62, v156, v157\n"
"v_cvt_pk_bf16_f32 v63, v158, v159\n"
"v_cvt_pk_bf16_f32 v64, v172, v173\n"
"v_cvt_pk_bf16_f32 v65, v174, v175\n"
"v_cvt_pk_bf16_f32 v66, v188, v189\n"
"v_cvt_pk_bf16_f32 v67, v190, v191\n"
"v_cvt_pk_bf16_f32 v68, v204, v205\n"
"v_cvt_pk_bf16_f32 v69, v206, v207\n"
"v_cvt_pk_bf16_f32 v70, v220, v221\n"
"v_cvt_pk_bf16_f32 v71, v222, v223\n"
"v_cvt_pk_bf16_f32 v72, v236, v237\n"
"v_cvt_pk_bf16_f32 v73, v238, v239\n"
"v_cvt_pk_bf16_f32 v74, v252, v253\n"
"v_cvt_pk_bf16_f32 v75, v254, v255\n"
"v_permlane32_swap_b32 v60, v62\n"
"v_permlane32_swap_b32 v61, v63\n"
"v_permlane32_swap_b32 v64, v66\n"
"v_permlane32_swap_b32 v65, v67\n"
"v_permlane32_swap_b32 v68, v70\n"
"v_permlane32_swap_b32 v69, v71\n"
"v_permlane32_swap_b32 v72, v74\n"
"v_permlane32_swap_b32 v73, v75\n"
"v_accvgpr_write_b32 a100, v60\n"
"v_accvgpr_write_b32 a101, v61\n"
"v_accvgpr_write_b32 a102, v62\n"
"v_accvgpr_write_b32 a103, v63\n"
"v_accvgpr_write_b32 a104, v64\n"
"v_accvgpr_write_b32 a105, v65\n"
"v_accvgpr_write_b32 a106, v66\n"
"v_accvgpr_write_b32 a107, v67\n"
"v_accvgpr_write_b32 a108, v68\n"
"v_accvgpr_write_b32 a109, v69\n"
"v_accvgpr_write_b32 a110, v70\n"
"v_accvgpr_write_b32 a111, v71\n"
"v_accvgpr_write_b32 a112, v72\n"
"v_accvgpr_write_b32 a113, v73\n"
"v_accvgpr_write_b32 a114, v74\n"
"v_accvgpr_write_b32 a115, v75\n"
"s_mov_b32 s94, 129024\n"
"buffer_load_dwordx4 v80, s[20:23], s94 offen offset:3072 lds\n"
"v_mfma_f32_16x16x32_bf16 v[16:19], a[148:151], a[52:55], 0\n"
"s_add_u32 m0, s42, 90112\n"
"ds_read_b64_tr_b16 a[196:197], v8 offset:0\n"
"v_mfma_f32_16x16x32_bf16 v[16:19], a[152:155], a[56:59], v[16:19]\n"
"s_waitcnt lgkmcnt(1)\n"
"s_mov_b32 s96, 49152\n"
"ds_read_b64_tr_b16 a[198:199], v8 offset:2048\n"
"v_mfma_f32_16x16x32_bf16 v[16:19], a[156:159], a[60:63], v[16:19]\n"
"ds_read_b64_tr_b16 a[200:201], v9 offset:0\n"
"buffer_load_dwordx4 v3, s[56:59], s96 offen offset:0 lds\n"
"v_mfma_f32_16x16x32_bf16 v[16:19], a[160:163], a[64:67], v[16:19]\n"
"ds_read_b128 a[180:183], v4 offset:36864\n"
"v_mfma_f32_16x16x32_bf16 v[20:23], a[176:179], a[64:67], 0\n"
"s_and_b32 s24, s90, 1\n"
"ds_read_b64_tr_b16 a[202:203], v9 offset:2048\n"
"v_mfma_f32_16x16x32_bf16 v[20:23], a[172:175], a[60:63], v[20:23]\n"
"s_mov_b32 s94, 50176\n"
"ds_read_b128 a[184:187], v5 offset:36864\n"
"v_mfma_f32_16x16x32_bf16 v[20:23], a[168:171], a[56:59], v[20:23]\n"
"s_lshl_b32 s24, s24, 2\n"
"buffer_load_dwordx4 v3, s[56:59], s94 offen offset:1024 lds\n"
"v_mfma_f32_16x16x32_bf16 v[20:23], a[164:167], a[52:55], v[20:23]\n"
"s_add_u32 s24, s24, s17\n"
"v_exp_f32 v16, v16\n"
"v_mfma_f32_16x16x32_bf16 v[24:27], a[148:151], a[68:71], 0\n"
"s_lshl_b32 s38, s24, 2\n"
"v_exp_f32 v17, v17\n"
"v_mfma_f32_16x16x32_bf16 v[24:27], a[152:155], a[72:75], v[24:27]\n"
"s_add_u32 s97, s7, 65536\n"
"v_exp_f32 v18, v18\n"
"v_mfma_f32_16x16x32_bf16 v[24:27], a[156:159], a[76:79], v[24:27]\n"
"s_lshl_b32 s30, s91, 7\n"
"buffer_load_dwordx4 a[144:147], v0, s[72:75], s97 offen\n"
"v_cvt_pk_bf16_f32 v228, v16, v17\n"
"v_mfma_f32_16x16x32_bf16 v[24:27], a[160:163], a[80:83], v[24:27]\n"
"s_add_u32 s30, s30, s38\n"
"v_exp_f32 v19, v19\n"
"v_mfma_f32_16x16x32_bf16 v[28:31], a[176:179], a[80:83], 0\n"
"s_lshl_b32 s30, s30, 12\n"
"ds_read_b128 a[188:191], v6 offset:36864\n"
"v_cvt_pk_bf16_f32 v229, v18, v19\n"
"v_mfma_f32_16x16x32_bf16 v[28:31], a[172:175], a[76:79], v[28:31]\n"
"s_add_u32 m0, s42, 0\n"
"v_exp_f32 v20, v20\n"
"v_mfma_f32_16x16x32_bf16 v[28:31], a[168:171], a[72:75], v[28:31]\n"
"s_mov_b32 s95, 128\n"
"v_exp_f32 v21, v21\n"
"v_mfma_f32_16x16x32_bf16 v[28:31], a[164:167], a[68:71], v[28:31]\n"
"s_add_u32 s30, s30, 4194304\n"
"buffer_load_dwordx4 v3, s[56:59], s95 offen offset:0 lds\n"
"v_cvt_pk_bf16_f32 v230, v20, v21\n"
"v_mfma_f32_16x16x32_bf16 v[32:35], a[148:151], a[84:87], 0\n"
"s_lshr_b32 s6, s90, 1\n"
"v_exp_f32 v22, v22\n"
"v_mfma_f32_16x16x32_bf16 v[32:35], a[152:155], a[88:91], v[32:35]\n"
"s_lshl_b32 s6, s6, 21\n"
"v_exp_f32 v23, v23\n"
"v_mfma_f32_16x16x32_bf16 v[32:35], a[156:159], a[92:95], v[32:35]\n"
"s_mov_b32 s96, 1152\n"
"v_exp_f32 v24, v24\n"
"v_mfma_f32_16x16x32_bf16 v[32:35], a[160:163], a[96:99], v[32:35]\n"
"s_add_u32 s6, s6, 33554432\n"
"buffer_load_dwordx4 v3, s[56:59], s96 offen offset:1024 lds\n"
"v_cvt_pk_bf16_f32 v231, v22, v23\n"
"v_mfma_f32_16x16x32_bf16 v[36:39], a[176:179], a[96:99], 0\n"
"s_and_b32 s31, s90, 1\n"
"v_exp_f32 v25, v25\n"
"v_mfma_f32_16x16x32_bf16 v[36:39], a[172:175], a[92:95], v[36:39]\n"
"s_lshl_b32 s31, s31, 5\n"
"ds_read_b128 a[192:195], v7 offset:36864\n"
"v_cvt_pk_bf16_f32 v232, v24, v25\n"
"v_mfma_f32_16x16x32_bf16 v[36:39], a[168:171], a[88:91], v[36:39]\n"
"s_add_u32 m0, s43, 0\n"
"v_exp_f32 v26, v26\n"
"v_mfma_f32_16x16x32_bf16 v[36:39], a[164:167], a[84:87], v[36:39]\n"
"s_waitcnt lgkmcnt(2)\n"
"s_add_u32 s31, s31, s38\n"
"v_exp_f32 v27, v27\n"
"v_mfma_f32_16x16x32_bf16 a[20:23], a[196:199], v[228:231], 0\n"
"s_lshl_b32 s31, s31, 12\n"
"v_exp_f32 v28, v28\n"
"v_mfma_f32_16x16x32_bf16 a[36:39], a[200:203], v[228:231], 0\n"
"s_add_u32 s31, s31, s6\n"
"buffer_load_dwordx4 v94, s[84:87], 0 offen offset:0 lds\n"
"v_cvt_pk_bf16_f32 v233, v26, v27\n"
"v_mfma_f32_16x16x32_bf16 a[4:7], a[0:3], v[228:231], 0\n"
"s_mov_b32 s96, 7168\n"
"ds_read_b64_tr_b16 a[204:205], v8 offset:4096\n"
"v_exp_f32 v29, v29\n"
"v_mfma_f32_16x16x32_bf16 v[16:19], a[148:151], a[100:103], 0\n"
"s_and_b32 s40, s91, 3\n"
"v_exp_f32 v30, v30\n"
"v_mfma_f32_16x16x32_bf16 v[16:19], a[152:155], a[104:107], v[16:19]\n"
"s_xor_b32 s40, s40, s17\n"
"ds_read_b128 a[148:151], v7 offset:38912\n"
"v_exp_f32 v31, v31\n"
"v_mfma_f32_16x16x32_bf16 v[16:19], a[156:159], a[108:111], v[16:19]\n"
"s_lshl_b32 s24, s40, 10\n"
"buffer_load_dwordx4 v94, s[84:87], s96 offen offset:1024 lds\n"
"v_exp_f32 v32, v32\n"
"v_mfma_f32_16x16x32_bf16 v[16:19], a[160:163], a[112:115], v[16:19]\n"
"s_add_u32 s31, s31, s24\n"
"v_exp_f32 v33, v33\n"
"v_cvt_pk_bf16_f32 v234, v28, v29\n"
"v_mfma_f32_16x16x32_bf16 v[20:23], a[176:179], a[112:115], 0\n"
"s_mov_b32 s94, 14336\n"
"ds_read_b128 a[152:155], v6 offset:38912\n"
"v_exp_f32 v34, v34\n"
"v_mfma_f32_16x16x32_bf16 v[20:23], a[172:175], a[108:111], v[20:23]\n"
"s_lshl_b32 s24, s90, 2\n"
"v_exp_f32 v35, v35\n"
"v_cvt_pk_bf16_f32 v235, v30, v31\n"
"v_mfma_f32_16x16x32_bf16 v[20:23], a[168:171], a[104:107], v[20:23]\n"
"s_add_u32 s40, s40, s24\n"
"ds_read_b128 a[156:159], v5 offset:38912\n"
"v_exp_f32 v36, v36\n"
"v_mfma_f32_16x16x32_bf16 v[20:23], a[164:167], a[100:103], v[20:23]\n"
"s_lshl_b32 s35, s38, 7\n"
"v_exp_f32 v37, v37\n"
"v_cvt_pk_bf16_f32 v236, v32, v33\n"
"v_mfma_f32_16x16x32_bf16 a[24:27], a[196:199], v[232:235], 0\n"
"buffer_load_dwordx4 v94, s[84:87], s94 offen offset:2048 lds\n"
"ds_read_b128 a[160:163], v4 offset:38912\n"
"v_mfma_f32_16x16x32_bf16 a[40:43], a[200:203], v[232:235], 0\n"
"s_mov_b32 s95, 21504\n"
"v_exp_f32 v38, v38\n"
"v_cvt_pk_bf16_f32 v237, v34, v35\n"
"v_mfma_f32_16x16x32_bf16 a[8:11], a[0:3], v[232:235], 0\n"
"s_add_u32 s35, s35, s6\n"
"v_exp_f32 v39, v39\n"
"v_cvt_pk_bf16_f32 v238, v36, v37\n"
"v_mfma_f32_16x16x32_bf16 v[24:27], a[180:183], a[52:55], 0\n"
"buffer_load_dwordx4 v94, s[84:87], s95 offen offset:3072 lds\n"
"ds_read_b64_tr_b16 a[206:207], v8 offset:6144\n"
"v_mfma_f32_16x16x32_bf16 v[24:27], a[184:187], a[56:59], v[24:27]\n"
"s_waitcnt lgkmcnt(4)\n"
"s_add_u32 s35, s35, 1048576\n"
"v_exp_f32 v16, v16\n"
"v_cvt_pk_bf16_f32 v239, v38, v39\n"
"v_mfma_f32_16x16x32_bf16 v[24:27], a[188:191], a[60:63], v[24:27]\n"
"s_lshl_b32 s24, s89, 1\n"
"ds_read_b64_tr_b16 a[208:209], v9 offset:4096\n"
"v_exp_f32 v17, v17\n"
"v_mfma_f32_16x16x32_bf16 v[24:27], a[192:195], a[64:67], v[24:27]\n"
"s_waitcnt vmcnt(29) lgkmcnt(2)\n"
"v_exp_f32 v18, v18\n"
"v_mfma_f32_16x16x32_bf16 v[28:31], a[148:151], a[64:67], 0\n"
"s_barrier\n"
"v_exp_f32 v19, v19\n"
"v_mfma_f32_16x16x32_bf16 v[28:31], a[152:155], a[60:63], v[28:31]\n"
"s_lshr_b32 s33, s17, 1\n"
"v_exp_f32 v20, v20\n"
"v_mfma_f32_16x16x32_bf16 v[28:31], a[156:159], a[56:59], v[28:31]\n"
"s_add_u32 s24, s24, s33\n"
"v_exp_f32 v21, v21\n"
"v_mfma_f32_16x16x32_bf16 v[28:31], a[160:163], a[52:55], v[28:31]\n"
"s_lshl_b32 s24, s24, 21\n"
"v_cvt_pk_bf16_f32 v240, v16, v17\n"
"v_cvt_pk_bf16_f32 v241, v18, v19\n"
"v_mfma_f32_16x16x32_bf16 a[28:31], a[196:199], v[236:239], 0\n"
"s_add_u32 m0, s41, 131072\n"
"v_exp_f32 v22, v22\n"
"v_mfma_f32_16x16x32_bf16 a[44:47], a[200:203], v[236:239], 0\n"
"s_mov_b32 s95, 49152\n"
"v_exp_f32 v23, v23\n"
"v_mfma_f32_16x16x32_bf16 a[12:15], a[0:3], v[236:239], 0\n"
"s_add_u32 s24, s24, 33554432\n"
"ds_read_b128 a[176:179], v4 offset:49152\n"
"v_cvt_pk_bf16_f32 v242, v20, v21\n"
"v_mfma_f32_16x16x32_bf16 v[32:35], a[180:183], a[68:71], 0\n"
"s_and_b32 s33, s17, 1\n"
"buffer_load_dwordx4 v1, s[52:55], s95 offen offset:0 lds\n"
"v_accvgpr_write_b32 a116, v228\n"
"v_mfma_f32_16x16x32_bf16 v[32:35], a[184:187], a[72:75], v[32:35]\n"
"s_lshl_b32 s34, s33, 7\n"
"v_exp_f32 v24, v24\n"
"v_cvt_pk_bf16_f32 v243, v22, v23\n"
"v_mfma_f32_16x16x32_bf16 v[32:35], a[188:191], a[76:79], v[32:35]\n"
"s_add_u32 s34, s34, s88\n"
"ds_read_b64_tr_b16 a[210:211], v9 offset:6144\n"
"v_exp_f32 v25, v25\n"
"v_mfma_f32_16x16x32_bf16 v[32:35], a[192:195], a[80:83], v[32:35]\n"
"s_lshl_b32 s34, s34, 7\n"
"v_exp_f32 v26, v26\n"
"v_mfma_f32_16x16x32_bf16 v[36:39], a[148:151], a[80:83], 0\n"
"s_add_u32 s34, s34, s24\n"
"v_exp_f32 v27, v27\n"
"v_mfma_f32_16x16x32_bf16 v[36:39], a[152:155], a[76:79], v[36:39]\n"
"s_add_u32 s34, s34, 1048576\n"
"ds_read_b128 a[172:175], v5 offset:49152\n"
"v_cvt_pk_bf16_f32 v244, v24, v25\n"
"v_mfma_f32_16x16x32_bf16 v[36:39], a[156:159], a[72:75], v[36:39]\n"
"s_lshl_b32 s33, s33, 5\n"
"v_exp_f32 v28, v28\n"
"v_cvt_pk_bf16_f32 v245, v26, v27\n"
"v_mfma_f32_16x16x32_bf16 v[36:39], a[160:163], a[68:71], v[36:39]\n"
"s_mov_b32 s96, 50176\n"
"v_exp_f32 v29, v29\n"
"v_accvgpr_write_b32 a117, v229\n"
"v_mfma_f32_16x16x32_bf16 a[32:35], a[196:199], v[240:243], 0\n"
"s_add_u32 s33, s33, s88\n"
"v_exp_f32 v30, v30\n"
"v_mfma_f32_16x16x32_bf16 a[48:51], a[200:203], v[240:243], 0\n"
"s_lshl_b32 s33, s33, 12\n"
"v_exp_f32 v31, v31\n"
"v_mfma_f32_16x16x32_bf16 a[16:19], a[0:3], v[240:243], 0\n"
"s_add_u32 s33, s33, s24\n"
"buffer_load_dwordx4 v2, s[52:55], s96 offen offset:1024 lds\n"
"v_cvt_pk_bf16_f32 v246, v28, v29\n"
"v_mfma_f32_16x16x32_bf16 v[16:19], a[180:183], a[84:87], 0\n"
"s_lshl_b32 s36, s38, 17\n"
"ds_read_b128 a[168:171], v6 offset:49152\n"
"v_cvt_pk_bf16_f32 v247, v30, v31\n"
"v_mfma_f32_16x16x32_bf16 v[16:19], a[184:187], a[88:91], v[16:19]\n"
"s_lshl_b32 s24, s91, 7\n"
"ds_read_b128 a[164:167], v7 offset:49152\n"
"v_exp_f32 v32, v32\n"
"v_mfma_f32_16x16x32_bf16 v[16:19], a[188:191], a[92:95], v[16:19]\n"
"s_add_u32 s36, s36, s24\n"
"v_exp_f32 v33, v33\n"
"v_accvgpr_write_b32 a118, v230\n"
"v_mfma_f32_16x16x32_bf16 v[16:19], a[192:195], a[96:99], v[16:19]\n"
"v_exp_f32 v34, v34\n"
"v_mfma_f32_16x16x32_bf16 v[20:23], a[148:151], a[96:99], 0\n"
"v_exp_f32 v35, v35\n"
"v_mfma_f32_16x16x32_bf16 v[20:23], a[152:155], a[92:95], v[20:23]\n"
"v_exp_f32 v36, v36\n"
"v_mfma_f32_16x16x32_bf16 v[20:23], a[156:159], a[88:91], v[20:23]\n"
"s_mov_b32 s94, 51200\n"
"v_exp_f32 v37, v37\n"
"v_mfma_f32_16x16x32_bf16 v[20:23], a[160:163], a[84:87], v[20:23]\n"
"s_waitcnt lgkmcnt(2)\n"
"v_cvt_pk_bf16_f32 v248, v32, v33\n"
"v_cvt_pk_bf16_f32 v249, v34, v35\n"
"v_mfma_f32_16x16x32_bf16 a[20:23], a[204:207], v[244:247], a[20:23]\n"
"v_exp_f32 v38, v38\n"
"v_mfma_f32_16x16x32_bf16 a[36:39], a[208:211], v[244:247], a[36:39]\n"
"buffer_load_dwordx4 v1, s[52:55], s94 offen offset:2048 lds\n"
"v_cvt_pk_bf16_f32 v250, v36, v37\n"
"v_mfma_f32_16x16x32_bf16 a[4:7], a[0:3], v[244:247], a[4:7]\n"
"s_lshl_b32 s24, s15, 7\n"
"ds_read_b64_tr_b16 a[196:197], v8 offset:8192\n"
"v_exp_f32 v39, v39\n"
"v_mfma_f32_16x16x32_bf16 v[24:27], a[180:183], a[100:103], 0\n"
"s_add_u32 s24, s24, 131072\n"
"v_exp_f32 v16, v16\n"
"v_mfma_f32_16x16x32_bf16 v[24:27], a[184:187], a[104:107], v[24:27]\n"
"s_waitcnt vmcnt(23)\n"
"v_exp_f32 v17, v17\n"
"v_mfma_f32_16x16x32_bf16 v[24:27], a[188:191], a[108:111], v[24:27]\n"
"ds_read_b128 a[180:183], v7 offset:51200\n"
"v_cvt_pk_bf16_f32 v251, v38, v39\n"
"v_mfma_f32_16x16x32_bf16 v[24:27], a[192:195], a[112:115], v[24:27]\n"
"buffer_store_dword v12, v14, s[64:67], s24 offen sc1\n"
"ds_read_b128 a[184:187], v6 offset:51200\n"
"v_mfma_f32_16x16x32_bf16 v[28:31], a[148:151], a[112:115], 0\n"
"ds_read_b128 a[188:191], v5 offset:51200\n"
"v_exp_f32 v18, v18\n"
"v_mfma_f32_16x16x32_bf16 v[28:31], a[152:155], a[108:111], v[28:31]\n"
"s_mov_b32 s95, 52224\n"
"v_exp_f32 v19, v19\n"
"v_accvgpr_write_b32 a119, v231\n"
"v_mfma_f32_16x16x32_bf16 v[28:31], a[156:159], a[104:107], v[28:31]\n"
"v_exp_f32 v20, v20\n"
"v_cvt_pk_bf16_f32 v252, v16, v17\n"
"v_mfma_f32_16x16x32_bf16 v[28:31], a[160:163], a[100:103], v[28:31]\n"
"ds_read_b128 a[192:195], v4 offset:51200\n"
"v_exp_f32 v21, v21\n"
"v_mfma_f32_16x16x32_bf16 a[24:27], a[204:207], v[248:251], a[24:27]\n"
"buffer_load_dwordx4 v2, s[52:55], s95 offen offset:3072 lds\n"
"v_cvt_pk_bf16_f32 v253, v18, v19\n"
"v_mfma_f32_16x16x32_bf16 a[40:43], a[208:211], v[248:251], a[40:43]\n"
"v_exp_f32 v22, v22\n"
"v_cvt_pk_bf16_f32 v254, v20, v21\n"
"v_mfma_f32_16x16x32_bf16 a[8:11], a[0:3], v[248:251], a[8:11]\n"
"ds_read_b64_tr_b16 a[198:199], v8 offset:10240\n"
"v_exp_f32 v23, v23\n"
"v_mfma_f32_16x16x32_bf16 v[32:35], a[176:179], a[52:55], 0\n"
"v_exp_f32 v24, v24\n"
"v_mfma_f32_16x16x32_bf16 v[32:35], a[172:175], a[56:59], v[32:35]\n"
"s_waitcnt lgkmcnt(5)\n"
"v_exp_f32 v25, v25\n"
"v_mfma_f32_16x16x32_bf16 v[32:35], a[168:171], a[60:63], v[32:35]\n"
"v_cvt_pk_bf16_f32 v255, v22, v23\n"
"v_accvgpr_write_b32 a120, v232\n"
"v_mfma_f32_16x16x32_bf16 v[32:35], a[164:167], a[64:67], v[32:35]\n"
"s_waitcnt lgkmcnt(3)\n"
"v_exp_f32 v26, v26\n"
"v_mfma_f32_16x16x32_bf16 v[36:39], a[180:183], a[64:67], 0\n"
"s_add_u32 s97, s7, 81920\n"
"v_exp_f32 v27, v27\n"
"v_mfma_f32_16x16x32_bf16 v[36:39], a[184:187], a[60:63], v[36:39]\n"
"s_waitcnt lgkmcnt(2)\n"
"v_exp_f32 v28, v28\n"
"v_mfma_f32_16x16x32_bf16 v[36:39], a[188:191], a[56:59], v[36:39]\n"
"s_waitcnt lgkmcnt(1)\n"
"v_exp_f32 v29, v29\n"
"v_mfma_f32_16x16x32_bf16 v[36:39], a[192:195], a[52:55], v[36:39]\n"
"buffer_load_dwordx4 a[144:147], v0, s[72:75], s97 offen\n"
"v_cvt_pk_bf16_f32 v40, v24, v25\n"
"v_mfma_f32_16x16x32_bf16 a[28:31], a[204:207], v[252:255], a[28:31]\n"
"s_bfe_u32 s11, s90, 0x20001\n"
"ds_read_b64_tr_b16 a[200:201], v9 offset:8192\n"
"v_cvt_pk_bf16_f32 v41, v26, v27\n"
"v_mfma_f32_16x16x32_bf16 a[44:47], a[208:211], v[252:255], a[44:47]\n"
"s_lshl_b32 s9, s11, 13\n"
"ds_read_b128 a[148:151], v4 offset:53248\n"
"v_exp_f32 v30, v30\n"
"v_mfma_f32_16x16x32_bf16 a[12:15], a[0:3], v[252:255], a[12:15]\n"
"s_add_u32 s9, s9, s36\n"
"v_exp_f32 v31, v31\n"
"v_cvt_pk_bf16_f32 v42, v28, v29\n"
"v_mfma_f32_16x16x32_bf16 v[16:19], a[176:179], a[68:71], 0\n"
"s_lshl_b32 s15, s11, 10\n"
"v_exp_f32 v32, v32\n"
"v_mfma_f32_16x16x32_bf16 v[16:19], a[172:175], a[72:75], v[16:19]\n"
"s_add_u32 s15, s15, s30\n"
"v_exp_f32 v33, v33\n"
"v_mfma_f32_16x16x32_bf16 v[16:19], a[168:171], a[76:79], v[16:19]\n"
"v_cvt_pk_bf16_f32 v43, v30, v31\n"
"v_accvgpr_write_b32 a121, v233\n"
"v_mfma_f32_16x16x32_bf16 v[16:19], a[164:167], a[80:83], v[16:19]\n"
"s_add_u32 m0, s42, 8192\n"
"v_exp_f32 v34, v34\n"
"v_mfma_f32_16x16x32_bf16 v[20:23], a[180:183], a[80:83], 0\n"
"s_mov_b32 s94, 16512\n"
"ds_read_b64_tr_b16 a[202:203], v9 offset:10240\n"
"v_cvt_pk_bf16_f32 v44, v32, v33\n"
"v_mfma_f32_16x16x32_bf16 v[20:23], a[184:187], a[76:79], v[20:23]\n"
"ds_read_b128 a[152:155], v5 offset:53248\n"
"v_exp_f32 v35, v35\n"
"v_mfma_f32_16x16x32_bf16 v[20:23], a[188:191], a[72:75], v[20:23]\n"
"buffer_load_dwordx4 v3, s[56:59], s94 offen offset:0 lds\n"
"v_exp_f32 v36, v36\n"
"v_mfma_f32_16x16x32_bf16 v[20:23], a[192:195], a[68:71], v[20:23]\n"
"v_exp_f32 v37, v37\n"
"v_cvt_pk_bf16_f32 v45, v34, v35\n"
"v_mfma_f32_16x16x32_bf16 a[32:35], a[204:207], v[40:43], a[32:35]\n"
"v_exp_f32 v38, v38\n"
"v_mfma_f32_16x16x32_bf16 a[48:51], a[208:211], v[40:43], a[48:51]\n"
"v_exp_f32 v39, v39\n"
"v_mfma_f32_16x16x32_bf16 a[16:19], a[0:3], v[40:43], a[16:19]\n"
"ds_read_b128 a[156:159], v6 offset:53248\n"
"v_cvt_pk_bf16_f32 v46, v36, v37\n"
"v_mfma_f32_16x16x32_bf16 v[24:27], a[176:179], a[84:87], 0\n"
"v_accvgpr_write_b32 a122, v234\n"
"v_cvt_pk_bf16_f32 v47, v38, v39\n"
"v_mfma_f32_16x16x32_bf16 v[24:27], a[172:175], a[88:91], v[24:27]\n"
"ds_read_b128 a[160:163], v7 offset:53248\n"
"v_exp_f32 v16, v16\n"
"v_mfma_f32_16x16x32_bf16 v[24:27], a[168:171], a[92:95], v[24:27]\n"
"s_mov_b32 s95, 17536\n"
"v_exp_f32 v17, v17\n"
"v_accvgpr_write_b32 a123, v235\n"
"v_mfma_f32_16x16x32_bf16 v[24:27], a[164:167], a[96:99], v[24:27]\n"
"v_exp_f32 v18, v18\n"
"v_mfma_f32_16x16x32_bf16 v[28:31], a[180:183], a[96:99], 0\n"
"v_exp_f32 v19, v19\n"
"v_mfma_f32_16x16x32_bf16 v[28:31], a[184:187], a[92:95], v[28:31]\n"
"buffer_load_dwordx4 v3, s[56:59], s95 offen offset:1024 lds\n"
"v_cvt_pk_bf16_f32 v48, v16, v17\n"
"v_mfma_f32_16x16x32_bf16 v[28:31], a[188:191], a[88:91], v[28:31]\n"
"v_exp_f32 v20, v20\n"
"v_cvt_pk_bf16_f32 v49, v18, v19\n"
"v_mfma_f32_16x16x32_bf16 v[28:31], a[192:195], a[84:87], v[28:31]\n"
"s_waitcnt lgkmcnt(2)\n"
"ds_read_b64_tr_b16 a[204:205], v8 offset:12288\n"
"v_exp_f32 v21, v21\n"
"v_mfma_f32_16x16x32_bf16 a[20:23], a[196:199], v[44:47], a[20:23]\n"
"v_exp_f32 v22, v22\n"
"v_mfma_f32_16x16x32_bf16 a[36:39], a[200:203], v[44:47], a[36:39]\n"
"v_exp_f32 v23, v23\n"
"v_mfma_f32_16x16x32_bf16 a[4:7], a[0:3], v[44:47], a[4:7]\n"
"v_cvt_pk_bf16_f32 v50, v20, v21\n"
"v_cvt_pk_bf16_f32 v51, v22, v23\n"
"v_mfma_f32_16x16x32_bf16 v[32:35], a[176:179], a[100:103], 0\n"
"v_exp_f32 v24, v24\n"
"v_mfma_f32_16x16x32_bf16 v[32:35], a[172:175], a[104:107], v[32:35]\n"
"s_add_u32 m0, s42, 16384\n"
"v_exp_f32 v25, v25\n"
"v_mfma_f32_16x16x32_bf16 v[32:35], a[168:171], a[108:111], v[32:35]\n"
"s_mov_b32 s96, 32896\n"
"ds_read_b128 a[176:179], v7 offset:55296\n"
"v_cvt_pk_bf16_f32 v52, v24, v25\n"
"v_mfma_f32_16x16x32_bf16 v[32:35], a[164:167], a[112:115], v[32:35]\n"
"v_exp_f32 v26, v26\n"
"v_mfma_f32_16x16x32_bf16 v[36:39], a[180:183], a[112:115], 0\n"
"ds_read_b128 a[172:175], v6 offset:55296\n"
"buffer_load_dwordx4 v3, s[56:59], s96 offen offset:0 lds\n"
"v_mfma_f32_16x16x32_bf16 v[36:39], a[184:187], a[108:111], v[36:39]\n"
"ds_read_b128 a[168:171], v5 offset:55296\n"
"v_exp_f32 v27, v27\n"
"v_mfma_f32_16x16x32_bf16 v[36:39], a[188:191], a[104:107], v[36:39]\n"
"v_exp_f32 v28, v28\n"
"v_accvgpr_write_b32 a124, v236\n"
"v_mfma_f32_16x16x32_bf16 v[36:39], a[192:195], a[100:103], v[36:39]\n"
"ds_read_b128 a[164:167], v4 offset:55296\n"
"v_exp_f32 v29, v29\n"
"v_mfma_f32_16x16x32_bf16 a[24:27], a[196:199], v[48:51], a[24:27]\n"
"v_exp_f32 v30, v30\n"
"v_mfma_f32_16x16x32_bf16 a[40:43], a[200:203], v[48:51], a[40:43]\n"
"v_cvt_pk_bf16_f32 v53, v26, v27\n"
"v_cvt_pk_bf16_f32 v54, v28, v29\n"
"v_mfma_f32_16x16x32_bf16 a[8:11], a[0:3], v[48:51], a[8:11]\n"
"ds_read_b64_tr_b16 a[206:207], v8 offset:14336\n"
"v_exp_f32 v31, v31\n"
"v_mfma_f32_16x16x32_bf16 v[16:19], a[148:151], a[52:55], 0\n"
"s_mov_b32 s94, 33920\n"
"v_exp_f32 v32, v32\n"
"v_mfma_f32_16x16x32_bf16 v[16:19], a[152:155], a[56:59], v[16:19]\n"
"s_waitcnt lgkmcnt(5)\n"
"v_exp_f32 v33, v33\n"
"v_mfma_f32_16x16x32_bf16 v[16:19], a[156:159], a[60:63], v[16:19]\n"
"s_waitcnt vmcnt(18) lgkmcnt(1)\n"
"buffer_load_dwordx4 v3, s[56:59], s94 offen offset:1024 lds\n"
"v_cvt_pk_bf16_f32 v55, v30, v31\n"
"v_mfma_f32_16x16x32_bf16 v[16:19], a[160:163], a[64:67], v[16:19]\n"
"s_barrier\n"
"v_exp_f32 v34, v34\n"
"v_mfma_f32_16x16x32_bf16 v[20:23], a[176:179], a[64:67], 0\n"
"v_exp_f32 v35, v35\n"
"v_mfma_f32_16x16x32_bf16 v[20:23], a[172:175], a[60:63], v[20:23]\n"
"v_accvgpr_write_b32 a125, v237\n"
"v_cvt_pk_bf16_f32 v56, v32, v33\n"
"v_mfma_f32_16x16x32_bf16 v[20:23], a[168:171], a[56:59], v[20:23]\n"
"ds_read_b64_tr_b16 a[208:209], v9 offset:12288\n"
"v_exp_f32 v36, v36\n"
"v_mfma_f32_16x16x32_bf16 v[20:23], a[164:167], a[52:55], v[20:23]\n"
"v_exp_f32 v37, v37\n"
"v_cvt_pk_bf16_f32 v57, v34, v35\n"
"v_mfma_f32_16x16x32_bf16 a[28:31], a[196:199], v[52:55], a[28:31]\n"
"v_exp_f32 v38, v38\n"
"v_mfma_f32_16x16x32_bf16 a[44:47], a[200:203], v[52:55], a[44:47]\n"
"v_exp_f32 v39, v39\n"
"v_mfma_f32_16x16x32_bf16 a[12:15], a[0:3], v[52:55], a[12:15]\n"
"ds_read_b64_tr_b16 a[212:213], v81 offset:0\n"
"v_cvt_pk_bf16_f32 v58, v36, v37\n"
"v_mfma_f32_16x16x32_bf16 v[24:27], a[148:151], a[68:71], 0\n"
"v_exp_f32 v16, v16\n"
"v_mfma_f32_16x16x32_bf16 v[24:27], a[152:155], a[72:75], v[24:27]\n"
"v_exp_f32 v17, v17\n"
"v_mfma_f32_16x16x32_bf16 v[24:27], a[156:159], a[76:79], v[24:27]\n"
"ds_read_b64_tr_b16 a[214:215], v81 offset:256\n"
"v_cvt_pk_bf16_f32 v59, v38, v39\n"
"v_mfma_f32_16x16x32_bf16 v[24:27], a[160:163], a[80:83], v[24:27]\n"
"ds_read_b64_tr_b16 a[210:211], v9 offset:14336\n"
"v_accvgpr_write_b32 a126, v238\n"
"v_mfma_f32_16x16x32_bf16 v[28:31], a[176:179], a[80:83], 0\n"
"ds_read_b64_tr_b16 a[216:217], v81 offset:2048\n"
"v_exp_f32 v18, v18\n"
"v_mfma_f32_16x16x32_bf16 v[28:31], a[172:175], a[76:79], v[28:31]\n"
"s_add_u32 m0, s41, 147456\n"
"v_exp_f32 v19, v19\n"
"v_cvt_pk_bf16_f32 v60, v16, v17\n"
"v_mfma_f32_16x16x32_bf16 v[28:31], a[168:171], a[72:75], v[28:31]\n"
"s_mov_b32 s96, 65536\n"
"ds_read_b128 a[180:183], v4 offset:0\n"
"v_exp_f32 v20, v20\n"
"v_mfma_f32_16x16x32_bf16 v[28:31], a[164:167], a[68:71], v[28:31]\n"
"buffer_load_dwordx4 v1, s[52:55], s96 offen offset:0 lds\n"
"v_exp_f32 v21, v21\n"
"v_mfma_f32_16x16x32_bf16 a[32:35], a[196:199], v[56:59], a[32:35]\n"
"v_exp_f32 v22, v22\n"
"v_mfma_f32_16x16x32_bf16 a[48:51], a[200:203], v[56:59], a[48:51]\n"
"ds_read_b64_tr_b16 a[218:219], v81 offset:2304\n"
"v_cvt_pk_bf16_f32 v61, v18, v19\n"
"v_mfma_f32_16x16x32_bf16 a[16:19], a[0:3], v[56:59], a[16:19]\n"
"v_exp_f32 v23, v23\n"
"v_cvt_pk_bf16_f32 v62, v20, v21\n"
"v_mfma_f32_16x16x32_bf16 v[32:35], a[148:151], a[84:87], 0\n"
"v_exp_f32 v24, v24\n"
"v_mfma_f32_16x16x32_bf16 v[32:35], a[152:155], a[88:91], v[32:35]\n"
"v_accvgpr_write_b32 a127, v239\n"
"v_cvt_pk_bf16_f32 v63, v22, v23\n"
"v_mfma_f32_16x16x32_bf16 v[32:35], a[156:159], a[92:95], v[32:35]\n"
"ds_read_b128 a[184:187], v5 offset:0\n"
"v_exp_f32 v25, v25\n"
"v_mfma_f32_16x16x32_bf16 v[32:35], a[160:163], a[96:99], v[32:35]\n"
"ds_read_b64_tr_b16 a[220:221], v81 offset:4096\n"
"v_cvt_pk_bf16_f32 v64, v24, v25\n"
"v_mfma_f32_16x16x32_bf16 v[36:39], a[176:179], a[96:99], 0\n"
"ds_read_b64_tr_b16 a[222:223], v81 offset:4352\n"
"v_exp_f32 v26, v26\n"
"v_mfma_f32_16x16x32_bf16 v[36:39], a[172:175], a[92:95], v[36:39]\n"
"s_mov_b32 s94, 66560\n"
"ds_read_b128 a[188:191], v6 offset:0\n"
"v_exp_f32 v27, v27\n"
"v_mfma_f32_16x16x32_bf16 v[36:39], a[168:171], a[88:91], v[36:39]\n"
"buffer_load_dwordx4 v2, s[52:55], s94 offen offset:1024 lds\n"
"v_exp_f32 v28, v28\n"
"v_mfma_f32_16x16x32_bf16 v[36:39], a[164:167], a[84:87], v[36:39]\n"
"s_waitcnt lgkmcnt(5)\n"
"ds_read_b64_tr_b16 a[224:225], v81 offset:6144\n"
"v_exp_f32 v29, v29\n"
"v_mfma_f32_16x16x32_bf16 a[20:23], a[204:207], v[60:63], a[20:23]\n"
"v_exp_f32 v30, v30\n"
"v_mfma_f32_16x16x32_bf16 a[36:39], a[208:211], v[60:63], a[36:39]\n"
"v_cvt_pk_bf16_f32 v65, v26, v27\n"
"v_accvgpr_write_b32 a128, v240\n"
"v_mfma_f32_16x16x32_bf16 a[4:7], a[0:3], v[60:63], a[4:7]\n"
"v_exp_f32 v31, v31\n"
"v_cvt_pk_bf16_f32 v66, v28, v29\n"
"v_mfma_f32_16x16x32_bf16 v[16:19], a[148:151], a[100:103], 0\n"
"v_exp_f32 v32, v32\n"
"v_mfma_f32_16x16x32_bf16 v[16:19], a[152:155], a[104:107], v[16:19]\n"
"ds_read_b64_tr_b16 a[226:227], v81 offset:6400\n"
"v_cvt_pk_bf16_f32 v67, v30, v31\n"
"v_mfma_f32_16x16x32_bf16 v[16:19], a[156:159], a[108:111], v[16:19]\n"
"ds_read_b128 a[192:195], v7 offset:0\n"
"v_exp_f32 v33, v33\n"
"v_mfma_f32_16x16x32_bf16 v[16:19], a[160:163], a[112:115], v[16:19]\n"
"ds_read_b128 a[148:151], v7 offset:2048\n"
"v_cvt_pk_bf16_f32 v68, v32, v33\n"
"v_mfma_f32_16x16x32_bf16 v[20:23], a[176:179], a[112:115], 0\n"
"ds_read_b128 a[152:155], v6 offset:2048\n"
"v_exp_f32 v34, v34\n"
"v_mfma_f32_16x16x32_bf16 v[20:23], a[172:175], a[108:111], v[20:23]\n"
"ds_read_b128 a[156:159], v5 offset:2048\n"
"v_exp_f32 v35, v35\n"
"v_mfma_f32_16x16x32_bf16 v[20:23], a[168:171], a[104:107], v[20:23]\n"
"s_mov_b32 s95, 67584\n"
"ds_read_b128 a[160:163], v4 offset:2048\n"
"v_exp_f32 v36, v36\n"
"v_mfma_f32_16x16x32_bf16 v[20:23], a[164:167], a[100:103], v[20:23]\n"
"v_exp_f32 v37, v37\n"
"v_accvgpr_write_b32 a129, v241\n"
"v_mfma_f32_16x16x32_bf16 a[24:27], a[204:207], v[64:67], a[24:27]\n"
"v_exp_f32 v38, v38\n"
"v_mfma_f32_16x16x32_bf16 a[40:43], a[208:211], v[64:67], a[40:43]\n"
"buffer_load_dwordx4 v1, s[52:55], s95 offen offset:2048 lds\n"
"v_cvt_pk_bf16_f32 v69, v34, v35\n"
"v_mfma_f32_16x16x32_bf16 a[8:11], a[0:3], v[64:67], a[8:11]\n"
"v_exp_f32 v39, v39\n"
"v_cvt_pk_bf16_f32 v70, v36, v37\n"
"v_mfma_f32_16x16x32_bf16 v[144:147], a[212:215], a[228:231], 0\n"
"s_waitcnt lgkmcnt(6)\n"
"v_cmp_ne_u32 s[28:29], s92, v88\n"
"v_cvt_pk_bf16_f32 v71, v38, v39\n"
"v_mfma_f32_16x16x32_bf16 v[144:147], a[216:219], a[232:235], v[144:147]\n"
"s_cmp_lg_u64 s[28:29], 0\n"
"s_cbranch_scc1 .Lrt2\n.Lbk2:\n"
"ds_read_b64_tr_b16 a[196:197], v8 offset:16384\n"
"v_mfma_f32_16x16x32_bf16 v[144:147], a[220:223], a[236:239], v[144:147]\n"
"s_waitcnt lgkmcnt(4)\n"
"v_exp_f32 v16, v16\n"
"v_lshlrev_b32 v88, 16, v89\n"
"v_mfma_f32_16x16x32_bf16 v[144:147], a[224:227], a[240:243], v[144:147]\n"
"v_exp_f32 v17, v17\n"
"v_and_b32 v89, 0xffff0000, v89\n"
"v_mfma_f32_16x16x32_bf16 v[148:151], a[212:215], a[0:3], 0\n"
"v_exp_f32 v18, v18\n"
"v_accvgpr_write_b32 a130, v242\n"
"v_mfma_f32_16x16x32_bf16 v[148:151], a[216:219], a[0:3], v[148:151]\n"
"v_exp_f32 v19, v19\n"
"v_cvt_pk_bf16_f32 v72, v16, v17\n"
"v_mfma_f32_16x16x32_bf16 v[148:151], a[220:223], a[0:3], v[148:151]\n"
"v_exp_f32 v20, v20\n"
"v_cvt_pk_bf16_f32 v73, v18, v19\n"
"v_mfma_f32_16x16x32_bf16 v[148:151], a[224:227], a[0:3], v[148:151]\n"
"s_mov_b32 s96, 68608\n"
"ds_read_b64_tr_b16 a[198:199], v8 offset:18432\n"
"v_exp_f32 v21, v21\n"
"v_mfma_f32_16x16x32_bf16 v[24:27], a[180:183], a[52:55], 0\n"
"v_exp_f32 v22, v22\n"
"v_mfma_f32_16x16x32_bf16 v[24:27], a[184:187], a[56:59], v[24:27]\n"
".Lpg5:\n"
"s_cbranch_execz .Lpg9\n"
"v_exp_f32 v23, v23\n"
"v_mfma_f32_16x16x32_bf16 v[24:27], a[188:191], a[60:63], v[24:27]\n"
"buffer_load_dwordx4 v2, s[52:55], s96 offen offset:3072 lds\n"
"v_cvt_pk_bf16_f32 v74, v20, v21\n"
"v_mfma_f32_16x16x32_bf16 v[24:27], a[192:195], a[64:67], v[24:27]\n"
"s_waitcnt lgkmcnt(2)\n"
"v_rcp_f32 v89, v89\n"
"v_mfma_f32_16x16x32_bf16 v[28:31], a[148:151], a[64:67], 0\n"
"s_mov_b32 s28, 0xcccccccc\n"
"v_cvt_pk_bf16_f32 v75, v22, v23\n"
"v_accvgpr_write_b32 a131, v243\n"
"v_mfma_f32_16x16x32_bf16 v[28:31], a[152:155], a[60:63], v[28:31]\n"
"s_mov_b32 s29, 0xcccccccc\n"
"ds_read_b64_tr_b16 a[200:201], v9 offset:16384\n"
"v_accvgpr_write_b32 a132, v244\n"
"v_mfma_f32_16x16x32_bf16 v[28:31], a[156:159], a[56:59], v[28:31]\n"
"ds_read_b128 a[176:179], v4 offset:4096\n"
"v_cndmask_b32 v144, v144, v146, s[28:29]\n"
"v_mfma_f32_16x16x32_bf16 v[28:31], a[160:163], a[52:55], v[28:31]\n"
"v_cndmask_b32 v145, v145, v147, s[28:29]\n"
"v_cndmask_b32 v148, v148, v150, s[28:29]\n"
"v_mfma_f32_16x16x32_bf16 a[28:31], a[204:207], v[68:71], a[28:31]\n"
"v_exp_f32 v24, v24\n"
"v_mfma_f32_16x16x32_bf16 a[44:47], a[208:211], v[68:71], a[44:47]\n"
"v_exp_f32 v25, v25\n"
"v_mfma_f32_16x16x32_bf16 a[12:15], a[0:3], v[68:71], a[12:15]\n"
"s_add_u32 s97, s7, 98304\n"
"v_exp_f32 v26, v26\n"
"v_mfma_f32_16x16x32_bf16 v[32:35], a[180:183], a[68:71], 0\n"
"buffer_load_dwordx4 a[144:147], v0, s[72:75], s97 offen\n"
"ds_read_b64_tr_b16 a[202:203], v9 offset:18432\n"
"v_mfma_f32_16x16x32_bf16 v[32:35], a[184:187], a[72:75], v[32:35]\n"
"ds_read_b128 a[172:175], v5 offset:4096\n"
"v_cndmask_b32 v149, v149, v151, s[28:29]\n"
"v_mfma_f32_16x16x32_bf16 v[32:35], a[188:191], a[76:79], v[32:35]\n"
"v_exp_f32 v27, v27\n"
"v_mfma_f32_16x16x32_bf16 v[32:35], a[192:195], a[80:83], v[32:35]\n"
"v_fma_f32 v144, -v88, v148, v144\n"
"v_cvt_pk_bf16_f32 v76, v24, v25\n"
"v_mfma_f32_16x16x32_bf16 v[36:39], a[148:151], a[80:83], 0\n"
"v_exp_f32 v28, v28\n"
"v_mfma_f32_16x16x32_bf16 v[36:39], a[152:155], a[76:79], v[36:39]\n"
"v_exp_f32 v29, v29\n"
"v_mfma_f32_16x16x32_bf16 v[36:39], a[156:159], a[72:75], v[36:39]\n"
"v_mov_b32 v136, v12\n"
"v_cvt_pk_bf16_f32 v77, v26, v27\n"
"v_mfma_f32_16x16x32_bf16 v[36:39], a[160:163], a[68:71], v[36:39]\n"
"v_exp_f32 v30, v30\n"
"v_mfma_f32_16x16x32_bf16 a[32:35], a[204:207], v[72:75], a[32:35]\n"
"v_fma_f32 v145, -v88, v149, v145\n"
"v_cvt_pk_bf16_f32 v78, v28, v29\n"
"v_mfma_f32_16x16x32_bf16 a[48:51], a[208:211], v[72:75], a[48:51]\n"
"v_exp_f32 v31, v31\n"
"v_mfma_f32_16x16x32_bf16 a[16:19], a[0:3], v[72:75], a[16:19]\n"
"ds_read_b128 a[168:171], v6 offset:4096\n"
"v_mov_b32 v138, v12\n"
"v_mfma_f32_16x16x32_bf16 v[16:19], a[180:183], a[84:87], 0\n"
"s_add_u32 s97, s6, 1572864\n"
"v_exp_f32 v32, v32\n"
"v_mfma_f32_16x16x32_bf16 v[16:19], a[184:187], a[88:91], v[16:19]\n"
"v_exp_f32 v33, v33\n"
"v_mfma_f32_16x16x32_bf16 v[16:19], a[188:191], a[92:95], v[16:19]\n"
"v_cvt_pk_bf16_f32 v79, v30, v31\n"
"v_mul_f32 v137, v89, v144\n"
"v_mfma_f32_16x16x32_bf16 v[16:19], a[192:195], a[96:99], v[16:19]\n"
"buffer_load_dword a144, v14, s[64:67], s97 offen\n"
"v_accvgpr_write_b32 a133, v245\n"
"v_mfma_f32_16x16x32_bf16 v[20:23], a[148:151], a[96:99], 0\n"
"v_exp_f32 v34, v34\n"
"v_mul_f32 v139, v89, v145\n"
"v_mfma_f32_16x16x32_bf16 v[20:23], a[152:155], a[92:95], v[20:23]\n"
"ds_read_b128 a[164:167], v7 offset:4096\n"
"v_exp_f32 v35, v35\n"
"v_mfma_f32_16x16x32_bf16 v[20:23], a[156:159], a[88:91], v[20:23]\n"
"buffer_store_dwordx4 v[136:139], v93, s[64:67], s37 offen sc1\n"
"v_exp_f32 v36, v36\n"
"v_mfma_f32_16x16x32_bf16 v[20:23], a[160:163], a[84:87], v[20:23]\n"
"s_waitcnt lgkmcnt(2)\n"
"v_exp_f32 v37, v37\n"
"v_cvt_pk_bf16_f32 v80, v32, v33\n"
"v_mfma_f32_16x16x32_bf16 a[20:23], a[196:199], v[76:79], a[20:23]\n"
"v_exp_f32 v38, v38\n"
"v_mfma_f32_16x16x32_bf16 a[36:39], a[200:203], v[76:79], a[36:39]\n"
"v_cvt_pk_bf16_f32 v81, v34, v35\n"
"v_cvt_pk_bf16_f32 v82, v36, v37\n"
"v_mfma_f32_16x16x32_bf16 a[4:7], a[0:3], v[76:79], a[4:7]\n"
"v_exp_f32 v39, v39\n"
"v_accvgpr_write_b32 a134, v246\n"
"v_mfma_f32_16x16x32_bf16 v[24:27], a[180:183], a[100:103], 0\n"
"v_exp_f32 v16, v16\n"
"v_mfma_f32_16x16x32_bf16 v[24:27], a[184:187], a[104:107], v[24:27]\n"
"s_add_u32 s97, s34, 524288\n"
"v_exp_f32 v17, v17\n"
"v_mfma_f32_16x16x32_bf16 v[24:27], a[188:191], a[108:111], v[24:27]\n"
"ds_read_b128 a[180:183], v7 offset:6144\n"
"v_cvt_pk_bf16_f32 v83, v38, v39\n"
"v_mfma_f32_16x16x32_bf16 v[24:27], a[192:195], a[112:115], v[24:27]\n"
"v_exp_f32 v18, v18\n"
"v_mfma_f32_16x16x32_bf16 v[28:31], a[148:151], a[112:115], 0\n"
"ds_read_b128 a[184:187], v6 offset:6144\n"
"buffer_load_dword a144, v14, s[64:67], s97 offen\n"
"v_mfma_f32_16x16x32_bf16 v[28:31], a[152:155], a[108:111], v[28:31]\n"
"ds_read_b64_tr_b16 a[204:205], v8 offset:20480\n"
"v_exp_f32 v19, v19\n"
"v_mfma_f32_16x16x32_bf16 v[28:31], a[156:159], a[104:107], v[28:31]\n"
"ds_read_b128 a[188:191], v5 offset:6144\n"
"v_exp_f32 v20, v20\n"
"v_mfma_f32_16x16x32_bf16 v[28:31], a[160:163], a[100:103], v[28:31]\n"
"v_exp_f32 v21, v21\n"
"v_cvt_pk_bf16_f32 v84, v16, v17\n"
"v_mfma_f32_16x16x32_bf16 a[24:27], a[196:199], v[80:83], a[24:27]\n"
"ds_read_b128 a[192:195], v4 offset:6144\n"
"v_cvt_pk_bf16_f32 v85, v18, v19\n"
"v_mfma_f32_16x16x32_bf16 a[40:43], a[200:203], v[80:83], a[40:43]\n"
"v_exp_f32 v22, v22\n"
"v_accvgpr_write_b32 a135, v247\n"
"v_mfma_f32_16x16x32_bf16 a[8:11], a[0:3], v[80:83], a[8:11]\n"
"v_exp_f32 v23, v23\n"
"v_cvt_pk_bf16_f32 v86, v20, v21\n"
"v_mfma_f32_16x16x32_bf16 v[32:35], a[176:179], a[52:55], 0\n"
"v_exp_f32 v24, v24\n"
"v_mfma_f32_16x16x32_bf16 v[32:35], a[172:175], a[56:59], v[32:35]\n"
"s_waitcnt lgkmcnt(5)\n"
"ds_read_b64_tr_b16 a[206:207], v8 offset:22528\n"
"v_cvt_pk_bf16_f32 v87, v22, v23\n"
"v_mfma_f32_16x16x32_bf16 v[32:35], a[168:171], a[60:63], v[32:35]\n"
"ds_read_b64_tr_b16 a[208:209], v9 offset:20480\n"
"v_exp_f32 v25, v25\n"
"v_mfma_f32_16x16x32_bf16 v[32:35], a[164:167], a[64:67], v[32:35]\n"
"s_waitcnt vmcnt(13) lgkmcnt(2)\n"
"v_exp_f32 v26, v26\n"
"v_mfma_f32_16x16x32_bf16 v[36:39], a[180:183], a[64:67], 0\n"
"s_barrier\n"
"v_exp_f32 v27, v27\n"
"v_mfma_f32_16x16x32_bf16 v[36:39], a[184:187], a[60:63], v[36:39]\n"
"v_exp_f32 v28, v28\n"
"v_mfma_f32_16x16x32_bf16 v[36:39], a[188:191], a[56:59], v[36:39]\n"
"v_exp_f32 v29, v29\n"
"v_mfma_f32_16x16x32_bf16 v[36:39], a[192:195], a[52:55], v[36:39]\n"
"v_cvt_pk_bf16_f32 v88, v24, v25\n"
"v_accvgpr_write_b32 a136, v248\n"
"v_mfma_f32_16x16x32_bf16 a[28:31], a[196:199], v[84:87], a[28:31]\n"
"s_add_u32 m0, s42, 98304\n"
"v_exp_f32 v30, v30\n"
"v_mfma_f32_16x16x32_bf16 a[44:47], a[200:203], v[84:87], a[44:47]\n"
"s_mov_b32 s94, 65536\n"
"ds_read_b128 a[148:151], v4 offset:32768\n"
"v_cvt_pk_bf16_f32 v89, v26, v27\n"
"v_mfma_f32_16x16x32_bf16 a[12:15], a[0:3], v[84:87], a[12:15]\n"
"v_exp_f32 v31, v31\n"
"v_cvt_pk_bf16_f32 v90, v28, v29\n"
"v_mfma_f32_16x16x32_bf16 v[16:19], a[176:179], a[68:71], 0\n"
"buffer_load_dwordx4 v3, s[56:59], s94 offen offset:0 lds\n"
"v_cvt_pk_bf16_f32 v91, v30, v31\n"
"v_mfma_f32_16x16x32_bf16 v[16:19], a[172:175], a[72:75], v[16:19]\n"
"ds_read_b64_tr_b16 a[210:211], v9 offset:22528\n"
"v_exp_f32 v32, v32\n"
"v_mfma_f32_16x16x32_bf16 v[16:19], a[168:171], a[76:79], v[16:19]\n"
"ds_read_b128 a[152:155], v5 offset:32768\n"
"v_exp_f32 v33, v33\n"
"v_mfma_f32_16x16x32_bf16 v[16:19], a[164:167], a[80:83], v[16:19]\n"
"v_exp_f32 v34, v34\n"
"v_mfma_f32_16x16x32_bf16 v[20:23], a[180:183], a[80:83], 0\n"
"v_exp_f32 v35, v35\n"
"v_mfma_f32_16x16x32_bf16 v[20:23], a[184:187], a[76:79], v[20:23]\n"
"v_exp_f32 v36, v36\n"
"v_mfma_f32_16x16x32_bf16 v[20:23], a[188:191], a[72:75], v[20:23]\n"
"s_mov_b32 s95, 66560\n"
"v_accvgpr_write_b32 a137, v249\n"
"v_cvt_pk_bf16_f32 v92, v32, v33\n"
"v_mfma_f32_16x16x32_bf16 v[20:23], a[192:195], a[68:71], v[20:23]\n"
"v_exp_f32 v37, v37\n"
"v_cvt_pk_bf16_f32 v93, v34, v35\n"
"v_mfma_f32_16x16x32_bf16 a[32:35], a[196:199], v[88:91], a[32:35]\n"
"v_exp_f32 v38, v38\n"
"v_mfma_f32_16x16x32_bf16 a[48:51], a[200:203], v[88:91], a[48:51]\n"
"buffer_load_dwordx4 v3, s[56:59], s95 offen offset:1024 lds\n"
"v_cvt_pk_bf16_f32 v94, v36, v37\n"
"v_mfma_f32_16x16x32_bf16 a[16:19], a[0:3], v[88:91], a[16:19]\n"
"ds_read_b128 a[156:159], v6 offset:32768\n"
"v_exp_f32 v39, v39\n"
"v_mfma_f32_16x16x32_bf16 v[24:27], a[176:179], a[84:87], 0\n"
"s_lshr_b32 s39, s12, 7\n"
"v_exp_f32 v16, v16\n"
"v_mfma_f32_16x16x32_bf16 v[24:27], a[172:175], a[88:91], v[24:27]\n"
"s_and_b32 s39, s39, 63\n"
"v_exp_f32 v17, v17\n"
"v_mfma_f32_16x16x32_bf16 v[24:27], a[168:171], a[92:95], v[24:27]\n"
"s_mul_i32 s37, s39, 6144\n"
"v_cvt_pk_bf16_f32 v95, v38, v39\n"
"v_accvgpr_write_b32 a138, v250\n"
"v_mfma_f32_16x16x32_bf16 v[24:27], a[164:167], a[96:99], v[24:27]\n"
"s_add_u32 s37, s37, 1048576\n"
"v_exp_f32 v18, v18\n"
"v_mfma_f32_16x16x32_bf16 v[28:31], a[180:183], a[96:99], 0\n"
"s_add_u32 m0, s42, 106496\n"
"v_exp_f32 v19, v19\n"
"v_mfma_f32_16x16x32_bf16 v[28:31], a[184:187], a[92:95], v[28:31]\n"
"s_mov_b32 s95, 81920\n"
"v_exp_f32 v20, v20\n"
"v_mfma_f32_16x16x32_bf16 v[28:31], a[188:191], a[88:91], v[28:31]\n"
"s_sub_u32 s39, s12, 262144\n"
"ds_read_b128 a[160:163], v7 offset:32768\n"
"v_cvt_pk_bf16_f32 v96, v16, v17\n"
"v_mfma_f32_16x16x32_bf16 v[28:31], a[192:195], a[84:87], v[28:31]\n"
"s_waitcnt lgkmcnt(2)\n"
"s_lshr_b32 s39, s39, 16\n"
"buffer_load_dwordx4 v3, s[56:59], s95 offen offset:0 lds\n"
"v_exp_f32 v21, v21\n"
"v_mfma_f32_16x16x32_bf16 a[20:23], a[204:207], v[92:95], a[20:23]\n"
"s_lshl_b32 s39, s39, 28\n"
"v_exp_f32 v22, v22\n"
"v_mfma_f32_16x16x32_bf16 a[36:39], a[208:211], v[92:95], a[36:39]\n"
"v_cvt_pk_bf16_f32 v97, v18, v19\n"
"v_cvt_pk_bf16_f32 v98, v20, v21\n"
"v_mfma_f32_16x16x32_bf16 a[4:7], a[0:3], v[92:95], a[4:7]\n"
"v_exp_f32 v23, v23\n"
"v_accvgpr_write_b32 a139, v251\n"
"v_mfma_f32_16x16x32_bf16 v[32:35], a[176:179], a[100:103], 0\n"
"v_exp_f32 v24, v24\n"
"v_mfma_f32_16x16x32_bf16 v[32:35], a[172:175], a[104:107], v[32:35]\n"
"v_exp_f32 v25, v25\n"
"v_mfma_f32_16x16x32_bf16 v[32:35], a[168:171], a[108:111], v[32:35]\n"
"s_mov_b32 s96, 82944\n"
"ds_read_b128 a[176:179], v7 offset:34816\n"
"v_cvt_pk_bf16_f32 v99, v22, v23\n"
"v_mfma_f32_16x16x32_bf16 v[32:35], a[164:167], a[112:115], v[32:35]\n"
"v_exp_f32 v26, v26\n"
"v_mfma_f32_16x16x32_bf16 v[36:39], a[180:183], a[112:115], 0\n"
"ds_read_b128 a[172:175], v6 offset:34816\n"
"buffer_load_dwordx4 v3, s[56:59], s96 offen offset:1024 lds\n"
"v_mfma_f32_16x16x32_bf16 v[36:39], a[184:187], a[108:111], v[36:39]\n"
"ds_read_b64_tr_b16 a[196:197], v8 offset:24576\n"
"v_exp_f32 v27, v27\n"
"v_mfma_f32_16x16x32_bf16 v[36:39], a[188:191], a[104:107], v[36:39]\n"
"ds_read_b128 a[168:171], v5 offset:34816\n"
"v_exp_f32 v28, v28\n"
"v_mfma_f32_16x16x32_bf16 v[36:39], a[192:195], a[100:103], v[36:39]\n"
"v_exp_f32 v29, v29\n"
"v_cvt_pk_bf16_f32 v100, v24, v25\n"
"v_mfma_f32_16x16x32_bf16 a[24:27], a[204:207], v[96:99], a[24:27]\n"
"ds_read_b128 a[164:167], v4 offset:34816\n"
"v_cvt_pk_bf16_f32 v101, v26, v27\n"
"v_mfma_f32_16x16x32_bf16 a[40:43], a[208:211], v[96:99], a[40:43]\n"
"v_exp_f32 v30, v30\n"
"v_cvt_pk_bf16_f32 v102, v28, v29\n"
"v_mfma_f32_16x16x32_bf16 a[8:11], a[0:3], v[96:99], a[8:11]\n"
"v_exp_f32 v31, v31\n"
"v_accvgpr_write_b32 a140, v252\n"
"v_mfma_f32_16x16x32_bf16 v[16:19], a[148:151], a[52:55], 0\n"
"s_add_u32 s97, s7, 114688\n"
"v_exp_f32 v32, v32\n"
"v_mfma_f32_16x16x32_bf16 v[16:19], a[152:155], a[56:59], v[16:19]\n"
"s_waitcnt lgkmcnt(5)\n"
"ds_read_b64_tr_b16 a[198:199], v8 offset:26624\n"
"v_lshrrev_b32 v228, 4, v0\n"
"v_mfma_f32_16x16x32_bf16 v[16:19], a[156:159], a[60:63], v[16:19]\n"
"v_exp_f32 v33, v33\n"
"v_cvt_pk_bf16_f32 v103, v30, v31\n"
"v_mfma_f32_16x16x32_bf16 v[16:19], a[160:163], a[64:67], v[16:19]\n"
"s_waitcnt lgkmcnt(5)\n"
"buffer_load_dwordx4 a[144:147], v0, s[72:75], s97 offen\n"
"v_and_b32 v228, 0x30, v228\n"
"v_mfma_f32_16x16x32_bf16 v[20:23], a[176:179], a[64:67], 0\n"
"s_waitcnt lgkmcnt(4)\n"
"v_exp_f32 v34, v34\n"
"v_cvt_pk_bf16_f32 v104, v32, v33\n"
"v_mfma_f32_16x16x32_bf16 v[20:23], a[172:175], a[60:63], v[20:23]\n"
"s_waitcnt lgkmcnt(2)\n"
"v_exp_f32 v35, v35\n"
"v_and_b32 v229, 16, v0\n"
"v_mfma_f32_16x16x32_bf16 v[20:23], a[168:171], a[56:59], v[20:23]\n"
"s_waitcnt lgkmcnt(1)\n"
"ds_read_b64_tr_b16 a[200:201], v9 offset:24576\n"
"v_exp_f32 v36, v36\n"
"v_mfma_f32_16x16x32_bf16 v[20:23], a[164:167], a[52:55], v[20:23]\n"
"v_exp_f32 v37, v37\n"
"v_cvt_pk_bf16_f32 v105, v34, v35\n"
"v_mfma_f32_16x16x32_bf16 a[28:31], a[204:207], v[100:103], a[28:31]\n"
"v_exp_f32 v38, v38\n"
"v_mfma_f32_16x16x32_bf16 a[44:47], a[208:211], v[100:103], a[44:47]\n"
"s_add_u32 m0, s42, 24576\n"
"ds_read_b128 a[180:183], v4 offset:36864\n"
"v_accvgpr_write_b32 a141, v253\n"
"v_mfma_f32_16x16x32_bf16 a[12:15], a[0:3], v[100:103], a[12:15]\n"
"s_mov_b32 s94, 49280\n"
"v_exp_f32 v39, v39\n"
"v_lshrrev_b32 v233, 1, v228\n"
"v_mfma_f32_16x16x32_bf16 v[24:27], a[148:151], a[68:71], 0\n"
"buffer_load_dwordx4 v3, s[56:59], s94 offen offset:0 lds\n"
"v_cvt_pk_bf16_f32 v106, v36, v37\n"
"v_mfma_f32_16x16x32_bf16 v[24:27], a[152:155], a[72:75], v[24:27]\n"
"v_exp_f32 v16, v16\n"
"v_cvt_pk_bf16_f32 v107, v38, v39\n"
"v_mfma_f32_16x16x32_bf16 v[24:27], a[156:159], a[76:79], v[24:27]\n"
"v_exp_f32 v17, v17\n"
"v_lshl_add_u32 v233, v229, 2, v233\n"
"v_mfma_f32_16x16x32_bf16 v[24:27], a[160:163], a[80:83], v[24:27]\n"
"v_exp_f32 v18, v18\n"
"v_mfma_f32_16x16x32_bf16 v[28:31], a[176:179], a[80:83], 0\n"
"ds_read_b64_tr_b16 a[202:203], v9 offset:26624\n"
"v_lshl_add_u32 v234, v229, 2, v228\n"
"v_mfma_f32_16x16x32_bf16 v[28:31], a[172:175], a[76:79], v[28:31]\n"
"ds_read_b128 a[184:187], v5 offset:36864\n"
"v_exp_f32 v19, v19\n"
"v_mfma_f32_16x16x32_bf16 v[28:31], a[168:171], a[72:75], v[28:31]\n"
"v_exp_f32 v20, v20\n"
"v_cvt_pk_bf16_f32 v108, v16, v17\n"
"v_mfma_f32_16x16x32_bf16 v[28:31], a[164:167], a[68:71], v[28:31]\n"
"s_mov_b32 s95, 50304\n"
"v_exp_f32 v21, v21\n"
"v_accvgpr_write_b32 a142, v254\n"
"v_mfma_f32_16x16x32_bf16 a[32:35], a[204:207], v[104:107], a[32:35]\n"
"v_and_b32 v229, 0xe0, v0\n"
"v_cvt_pk_bf16_f32 v109, v18, v19\n"
"v_mfma_f32_16x16x32_bf16 a[48:51], a[208:211], v[104:107], a[48:51]\n"
"buffer_load_dwordx4 v3, s[56:59], s95 offen offset:1024 lds\n"
"v_exp_f32 v22, v22\n"
"v_mfma_f32_16x16x32_bf16 a[16:19], a[0:3], v[104:107], a[16:19]\n"
"v_exp_f32 v23, v23\n"
"v_cvt_pk_bf16_f32 v110, v20, v21\n"
"v_mfma_f32_16x16x32_bf16 v[32:35], a[148:151], a[84:87], 0\n"
"ds_read_b128 a[188:191], v6 offset:36864\n"
"v_lshlrev_b32 v229, 22, v229\n"
"v_mfma_f32_16x16x32_bf16 v[32:35], a[152:155], a[88:91], v[32:35]\n"
"v_exp_f32 v24, v24\n"
"v_cvt_pk_bf16_f32 v111, v22, v23\n"
"v_mfma_f32_16x16x32_bf16 v[32:35], a[156:159], a[92:95], v[32:35]\n"
"v_exp_f32 v25, v25\n"
"v_add3_u32 v234, v234, v229, s39\n"
"v_mfma_f32_16x16x32_bf16 v[32:35], a[160:163], a[96:99], v[32:35]\n"
"s_sub_u32 s8, s43, 65536\n"
"v_exp_f32 v26, v26\n"
"v_mfma_f32_16x16x32_bf16 v[36:39], a[176:179], a[96:99], 0\n"
"v_exp_f32 v27, v27\n"
"v_mfma_f32_16x16x32_bf16 v[36:39], a[172:175], a[92:95], v[36:39]\n"
"s_add_u32 m0, s42, 32768\n"
"ds_read_b128 a[192:195], v7 offset:36864\n"
"v_accvgpr_write_b32 a143, v255\n"
"v_mfma_f32_16x16x32_bf16 v[36:39], a[168:171], a[88:91], v[36:39]\n"
"s_mov_b32 s96, 65664\n"
"v_exp_f32 v28, v28\n"
"v_cvt_pk_bf16_f32 v112, v24, v25\n"
"v_mfma_f32_16x16x32_bf16 v[36:39], a[164:167], a[84:87], v[36:39]\n"
"s_waitcnt lgkmcnt(2)\n"
"buffer_load_dwordx4 v3, s[56:59], s96 offen offset:0 lds\n"
"v_exp_f32 v29, v29\n"
"v_mfma_f32_16x16x32_bf16 a[20:23], a[196:199], v[108:111], a[20:23]\n"
"v_lshlrev_b32 v193, 3, v0\n"
"v_cvt_pk_bf16_f32 v113, v26, v27\n"
"v_mfma_f32_16x16x32_bf16 a[36:39], a[200:203], v[108:111], a[36:39]\n"
"v_exp_f32 v30, v30\n"
"v_add_u32 v188, s8, v8\n"
"v_mfma_f32_16x16x32_bf16 a[4:7], a[0:3], v[108:111], a[4:7]\n"
"v_exp_f32 v31, v31\n"
"v_and_b32 v193, 0x780, v193\n"
"v_mfma_f32_16x16x32_bf16 v[16:19], a[148:151], a[100:103], 0\n"
"s_lshl_b32 s24, s91, 5\n"
"v_cvt_pk_bf16_f32 v114, v28, v29\n"
"v_add_u32 v189, s8, v9\n"
"v_mfma_f32_16x16x32_bf16 v[16:19], a[152:155], a[104:107], v[16:19]\n"
"s_add_u32 s24, s24, s38\n"
"v_exp_f32 v32, v32\n"
"v_cvt_pk_bf16_f32 v115, v30, v31\n"
"v_mfma_f32_16x16x32_bf16 v[16:19], a[156:159], a[108:111], v[16:19]\n"
"s_lshl_b32 s24, s24, 9\n"
"ds_read_b128 a[148:151], v7 offset:38912\n"
"v_exp_f32 v33, v33\n"
"v_mfma_f32_16x16x32_bf16 v[16:19], a[160:163], a[112:115], v[16:19]\n"
"s_mov_b32 s94, 66688\n"
"v_exp_f32 v34, v34\n"
"v_mfma_f32_16x16x32_bf16 v[20:23], a[176:179], a[112:115], 0\n"
"s_add_u32 s24, s24, 131072\n"
"ds_read_b128 a[152:155], v6 offset:38912\n"
"v_add_u32 v190, s8, v10\n"
"v_mfma_f32_16x16x32_bf16 v[20:23], a[172:175], a[108:111], v[20:23]\n"
"ds_read_b64_tr_b16 a[204:205], v8 offset:28672\n"
"v_exp_f32 v35, v35\n"
"v_mfma_f32_16x16x32_bf16 v[20:23], a[168:171], a[104:107], v[20:23]\n"
"ds_read_b128 a[156:159], v5 offset:38912\n"
"v_exp_f32 v36, v36\n"
"v_mfma_f32_16x16x32_bf16 v[20:23], a[164:167], a[100:103], v[20:23]\n"
"buffer_load_dwordx4 v3, s[56:59], s94 offen offset:1024 lds\n"
"v_exp_f32 v37, v37\n"
"v_mfma_f32_16x16x32_bf16 a[24:27], a[196:199], v[112:115], a[24:27]\n"
"v_cvt_pk_bf16_f32 v116, v32, v33\n"
"v_add_u32 v191, s8, v11\n"
"v_mfma_f32_16x16x32_bf16 a[40:43], a[200:203], v[112:115], a[40:43]\n"
"ds_read_b128 a[160:163], v4 offset:38912\n"
"v_exp_f32 v38, v38\n"
"v_mfma_f32_16x16x32_bf16 a[8:11], a[0:3], v[112:115], a[8:11]\n"
"v_cvt_pk_bf16_f32 v117, v34, v35\n"
"v_exp_f32 v39, v39\n"
"v_mfma_f32_16x16x32_bf16 v[24:27], a[180:183], a[52:55], 0\n"
"s_waitcnt lgkmcnt(5)\n"
"v_add_u32 v235, s39, v0\n"
"v_cvt_pk_bf16_f32 v118, v36, v37\n"
"v_mfma_f32_16x16x32_bf16 v[24:27], a[184:187], a[56:59], v[24:27]\n"
"s_mov_b32 s80, s39\n"
"buffer_load_dword v192, v193, s[64:67], s24 offen sc1\n"
"v_exp_f32 v16, v16\n"
"v_mfma_f32_16x16x32_bf16 v[24:27], a[188:191], a[60:63], v[24:27]\n"
"s_cmp_eq_u32 s39, 0\n"
"s_cselect_b64 s[82:83], -1, 0\n"
"ds_read_b64_tr_b16 a[206:207], v8 offset:30720\n"
"v_exp_f32 v17, v17\n"
"v_mfma_f32_16x16x32_bf16 v[24:27], a[192:195], a[64:67], v[24:27]\n"
"s_waitcnt lgkmcnt(4)\n"
"v_cvt_pk_bf16_f32 v119, v38, v39\n"
"v_cvt_pk_bf16_f32 v120, v16, v17\n"
"v_mfma_f32_16x16x32_bf16 v[28:31], a[148:151], a[64:67], 0\n"
"s_waitcnt vmcnt(8) lgkmcnt(1)\n"
"ds_read_b64_tr_b16 a[208:209], v9 offset:28672\n"
"v_exp_f32 v18, v18\n"
"v_mfma_f32_16x16x32_bf16 v[28:31], a[152:155], a[60:63], v[28:31]\n"
"s_barrier\n"
"v_exp_f32 v19, v19\n"
"v_mfma_f32_16x16x32_bf16 v[28:31], a[156:159], a[56:59], v[28:31]\n"
"v_exp_f32 v20, v20\n"
"v_cvt_pk_bf16_f32 v121, v18, v19\n"
"v_mfma_f32_16x16x32_bf16 v[28:31], a[160:163], a[52:55], v[28:31]\n"
"ds_read_b128 a[176:179], v4 offset:49152\n"
"v_exp_f32 v21, v21\n"
"v_mfma_f32_16x16x32_bf16 a[28:31], a[196:199], v[116:119], a[28:31]\n"
"s_add_u32 m0, s41, 131072\n"
"v_exp_f32 v22, v22\n"
"v_mfma_f32_16x16x32_bf16 a[44:47], a[200:203], v[116:119], a[44:47]\n"
"s_mov_b32 s94, 81920\n"
"v_exp_f32 v23, v23\n"
"v_mfma_f32_16x16x32_bf16 a[12:15], a[0:3], v[116:119], a[12:15]\n"
"buffer_load_dwordx4 v1, s[52:55], s94 offen offset:0 lds\n"
"v_cvt_pk_bf16_f32 v122, v20, v21\n"
"v_mfma_f32_16x16x32_bf16 v[32:35], a[180:183], a[68:71], 0\n"
"v_exp_f32 v24, v24\n"
"v_mfma_f32_16x16x32_bf16 v[32:35], a[184:187], a[72:75], v[32:35]\n"
"v_exp_f32 v25, v25\n"
"v_mfma_f32_16x16x32_bf16 v[32:35], a[188:191], a[76:79], v[32:35]\n"
"ds_read_b64_tr_b16 a[210:211], v9 offset:30720\n"
"v_cvt_pk_bf16_f32 v123, v22, v23\n"
"v_mfma_f32_16x16x32_bf16 v[32:35], a[192:195], a[80:83], v[32:35]\n"
"v_exp_f32 v26, v26\n"
"v_mfma_f32_16x16x32_bf16 v[36:39], a[148:151], a[80:83], 0\n"
"v_exp_f32 v27, v27\n"
"v_mfma_f32_16x16x32_bf16 v[36:39], a[152:155], a[76:79], v[36:39]\n"
"ds_read_b128 a[172:175], v5 offset:49152\n"
"v_cvt_pk_bf16_f32 v124, v24, v25\n"
"v_mfma_f32_16x16x32_bf16 v[36:39], a[156:159], a[72:75], v[36:39]\n"
"ds_read_b64_tr_b16 v[212:213], v188 offset:0\n"
"v_exp_f32 v28, v28\n"
"v_mfma_f32_16x16x32_bf16 v[36:39], a[160:163], a[68:71], v[36:39]\n"
"s_mov_b32 s95, 82944\n"
"v_exp_f32 v29, v29\n"
"v_cvt_pk_bf16_f32 v125, v26, v27\n"
"v_mfma_f32_16x16x32_bf16 a[32:35], a[196:199], v[120:123], a[32:35]\n"
"v_exp_f32 v30, v30\n"
"v_mfma_f32_16x16x32_bf16 a[48:51], a[200:203], v[120:123], a[48:51]\n"
"v_exp_f32 v31, v31\n"
"v_mfma_f32_16x16x32_bf16 a[16:19], a[0:3], v[120:123], a[16:19]\n"
"buffer_load_dwordx4 v2, s[52:55], s95 offen offset:1024 lds\n"
"v_cvt_pk_bf16_f32 v126, v28, v29\n"
"v_mfma_f32_16x16x32_bf16 v[16:19], a[180:183], a[84:87], 0\n"
"ds_read_b128 a[168:171], v6 offset:49152\n"
"v_cvt_pk_bf16_f32 v127, v30, v31\n"
"v_mfma_f32_16x16x32_bf16 v[16:19], a[184:187], a[88:91], v[16:19]\n"
"ds_read_b64_tr_b16 v[214:215], v188 offset:2048\n"
"v_exp_f32 v32, v32\n"
"v_mfma_f32_16x16x32_bf16 v[16:19], a[188:191], a[92:95], v[16:19]\n"
"ds_read_b128 a[164:167], v7 offset:49152\n"
"v_exp_f32 v33, v33\n"
"v_mfma_f32_16x16x32_bf16 v[16:19], a[192:195], a[96:99], v[16:19]\n"
"v_exp_f32 v34, v34\n"
"v_mfma_f32_16x16x32_bf16 v[20:23], a[148:151], a[96:99], 0\n"
"v_exp_f32 v35, v35\n"
"v_mfma_f32_16x16x32_bf16 v[20:23], a[152:155], a[92:95], v[20:23]\n"
"v_exp_f32 v36, v36\n"
"v_mfma_f32_16x16x32_bf16 v[20:23], a[156:159], a[88:91], v[20:23]\n"
"s_mov_b32 s96, 83968\n"
"v_exp_f32 v37, v37\n"
"v_mfma_f32_16x16x32_bf16 v[20:23], a[160:163], a[84:87], v[20:23]\n"
"s_waitcnt lgkmcnt(3)\n"
"ds_read_b64_tr_b16 v[216:217], v189 offset:0\n"
"v_cvt_pk_bf16_f32 v128, v32, v33\n"
"v_mfma_f32_16x16x32_bf16 a[20:23], a[204:207], v[124:127], a[20:23]\n"
"v_exp_f32 v38, v38\n"
"v_mfma_f32_16x16x32_bf16 a[36:39], a[208:211], v[124:127], a[36:39]\n"
"buffer_load_dwordx4 v1, s[52:55], s96 offen offset:2048 lds\n"
"v_cvt_pk_bf16_f32 v129, v34, v35\n"
"v_mfma_f32_16x16x32_bf16 a[4:7], a[0:3], v[124:127], a[4:7]\n"
"v_exp_f32 v39, v39\n"
"v_cvt_pk_bf16_f32 v130, v36, v37\n"
"v_mfma_f32_16x16x32_bf16 v[24:27], a[180:183], a[100:103], 0\n"
"v_exp_f32 v16, v16\n"
"v_mfma_f32_16x16x32_bf16 v[24:27], a[184:187], a[104:107], v[24:27]\n"
"v_exp_f32 v17, v17\n"
"v_mfma_f32_16x16x32_bf16 v[24:27], a[188:191], a[108:111], v[24:27]\n"
"ds_read_b128 a[180:183], v7 offset:51200\n"
"v_cvt_pk_bf16_f32 v131, v38, v39\n"
"v_mfma_f32_16x16x32_bf16 v[24:27], a[192:195], a[112:115], v[24:27]\n"
"ds_read_b64_tr_b16 v[218:219], v189 offset:2048\n"
"v_cvt_pk_bf16_f32 v132, v16, v17\n"
"v_mfma_f32_16x16x32_bf16 v[28:31], a[148:151], a[112:115], 0\n"
"ds_read_b128 a[184:187], v6 offset:51200\n"
"v_exp_f32 v18, v18\n"
"v_mfma_f32_16x16x32_bf16 v[28:31], a[152:155], a[108:111], v[28:31]\n"
"s_mov_b32 s94, 84992\n"
"ds_read_b64_tr_b16 a[196:197], v8 offset:32768\n"
"v_exp_f32 v19, v19\n"
"v_mfma_f32_16x16x32_bf16 v[28:31], a[156:159], a[104:107], v[28:31]\n"
"ds_read_b128 a[188:191], v5 offset:51200\n"
"v_exp_f32 v20, v20\n"
"v_mfma_f32_16x16x32_bf16 v[28:31], a[160:163], a[100:103], v[28:31]\n"
"ds_read_b128 a[192:195], v4 offset:51200\n"
"v_exp_f32 v21, v21\n"
"v_mfma_f32_16x16x32_bf16 a[24:27], a[204:207], v[128:131], a[24:27]\n"
"buffer_load_dwordx4 v2, s[52:55], s94 offen offset:3072 lds\n"
"ds_read_b64_tr_b16 v[220:221], v190 offset:0\n"
"v_mfma_f32_16x16x32_bf16 a[40:43], a[208:211], v[128:131], a[40:43]\n"
"v_exp_f32 v22, v22\n"
"v_cvt_pk_bf16_f32 v133, v18, v19\n"
"v_mfma_f32_16x16x32_bf16 a[8:11], a[0:3], v[128:131], a[8:11]\n"
"v_exp_f32 v23, v23\n"
"v_cvt_pk_bf16_f32 v134, v20, v21\n"
"v_mfma_f32_16x16x32_bf16 v[32:35], a[176:179], a[52:55], 0\n"
"v_exp_f32 v24, v24\n"
"v_mfma_f32_16x16x32_bf16 v[32:35], a[172:175], a[56:59], v[32:35]\n"
"s_waitcnt lgkmcnt(7)\n"
"ds_read_b64_tr_b16 a[198:199], v8 offset:34816\n"
"v_cvt_pk_bf16_f32 v135, v22, v23\n"
"v_mfma_f32_16x16x32_bf16 v[32:35], a[168:171], a[60:63], v[32:35]\n"
"ds_read_b64_tr_b16 v[222:223], v190 offset:2048\n"
"v_exp_f32 v25, v25\n"
"v_mfma_f32_16x16x32_bf16 v[32:35], a[164:167], a[64:67], v[32:35]\n"
"s_waitcnt lgkmcnt(7)\n"
"v_exp_f32 v26, v26\n"
"v_mfma_f32_16x16x32_bf16 v[36:39], a[180:183], a[64:67], 0\n"
"s_waitcnt lgkmcnt(3)\n"
"v_exp_f32 v27, v27\n"
"v_mfma_f32_16x16x32_bf16 v[36:39], a[184:187], a[60:63], v[36:39]\n"
"s_add_u32 m0, s42, 40960\n"
"v_exp_f32 v28, v28\n"
"v_mfma_f32_16x16x32_bf16 v[36:39], a[188:191], a[56:59], v[36:39]\n"
"s_mov_b32 s95, 82048\n"
"v_exp_f32 v29, v29\n"
"v_mfma_f32_16x16x32_bf16 v[36:39], a[192:195], a[52:55], v[36:39]\n"
"ds_read_b64_tr_b16 a[200:201], v9 offset:32768\n"
"v_cvt_pk_bf16_f32 v136, v24, v25\n"
"v_mfma_f32_16x16x32_bf16 a[28:31], a[204:207], v[132:135], a[28:31]\n"
"buffer_load_dwordx4 v3, s[56:59], s95 offen offset:0 lds\n"
"ds_read_b64_tr_b16 v[224:225], v191 offset:0\n"
"v_mfma_f32_16x16x32_bf16 a[44:47], a[208:211], v[132:135], a[44:47]\n"
"v_exp_f32 v30, v30\n"
"v_cvt_pk_bf16_f32 v137, v26, v27\n"
"v_mfma_f32_16x16x32_bf16 a[12:15], a[0:3], v[132:135], a[12:15]\n"
"ds_read_b128 a[148:151], v4 offset:53248\n"
"v_exp_f32 v31, v31\n"
"v_mfma_f32_16x16x32_bf16 v[16:19], a[176:179], a[68:71], 0\n"
"v_cvt_pk_bf16_f32 v138, v28, v29\n"
"v_cvt_pk_bf16_f32 v139, v30, v31\n"
"v_mfma_f32_16x16x32_bf16 v[16:19], a[172:175], a[72:75], v[16:19]\n"
"ds_read_b64_tr_b16 v[226:227], v191 offset:2048\n"
"v_exp_f32 v32, v32\n"
"v_mfma_f32_16x16x32_bf16 v[16:19], a[168:171], a[76:79], v[16:19]\n"
"ds_read_b64_tr_b16 a[202:203], v9 offset:34816\n"
"v_exp_f32 v33, v33\n"
"v_mfma_f32_16x16x32_bf16 v[16:19], a[164:167], a[80:83], v[16:19]\n"
"v_exp_f32 v34, v34\n"
"v_mfma_f32_16x16x32_bf16 v[20:23], a[180:183], a[80:83], 0\n"
"s_mov_b32 s96, 83072\n"
"v_exp_f32 v35, v35\n"
"v_mfma_f32_16x16x32_bf16 v[20:23], a[184:187], a[76:79], v[20:23]\n"
"ds_read_b128 a[152:155], v5 offset:53248\n"
"v_cvt_pk_bf16_f32 v140, v32, v33\n"
"v_mfma_f32_16x16x32_bf16 v[20:23], a[188:191], a[72:75], v[20:23]\n"
"buffer_load_dwordx4 v3, s[56:59], s96 offen offset:1024 lds\n"
"v_exp_f32 v36, v36\n"
"v_mfma_f32_16x16x32_bf16 v[20:23], a[192:195], a[68:71], v[20:23]\n"
"v_exp_f32 v37, v37\n"
"v_cvt_pk_bf16_f32 v141, v34, v35\n"
"v_mfma_f32_16x16x32_bf16 a[32:35], a[204:207], v[136:139], a[32:35]\n"
"v_exp_f32 v38, v38\n"
"v_mfma_f32_16x16x32_bf16 a[48:51], a[208:211], v[136:139], a[48:51]\n"
"v_exp_f32 v39, v39\n"
"v_mfma_f32_16x16x32_bf16 a[16:19], a[0:3], v[136:139], a[16:19]\n"
"ds_read_b128 a[156:159], v6 offset:53248\n"
"v_cvt_pk_bf16_f32 v142, v36, v37\n"
"v_mfma_f32_16x16x32_bf16 v[24:27], a[176:179], a[84:87], 0\n"
"buffer_load_dwordx4 v[236:239], v235, s[64:67], s37 offen offset:0 sc1\n"
"v_cvt_pk_bf16_f32 v143, v38, v39\n"
"v_mfma_f32_16x16x32_bf16 v[24:27], a[172:175], a[88:91], v[24:27]\n"
"ds_read_b128 a[160:163], v7 offset:53248\n"
"v_exp_f32 v16, v16\n"
"v_mfma_f32_16x16x32_bf16 v[24:27], a[168:171], a[92:95], v[24:27]\n"
"ds_read_b64_tr_b16 a[204:205], v8 offset:36864\n"
"v_exp_f32 v17, v17\n"
"v_mfma_f32_16x16x32_bf16 v[24:27], a[164:167], a[96:99], v[24:27]\n"
"s_add_u32 m0, s42, 49152\n"
"v_exp_f32 v18, v18\n"
"v_mfma_f32_16x16x32_bf16 v[28:31], a[180:183], a[96:99], 0\n"
"s_mov_b32 s94, 98432\n"
"v_exp_f32 v19, v19\n"
"v_mfma_f32_16x16x32_bf16 v[28:31], a[184:187], a[92:95], v[28:31]\n"
"v_exp_f32 v20, v20\n"
"v_mfma_f32_16x16x32_bf16 v[28:31], a[188:191], a[88:91], v[28:31]\n"
"buffer_load_dwordx4 v3, s[56:59], s94 offen offset:0 lds\n"
"v_cvt_pk_bf16_f32 v144, v16, v17\n"
"v_mfma_f32_16x16x32_bf16 v[28:31], a[192:195], a[84:87], v[28:31]\n"
"s_waitcnt lgkmcnt(3)\n"
"buffer_load_dwordx4 v[240:243], v235, s[64:67], s37 offen offset:1024 sc1\n"
"v_exp_f32 v21, v21\n"
"v_mfma_f32_16x16x32_bf16 a[20:23], a[196:199], v[140:143], a[20:23]\n"
".Lpg6:\n"
"s_cbranch_execz .Lpg10\n"
"v_exp_f32 v22, v22\n"
"v_mfma_f32_16x16x32_bf16 a[36:39], a[200:203], v[140:143], a[36:39]\n"
"v_cvt_pk_bf16_f32 v145, v18, v19\n"
"v_cvt_pk_bf16_f32 v146, v20, v21\n"
"v_mfma_f32_16x16x32_bf16 a[4:7], a[0:3], v[140:143], a[4:7]\n"
"ds_read_b64_tr_b16 a[206:207], v8 offset:38912\n"
"v_exp_f32 v23, v23\n"
"v_mfma_f32_16x16x32_bf16 v[32:35], a[176:179], a[100:103], 0\n"
"v_exp_f32 v24, v24\n"
"v_mfma_f32_16x16x32_bf16 v[32:35], a[172:175], a[104:107], v[32:35]\n"
"v_exp_f32 v25, v25\n"
"v_mfma_f32_16x16x32_bf16 v[32:35], a[168:171], a[108:111], v[32:35]\n"
"s_mov_b32 s95, 99456\n"
"buffer_load_dwordx4 v[244:247], v235, s[64:67], s37 offen offset:2048 sc1\n"
"v_cvt_pk_bf16_f32 v147, v22, v23\n"
"v_mfma_f32_16x16x32_bf16 v[32:35], a[164:167], a[112:115], v[32:35]\n"
"ds_read_b128 a[176:179], v7 offset:55296\n"
"buffer_load_dwordx4 v3, s[56:59], s95 offen offset:1024 lds\n"
"v_mfma_f32_16x16x32_bf16 v[36:39], a[180:183], a[112:115], 0\n"
"ds_read_b128 a[172:175], v6 offset:55296\n"
"v_exp_f32 v26, v26\n"
"v_mfma_f32_16x16x32_bf16 v[36:39], a[184:187], a[108:111], v[36:39]\n"
"ds_read_b128 a[168:171], v5 offset:55296\n"
"v_exp_f32 v27, v27\n"
"v_mfma_f32_16x16x32_bf16 v[36:39], a[188:191], a[104:107], v[36:39]\n"
"v_exp_f32 v28, v28\n"
"v_cvt_pk_bf16_f32 v148, v24, v25\n"
"v_mfma_f32_16x16x32_bf16 v[36:39], a[192:195], a[100:103], v[36:39]\n"
"ds_read_b128 a[164:167], v4 offset:55296\n"
"v_exp_f32 v29, v29\n"
"v_mfma_f32_16x16x32_bf16 a[24:27], a[196:199], v[144:147], a[24:27]\n"
"buffer_load_dwordx4 v[248:251], v235, s[64:67], s37 offen offset:3072 sc1\n"
"v_cvt_pk_bf16_f32 v149, v26, v27\n"
"v_mfma_f32_16x16x32_bf16 a[40:43], a[200:203], v[144:147], a[40:43]\n"
"v_exp_f32 v30, v30\n"
"v_cvt_pk_bf16_f32 v150, v28, v29\n"
"v_mfma_f32_16x16x32_bf16 a[8:11], a[0:3], v[144:147], a[8:11]\n"
"ds_read_b64_tr_b16 a[208:209], v9 offset:36864\n"
"v_exp_f32 v31, v31\n"
"v_mfma_f32_16x16x32_bf16 v[16:19], a[148:151], a[52:55], 0\n"
"v_exp_f32 v32, v32\n"
"v_mfma_f32_16x16x32_bf16 v[16:19], a[152:155], a[56:59], v[16:19]\n"
"s_waitcnt lgkmcnt(5)\n"
"v_exp_f32 v33, v33\n"
"v_mfma_f32_16x16x32_bf16 v[16:19], a[156:159], a[60:63], v[16:19]\n"
"s_waitcnt vmcnt(8) lgkmcnt(1)\n"
"v_cvt_pk_bf16_f32 v151, v30, v31\n"
"v_cvt_pk_bf16_f32 v152, v32, v33\n"
"v_mfma_f32_16x16x32_bf16 v[16:19], a[160:163], a[64:67], v[16:19]\n"
"s_barrier\n"
"v_exp_f32 v34, v34\n"
"v_mfma_f32_16x16x32_bf16 v[20:23], a[176:179], a[64:67], 0\n"
"v_exp_f32 v35, v35\n"
"v_mfma_f32_16x16x32_bf16 v[20:23], a[172:175], a[60:63], v[20:23]\n"
"v_exp_f32 v36, v36\n"
"v_mfma_f32_16x16x32_bf16 v[20:23], a[168:171], a[56:59], v[20:23]\n"
"v_exp_f32 v37, v37\n"
"v_mfma_f32_16x16x32_bf16 v[20:23], a[164:167], a[52:55], v[20:23]\n"
"v_exp_f32 v38, v38\n"
"v_mfma_f32_16x16x32_bf16 a[28:31], a[196:199], v[148:151], a[28:31]\n"
"s_add_u32 m0, s41, 147456\n"
"ds_read_b128 a[180:183], v4 offset:32768\n"
"v_cvt_pk_bf16_f32 v153, v34, v35\n"
"v_mfma_f32_16x16x32_bf16 a[44:47], a[200:203], v[148:151], a[44:47]\n"
"s_mov_b32 s95, 98304\n"
"v_exp_f32 v39, v39\n"
"v_mfma_f32_16x16x32_bf16 a[12:15], a[0:3], v[148:151], a[12:15]\n"
"buffer_load_dwordx4 v1, s[52:55], s95 offen offset:0 lds\n"
"v_cvt_pk_bf16_f32 v154, v36, v37\n"
"v_mfma_f32_16x16x32_bf16 v[24:27], a[148:151], a[68:71], 0\n"
"v_exp_f32 v16, v16\n"
"v_mfma_f32_16x16x32_bf16 v[24:27], a[152:155], a[72:75], v[24:27]\n"
"v_exp_f32 v17, v17\n"
"v_mfma_f32_16x16x32_bf16 v[24:27], a[156:159], a[76:79], v[24:27]\n"
"v_cvt_pk_bf16_f32 v155, v38, v39\n"
"v_cmp_ne_u32 vcc, s92, v192\n"
"v_mfma_f32_16x16x32_bf16 v[24:27], a[160:163], a[80:83], v[24:27]\n"
"v_exp_f32 v18, v18\n"
"v_mfma_f32_16x16x32_bf16 v[28:31], a[176:179], a[80:83], 0\n"
"s_cbranch_vccnz .Lrt1\n"
".Lbk1:\n"
"v_mfma_f32_16x16x32_bf16 v[28:31], a[172:175], a[76:79], v[28:31]\n"
"s_mov_b32 s96, 99328\n"
"ds_read_b64_tr_b16 a[210:211], v9 offset:38912\n"
"v_exp_f32 v19, v19\n"
"v_mfma_f32_16x16x32_bf16 v[28:31], a[168:171], a[72:75], v[28:31]\n"
"ds_read_b128 a[184:187], v5 offset:32768\n"
"v_exp_f32 v20, v20\n"
"v_mfma_f32_16x16x32_bf16 v[28:31], a[164:167], a[68:71], v[28:31]\n"
"v_exp_f32 v21, v21\n"
"v_cvt_pk_bf16_f32 v156, v16, v17\n"
"v_mfma_f32_16x16x32_bf16 a[32:35], a[196:199], v[152:155], a[32:35]\n"
"buffer_load_dwordx4 v2, s[52:55], s96 offen offset:1024 lds\n"
"v_cvt_pk_bf16_f32 v157, v18, v19\n"
"v_mfma_f32_16x16x32_bf16 a[48:51], a[200:203], v[152:155], a[48:51]\n"
"v_exp_f32 v22, v22\n"
"v_cvt_pk_bf16_f32 v158, v20, v21\n"
"v_mfma_f32_16x16x32_bf16 a[16:19], a[0:3], v[152:155], a[16:19]\n"
"ds_read_b128 a[188:191], v6 offset:32768\n"
"v_exp_f32 v23, v23\n"
"v_mfma_f32_16x16x32_bf16 v[32:35], a[148:151], a[84:87], 0\n"
"s_mov_b32 s94, 100352\n"
"v_exp_f32 v24, v24\n"
"v_mfma_f32_16x16x32_bf16 v[32:35], a[152:155], a[88:91], v[32:35]\n"
"v_exp_f32 v25, v25\n"
"v_mfma_f32_16x16x32_bf16 v[32:35], a[156:159], a[92:95], v[32:35]\n"
"buffer_load_dwordx4 v1, s[52:55], s94 offen offset:2048 lds\n"
"v_cvt_pk_bf16_f32 v159, v22, v23\n"
"v_mfma_f32_16x16x32_bf16 v[32:35], a[160:163], a[96:99], v[32:35]\n"
"v_exp_f32 v26, v26\n"
"v_mfma_f32_16x16x32_bf16 v[36:39], a[176:179], a[96:99], 0\n"
"v_exp_f32 v27, v27\n"
"v_mfma_f32_16x16x32_bf16 v[36:39], a[172:175], a[92:95], v[36:39]\n"
"v_exp_f32 v28, v28\n"
"v_mfma_f32_16x16x32_bf16 v[36:39], a[168:171], a[88:91], v[36:39]\n"
"ds_read_b128 a[192:195], v7 offset:32768\n"
"v_cvt_pk_bf16_f32 v160, v24, v25\n"
"v_mfma_f32_16x16x32_bf16 v[36:39], a[164:167], a[84:87], v[36:39]\n"
"s_waitcnt lgkmcnt(2)\n"
"v_exp_f32 v29, v29\n"
"v_cvt_pk_bf16_f32 v161, v26, v27\n"
"v_mfma_f32_16x16x32_bf16 a[20:23], a[204:207], v[156:159], a[20:23]\n"
"s_mov_b32 s95, 101376\n"
"v_exp_f32 v30, v30\n"
"v_mfma_f32_16x16x32_bf16 a[36:39], a[208:211], v[156:159], a[36:39]\n"
"v_exp_f32 v31, v31\n"
"v_mfma_f32_16x16x32_bf16 a[4:7], a[0:3], v[156:159], a[4:7]\n"
"buffer_load_dwordx4 v2, s[52:55], s95 offen offset:3072 lds\n"
"v_cvt_pk_bf16_f32 v162, v28, v29\n"
"v_mfma_f32_16x16x32_bf16 v[16:19], a[148:151], a[100:103], 0\n"
"v_exp_f32 v32, v32\n"
"v_mfma_f32_16x16x32_bf16 v[16:19], a[152:155], a[104:107], v[16:19]\n"
"v_exp_f32 v33, v33\n"
"v_mfma_f32_16x16x32_bf16 v[16:19], a[156:159], a[108:111], v[16:19]\n"
"ds_read_b128 a[148:151], v7 offset:34816\n"
"v_cvt_pk_bf16_f32 v163, v30, v31\n"
"v_mfma_f32_16x16x32_bf16 v[16:19], a[160:163], a[112:115], v[16:19]\n"
"v_exp_f32 v34, v34\n"
"v_mfma_f32_16x16x32_bf16 v[20:23], a[176:179], a[112:115], 0\n"
"s_add_u32 m0, s42, 114688\n"
"ds_read_b128 a[152:155], v6 offset:34816\n"
"v_cvt_pk_bf16_f32 v164, v32, v33\n"
"v_mfma_f32_16x16x32_bf16 v[20:23], a[172:175], a[108:111], v[20:23]\n"
"s_mov_b32 s96, 98304\n"
"ds_read_b64_tr_b16 a[196:197], v8 offset:40960\n"
"v_exp_f32 v35, v35\n"
"v_mfma_f32_16x16x32_bf16 v[20:23], a[168:171], a[104:107], v[20:23]\n"
"s_or_b32 s24, s16, s17\n"
"s_cselect_b32 s24, 2130706432, 0\n"
"ds_read_b128 a[156:159], v5 offset:34816\n"
"v_exp_f32 v36, v36\n"
"v_mfma_f32_16x16x32_bf16 v[20:23], a[164:167], a[100:103], v[20:23]\n"
"s_add_u32 s25, s93, 1\n"
"buffer_load_dwordx4 v3, s[56:59], s96 offen offset:0 lds\n"
"v_exp_f32 v37, v37\n"
"v_mfma_f32_16x16x32_bf16 a[24:27], a[204:207], v[160:163], a[24:27]\n"
"ds_read_b128 a[160:163], v4 offset:34816\n"
"v_cvt_pk_bf16_f32 v165, v34, v35\n"
"v_mfma_f32_16x16x32_bf16 a[40:43], a[208:211], v[160:163], a[40:43]\n"
"v_exp_f32 v38, v38\n"
"v_cvt_pk_bf16_f32 v166, v36, v37\n"
"v_mfma_f32_16x16x32_bf16 a[8:11], a[0:3], v[160:163], a[8:11]\n"
"ds_read_b64_tr_b16 a[198:199], v8 offset:43008\n"
"v_exp_f32 v39, v39\n"
"v_mfma_f32_16x16x32_bf16 v[24:27], a[180:183], a[52:55], 0\n"
"s_waitcnt lgkmcnt(6)\n"
"v_exp_f32 v16, v16\n"
"v_mfma_f32_16x16x32_bf16 v[24:27], a[184:187], a[56:59], v[24:27]\n"
"s_mov_b32 s94, 99328\n"
"v_exp_f32 v17, v17\n"
"v_mfma_f32_16x16x32_bf16 v[24:27], a[188:191], a[60:63], v[24:27]\n"
"buffer_load_dwordx4 v3, s[56:59], s94 offen offset:1024 lds\n"
"v_cvt_pk_bf16_f32 v167, v38, v39\n"
"v_mfma_f32_16x16x32_bf16 v[24:27], a[192:195], a[64:67], v[24:27]\n"
"s_waitcnt lgkmcnt(5)\n"
"v_exp_f32 v18, v18\n"
"v_mfma_f32_16x16x32_bf16 v[28:31], a[148:151], a[64:67], 0\n"
"s_waitcnt lgkmcnt(4)\n"
"v_exp_f32 v19, v19\n"
"v_mfma_f32_16x16x32_bf16 v[28:31], a[152:155], a[60:63], v[28:31]\n"
"s_waitcnt lgkmcnt(2)\n"
"v_exp_f32 v20, v20\n"
"v_mfma_f32_16x16x32_bf16 v[28:31], a[156:159], a[56:59], v[28:31]\n"
"s_waitcnt lgkmcnt(1)\n"
"v_exp_f32 v21, v21\n"
"v_mfma_f32_16x16x32_bf16 v[28:31], a[160:163], a[52:55], v[28:31]\n"
"v_cvt_pk_bf16_f32 v168, v16, v17\n"
"v_or_b32 v193, s24, v14\n"
"v_mfma_f32_16x16x32_bf16 a[28:31], a[204:207], v[164:167], a[28:31]\n"
"s_add_u32 m0, s42, 122880\n"
"ds_read_b64_tr_b16 a[200:201], v9 offset:40960\n"
"v_cvt_pk_bf16_f32 v169, v18, v19\n"
"v_mfma_f32_16x16x32_bf16 a[44:47], a[208:211], v[164:167], a[44:47]\n"
"s_mov_b32 s95, 114688\n"
"ds_read_b128 a[176:179], v4 offset:36864\n"
"v_exp_f32 v22, v22\n"
"v_mfma_f32_16x16x32_bf16 a[12:15], a[0:3], v[164:167], a[12:15]\n"
"v_exp_f32 v23, v23\n"
"v_mov_b32 v192, s25\n"
"v_mfma_f32_16x16x32_bf16 v[32:35], a[180:183], a[68:71], 0\n"
"buffer_load_dwordx4 v3, s[56:59], s95 offen offset:0 lds\n"
"v_cvt_pk_bf16_f32 v170, v20, v21\n"
"v_mfma_f32_16x16x32_bf16 v[32:35], a[184:187], a[72:75], v[32:35]\n"
"v_exp_f32 v24, v24\n"
"v_cvt_pk_bf16_f32 v171, v22, v23\n"
"v_mfma_f32_16x16x32_bf16 v[32:35], a[188:191], a[76:79], v[32:35]\n"
"buffer_store_dword v192, v193, s[64:67], 0 offen sc1\n"
"v_exp_f32 v25, v25\n"
"v_mfma_f32_16x16x32_bf16 v[32:35], a[192:195], a[80:83], v[32:35]\n"
"v_exp_f32 v26, v26\n"
"v_mfma_f32_16x16x32_bf16 v[36:39], a[148:151], a[80:83], 0\n"
"ds_read_b64_tr_b16 a[202:203], v9 offset:43008\n"
"v_cvt_pk_bf16_f32 v172, v24, v25\n"
"v_mfma_f32_16x16x32_bf16 v[36:39], a[152:155], a[76:79], v[36:39]\n"
"s_mov_b32 s96, 115712\n"
"ds_read_b128 a[172:175], v5 offset:36864\n"
"v_exp_f32 v27, v27\n"
"v_mfma_f32_16x16x32_bf16 v[36:39], a[156:159], a[72:75], v[36:39]\n"
"buffer_load_dwordx4 v3, s[56:59], s96 offen offset:1024 lds\n"
"v_exp_f32 v28, v28\n"
"v_mfma_f32_16x16x32_bf16 v[36:39], a[160:163], a[68:71], v[36:39]\n"
"v_exp_f32 v29, v29\n"
"v_cvt_pk_bf16_f32 v173, v26, v27\n"
"v_mfma_f32_16x16x32_bf16 a[32:35], a[204:207], v[168:171], a[32:35]\n"
"v_exp_f32 v30, v30\n"
"v_mfma_f32_16x16x32_bf16 a[48:51], a[208:211], v[168:171], a[48:51]\n"
"v_exp_f32 v31, v31\n"
"v_mfma_f32_16x16x32_bf16 a[16:19], a[0:3], v[168:171], a[16:19]\n"
"ds_read_b128 a[168:171], v6 offset:36864\n"
"v_cvt_pk_bf16_f32 v174, v28, v29\n"
"v_mfma_f32_16x16x32_bf16 v[16:19], a[180:183], a[84:87], 0\n"
"v_exp_f32 v32, v32\n"
"v_mfma_f32_16x16x32_bf16 v[16:19], a[184:187], a[88:91], v[16:19]\n"
"s_xor_b32 s98, s30, 0\n"
"v_exp_f32 v33, v33\n"
"v_mfma_f32_16x16x32_bf16 v[16:19], a[188:191], a[92:95], v[16:19]\n"
"buffer_load_dwordx4 a[212:215], v0, s[64:67], s98 offen offset:0 sc1\n"
"v_cvt_pk_bf16_f32 v175, v30, v31\n"
"v_mfma_f32_16x16x32_bf16 v[16:19], a[192:195], a[96:99], v[16:19]\n"
"v_exp_f32 v34, v34\n"
"v_mfma_f32_16x16x32_bf16 v[20:23], a[148:151], a[96:99], 0\n"
"v_exp_f32 v35, v35\n"
"v_mfma_f32_16x16x32_bf16 v[20:23], a[152:155], a[92:95], v[20:23]\n"
"v_exp_f32 v36, v36\n"
"v_mfma_f32_16x16x32_bf16 v[20:23], a[156:159], a[88:91], v[20:23]\n"
"ds_read_b128 a[164:167], v7 offset:36864\n"
"v_cvt_pk_bf16_f32 v176, v32, v33\n"
"v_mfma_f32_16x16x32_bf16 v[20:23], a[160:163], a[84:87], v[20:23]\n"
"s_waitcnt lgkmcnt(2)\n"
"v_exp_f32 v37, v37\n"
"v_cvt_pk_bf16_f32 v177, v34, v35\n"
"v_mfma_f32_16x16x32_bf16 a[20:23], a[196:199], v[172:175], a[20:23]\n"
"v_exp_f32 v38, v38\n"
"v_mfma_f32_16x16x32_bf16 a[36:39], a[200:203], v[172:175], a[36:39]\n"
"v_exp_f32 v39, v39\n"
"v_mfma_f32_16x16x32_bf16 a[4:7], a[0:3], v[172:175], a[4:7]\n"
"buffer_load_dwordx4 a[216:219], v0, s[64:67], s98 offen offset:1024 sc1\n"
"v_cvt_pk_bf16_f32 v178, v36, v37\n"
"v_mfma_f32_16x16x32_bf16 v[24:27], a[180:183], a[100:103], 0\n"
"v_exp_f32 v16, v16\n"
"v_mfma_f32_16x16x32_bf16 v[24:27], a[184:187], a[104:107], v[24:27]\n"
"v_exp_f32 v17, v17\n"
"v_mfma_f32_16x16x32_bf16 v[24:27], a[188:191], a[108:111], v[24:27]\n"
"ds_read_b128 a[180:183], v7 offset:38912\n"
"v_cvt_pk_bf16_f32 v179, v38, v39\n"
"v_mfma_f32_16x16x32_bf16 v[24:27], a[192:195], a[112:115], v[24:27]\n"
"v_exp_f32 v18, v18\n"
"v_mfma_f32_16x16x32_bf16 v[28:31], a[148:151], a[112:115], 0\n"
"ds_read_b128 a[184:187], v6 offset:38912\n"
"v_cvt_pk_bf16_f32 v180, v16, v17\n"
"v_mfma_f32_16x16x32_bf16 v[28:31], a[152:155], a[108:111], v[28:31]\n"
"ds_read_b64_tr_b16 a[204:205], v8 offset:45056\n"
"v_exp_f32 v19, v19\n"
"v_mfma_f32_16x16x32_bf16 v[28:31], a[156:159], a[104:107], v[28:31]\n"
"ds_read_b128 a[188:191], v5 offset:38912\n"
"v_exp_f32 v20, v20\n"
"v_mfma_f32_16x16x32_bf16 v[28:31], a[160:163], a[100:103], v[28:31]\n"
"ds_read_b128 a[192:195], v4 offset:38912\n"
"v_exp_f32 v21, v21\n"
"v_mfma_f32_16x16x32_bf16 a[24:27], a[196:199], v[176:179], a[24:27]\n"
"buffer_load_dwordx4 a[220:223], v0, s[64:67], s98 offen offset:2048 sc1\n"
"v_cvt_pk_bf16_f32 v181, v18, v19\n"
"v_mfma_f32_16x16x32_bf16 a[40:43], a[200:203], v[176:179], a[40:43]\n"
"v_exp_f32 v22, v22\n"
"v_cvt_pk_bf16_f32 v182, v20, v21\n"
"v_mfma_f32_16x16x32_bf16 a[8:11], a[0:3], v[176:179], a[8:11]\n"
"ds_read_b64_tr_b16 a[206:207], v8 offset:47104\n"
"v_exp_f32 v23, v23\n"
"v_mfma_f32_16x16x32_bf16 v[32:35], a[176:179], a[52:55], 0\n"
"v_exp_f32 v24, v24\n"
"v_mfma_f32_16x16x32_bf16 v[32:35], a[172:175], a[56:59], v[32:35]\n"
"s_waitcnt lgkmcnt(6)\n"
"v_exp_f32 v25, v25\n"
"v_mfma_f32_16x16x32_bf16 v[32:35], a[168:171], a[60:63], v[32:35]\n"
"s_waitcnt vmcnt(6) lgkmcnt(1)\n"
"v_cvt_pk_bf16_f32 v183, v22, v23\n"
"v_cvt_pk_bf16_f32 v184, v24, v25\n"
"v_mfma_f32_16x16x32_bf16 v[32:35], a[164:167], a[64:67], v[32:35]\n"
"s_barrier\n"
"v_exp_f32 v26, v26\n"
"v_mfma_f32_16x16x32_bf16 v[36:39], a[180:183], a[64:67], 0\n"
"v_exp_f32 v27, v27\n"
"v_mfma_f32_16x16x32_bf16 v[36:39], a[184:187], a[60:63], v[36:39]\n"
"v_exp_f32 v28, v28\n"
"v_mfma_f32_16x16x32_bf16 v[36:39], a[188:191], a[56:59], v[36:39]\n"
"v_exp_f32 v29, v29\n"
"v_mfma_f32_16x16x32_bf16 v[36:39], a[192:195], a[52:55], v[36:39]\n"
"ds_read_b64_tr_b16 a[208:209], v9 offset:45056\n"
"v_cvt_pk_bf16_f32 v185, v26, v27\n"
"v_mfma_f32_16x16x32_bf16 a[28:31], a[196:199], v[180:183], a[28:31]\n"
"s_add_u32 m0, s41, 131072\n"
"v_exp_f32 v30, v30\n"
"v_mfma_f32_16x16x32_bf16 a[44:47], a[200:203], v[180:183], a[44:47]\n"
"s_mov_b32 s96, 114688\n"
"v_exp_f32 v31, v31\n"
"v_mfma_f32_16x16x32_bf16 a[12:15], a[0:3], v[180:183], a[12:15]\n"
"ds_read_b128 a[148:151], v4 offset:49152\n"
"v_xor_b32 v228, s92, v236\n"
"v_mfma_f32_16x16x32_bf16 v[16:19], a[176:179], a[68:71], 0\n"
"buffer_load_dwordx4 v1, s[52:55], s96 offen offset:0 lds\n"
"v_cvt_pk_bf16_f32 v186, v28, v29\n"
"v_mfma_f32_16x16x32_bf16 v[16:19], a[172:175], a[72:75], v[16:19]\n"
"v_bitop3_b32 v228, v228, v238, s92 bitop3:0xf6\n"
"v_exp_f32 v32, v32\n"
"v_mfma_f32_16x16x32_bf16 v[16:19], a[168:171], a[76:79], v[16:19]\n"
"v_bitop3_b32 v228, v228, v240, s92 bitop3:0xf6\n"
"v_exp_f32 v33, v33\n"
"v_mfma_f32_16x16x32_bf16 v[16:19], a[164:167], a[80:83], v[16:19]\n"
"v_cvt_pk_bf16_f32 v187, v30, v31\n"
"v_bitop3_b32 v228, v228, v242, s92 bitop3:0xf6\n"
"v_mfma_f32_16x16x32_bf16 v[20:23], a[180:183], a[80:83], 0\n"
"v_bitop3_b32 v228, v228, v244, s92 bitop3:0xf6\n"
"v_exp_f32 v34, v34\n"
"v_mfma_f32_16x16x32_bf16 v[20:23], a[184:187], a[76:79], v[20:23]\n"
"s_mov_b32 s94, 115712\n"
"ds_read_b64_tr_b16 a[210:211], v9 offset:47104\n"
"v_bitop3_b32 v228, v228, v246, s92 bitop3:0xf6\n"
"v_bitop3_b32 v228, v228, v248, s92 bitop3:0xf6\n"
"v_mfma_f32_16x16x32_bf16 v[20:23], a[188:191], a[72:75], v[20:23]\n"
"ds_read_b128 a[152:155], v5 offset:49152\n"
"v_exp_f32 v35, v35\n"
"v_mfma_f32_16x16x32_bf16 v[20:23], a[192:195], a[68:71], v[20:23]\n"
"v_exp_f32 v36, v36\n"
"v_bitop3_b32 v228, v228, v250, s92 bitop3:0xf6\n"
"v_mfma_f32_16x16x32_bf16 a[32:35], a[196:199], v[184:187], a[32:35]\n"
"v_exp_f32 v37, v37\n"
"v_cvt_pk_bf16_f32 v188, v32, v33\n"
"v_mfma_f32_16x16x32_bf16 a[48:51], a[200:203], v[184:187], a[48:51]\n"
"buffer_load_dwordx4 v2, s[52:55], s94 offen offset:1024 lds\n"
"v_cmp_ne_u32 s[28:29], 0, v228\n"
"v_cvt_pk_bf16_f32 v189, v34, v35\n"
"v_mfma_f32_16x16x32_bf16 a[16:19], a[0:3], v[184:187], a[16:19]\n"
"s_and_b64 s[28:29], s[28:29], s[82:83]\n"
"v_exp_f32 v38, v38\n"
"v_cvt_pk_bf16_f32 v190, v36, v37\n"
"v_mfma_f32_16x16x32_bf16 v[24:27], a[176:179], a[84:87], 0\n"
"s_cmp_lg_u64 s[28:29], 0\n"
"s_cbranch_scc1 .Lrt3\n.Lbk3:\n"
"ds_read_b128 a[156:159], v6 offset:49152\n"
"v_mfma_f32_16x16x32_bf16 v[24:27], a[172:175], a[88:91], v[24:27]\n"
"ds_read_b128 a[160:163], v7 offset:49152\n"
"v_exp_f32 v39, v39\n"
"v_mfma_f32_16x16x32_bf16 v[24:27], a[168:171], a[92:95], v[24:27]\n"
"v_exp_f32 v16, v16\n"
"v_cvt_pk_bf16_f32 v191, v38, v39\n"
"v_mfma_f32_16x16x32_bf16 v[24:27], a[164:167], a[96:99], v[24:27]\n"
"s_mov_b32 s95, 116736\n"
"ds_read_b64_tr_b16 a[196:197], v8 offset:49152\n"
"v_exp_f32 v17, v17\n"
"v_mfma_f32_16x16x32_bf16 v[28:31], a[180:183], a[96:99], 0\n"
"v_exp_f32 v18, v18\n"
"v_add_f32 v228, v237, v241\n"
"v_mfma_f32_16x16x32_bf16 v[28:31], a[184:187], a[92:95], v[28:31]\n"
"buffer_load_dwordx4 v1, s[52:55], s95 offen offset:2048 lds\n"
"v_exp_f32 v19, v19\n"
"v_mfma_f32_16x16x32_bf16 v[28:31], a[188:191], a[88:91], v[28:31]\n"
"v_exp_f32 v20, v20\n"
"v_add_f32 v229, v239, v243\n"
"v_mfma_f32_16x16x32_bf16 v[28:31], a[192:195], a[84:87], v[28:31]\n"
"s_waitcnt lgkmcnt(3)\n"
"v_exp_f32 v21, v21\n"
"v_cvt_pk_bf16_f32 v192, v16, v17\n"
"v_mfma_f32_16x16x32_bf16 a[20:23], a[204:207], v[188:191], a[20:23]\n"
"v_add_f32 v230, v245, v249\n"
"v_add_f32 v231, v247, v251\n"
"v_mfma_f32_16x16x32_bf16 a[36:39], a[208:211], v[188:191], a[36:39]\n"
"v_exp_f32 v22, v22\n"
"v_cvt_pk_bf16_f32 v193, v18, v19\n"
"v_mfma_f32_16x16x32_bf16 a[4:7], a[0:3], v[188:191], a[4:7]\n"
"v_exp_f32 v23, v23\n"
"v_add_f32 v228, v228, v230\n"
"v_mfma_f32_16x16x32_bf16 v[32:35], a[176:179], a[100:103], 0\n"
"s_mov_b32 s96, 117760\n"
"v_cvt_pk_bf16_f32 v194, v20, v21\n"
"v_add_f32 v229, v229, v231\n"
"v_mfma_f32_16x16x32_bf16 v[32:35], a[172:175], a[104:107], v[32:35]\n"
"v_exp_f32 v24, v24\n"
"v_permlane32_swap_b32 v228, v229\n"
"v_mfma_f32_16x16x32_bf16 v[32:35], a[168:171], a[108:111], v[32:35]\n"
"v_exp_f32 v25, v25\n"
"v_cvt_pk_bf16_f32 v195, v22, v23\n"
"v_mfma_f32_16x16x32_bf16 v[32:35], a[164:167], a[112:115], v[32:35]\n"
"ds_read_b128 a[176:179], v7 offset:51200\n"
"v_cvt_pk_bf16_f32 v229, v228, v229\n"
"v_mfma_f32_16x16x32_bf16 v[36:39], a[180:183], a[112:115], 0\n"
"buffer_load_dwordx4 v2, s[52:55], s96 offen offset:3072 lds\n"
"ds_read_b128 a[172:175], v6 offset:51200\n"
"v_lshlrev_b32 v228, 16, v229\n"
"v_mfma_f32_16x16x32_bf16 v[36:39], a[184:187], a[108:111], v[36:39]\n"
"v_exp_f32 v26, v26\n"
"v_and_b32 v229, 0xffff0000, v229\n"
"v_mfma_f32_16x16x32_bf16 v[36:39], a[188:191], a[104:107], v[36:39]\n"
"ds_read_b128 a[168:171], v5 offset:51200\n"
"v_exp_f32 v27, v27\n"
"v_mfma_f32_16x16x32_bf16 v[36:39], a[192:195], a[100:103], v[36:39]\n"
"v_exp_f32 v28, v28\n"
"v_mul_f32 v230, 0xbfb8aa3b, v228\n"
"v_mfma_f32_16x16x32_bf16 a[24:27], a[204:207], v[192:195], a[24:27]\n"
"v_exp_f32 v29, v29\n"
"v_cvt_pk_bf16_f32 v196, v24, v25\n"
"v_mfma_f32_16x16x32_bf16 a[40:43], a[208:211], v[192:195], a[40:43]\n"
"s_add_u32 m0, s42, 57344\n"
"ds_read_b128 a[164:167], v4 offset:51200\n"
"v_exp_f32 v230, v230\n"
"v_mfma_f32_16x16x32_bf16 a[8:11], a[0:3], v[192:195], a[8:11]\n"
"s_mov_b32 s94, 114816\n"
"v_add_f32 v230, 1.0, v230\n"
"v_exp_f32 v30, v30\n"
"v_mfma_f32_16x16x32_bf16 v[16:19], a[148:151], a[52:55], 0\n"
"v_cvt_pk_bf16_f32 v197, v26, v27\n"
"v_rcp_f32 v230, v230\n"
"v_mfma_f32_16x16x32_bf16 v[16:19], a[152:155], a[56:59], v[16:19]\n"
"s_waitcnt lgkmcnt(4)\n"
"v_exp_f32 v31, v31\n"
"v_cvt_pk_bf16_f32 v198, v28, v29\n"
"v_mfma_f32_16x16x32_bf16 v[16:19], a[156:159], a[60:63], v[16:19]\n"
"v_mul_f32 v228, v228, v230\n"
"v_exp_f32 v32, v32\n"
"v_mfma_f32_16x16x32_bf16 v[16:19], a[160:163], a[64:67], v[16:19]\n"
"s_waitcnt lgkmcnt(2)\n"
"buffer_load_dwordx4 v3, s[56:59], s94 offen offset:0 lds\n"
"v_cvt_pk_bf16_f32 v228, v228, v228\n"
"v_cvt_pk_bf16_f32 v199, v30, v31\n"
"v_mfma_f32_16x16x32_bf16 v[20:23], a[176:179], a[64:67], 0\n"
"v_exp_f32 v33, v33\n"
"v_lshlrev_b32 v228, 16, v228\n"
"v_mfma_f32_16x16x32_bf16 v[20:23], a[172:175], a[60:63], v[20:23]\n"
"s_waitcnt lgkmcnt(1)\n"
"v_mul_f32 v228, v228, v229\n"
"v_exp_f32 v34, v34\n"
"v_mfma_f32_16x16x32_bf16 v[20:23], a[168:171], a[56:59], v[20:23]\n"
"s_waitcnt lgkmcnt(0)\n"
"v_mov_b32 v229, v228\n"
"v_exp_f32 v35, v35\n"
"v_mfma_f32_16x16x32_bf16 v[20:23], a[164:167], a[52:55], v[20:23]\n"
"s_mov_b32 s95, 115840\n"
"ds_read_b64_tr_b16 a[198:199], v8 offset:51200\n"
"v_exp_f32 v36, v36\n"
"v_mfma_f32_16x16x32_bf16 a[28:31], a[204:207], v[196:199], a[28:31]\n"
"v_permlane32_swap_b32 v228, v229\n"
"v_exp_f32 v37, v37\n"
"v_mfma_f32_16x16x32_bf16 a[44:47], a[208:211], v[196:199], a[44:47]\n"
"v_cvt_pk_bf16_f32 v200, v32, v33\n"
"v_cvt_pk_bf16_f32 v232, v228, v229\n"
"v_cvt_pk_bf16_f32 v201, v34, v35\n"
"v_mfma_f32_16x16x32_bf16 a[12:15], a[0:3], v[196:199], a[12:15]\n"
"ds_bpermute_b32 v228, v233, v232 offset:0\n"
"v_exp_f32 v38, v38\n"
"v_mfma_f32_16x16x32_bf16 v[24:27], a[148:151], a[68:71], 0\n"
"ds_bpermute_b32 v229, v233, v232 offset:4\n"
"v_exp_f32 v39, v39\n"
"v_mfma_f32_16x16x32_bf16 v[24:27], a[152:155], a[72:75], v[24:27]\n"
"buffer_load_dwordx4 v3, s[56:59], s95 offen offset:1024 lds\n"
"ds_bpermute_b32 v230, v233, v232 offset:32\n"
"v_cvt_pk_bf16_f32 v202, v36, v37\n"
"v_mfma_f32_16x16x32_bf16 v[24:27], a[156:159], a[76:79], v[24:27]\n"
"ds_read_b64_tr_b16 a[200:201], v9 offset:49152\n"
"v_exp_f32 v16, v16\n"
"v_mfma_f32_16x16x32_bf16 v[24:27], a[160:163], a[80:83], v[24:27]\n"
"ds_bpermute_b32 v231, v233, v232 offset:36\n"
"v_exp_f32 v17, v17\n"
"v_mfma_f32_16x16x32_bf16 v[28:31], a[176:179], a[80:83], 0\n"
"ds_read_b128 a[180:183], v4 offset:53248\n"
"v_cvt_pk_bf16_f32 v203, v38, v39\n"
"v_cvt_pk_bf16_f32 v204, v16, v17\n"
"v_mfma_f32_16x16x32_bf16 v[28:31], a[172:175], a[76:79], v[28:31]\n"
"buffer_load_dwordx4 a[224:227], v0, s[64:67], s98 offen offset:3072 sc1\n"
"v_exp_f32 v18, v18\n"
"v_mfma_f32_16x16x32_bf16 v[28:31], a[168:171], a[72:75], v[28:31]\n"
"ds_read_b64_tr_b16 a[202:203], v9 offset:51200\n"
"v_exp_f32 v19, v19\n"
"v_mfma_f32_16x16x32_bf16 v[28:31], a[164:167], a[68:71], v[28:31]\n"
"v_exp_f32 v20, v20\n"
"v_cvt_pk_bf16_f32 v205, v18, v19\n"
"v_mfma_f32_16x16x32_bf16 a[32:35], a[204:207], v[200:203], a[32:35]\n"
"ds_read_b128 a[184:187], v5 offset:53248\n"
"v_exp_f32 v21, v21\n"
"v_mfma_f32_16x16x32_bf16 a[48:51], a[208:211], v[200:203], a[48:51]\n"
"v_exp_f32 v22, v22\n"
"v_cvt_pk_bf16_f32 v206, v20, v21\n"
"v_mfma_f32_16x16x32_bf16 a[16:19], a[0:3], v[200:203], a[16:19]\n"
"s_waitcnt lgkmcnt(3)\n"
"ds_read_b128 a[188:191], v6 offset:53248\n"
"v_exp_f32 v23, v23\n"
"v_mfma_f32_16x16x32_bf16 v[236:239], v[212:215], v[228:231], 0\n"
"v_exp_f32 v24, v24\n"
"v_mfma_f32_16x16x32_bf16 v[240:243], v[216:219], v[228:231], 0\n"
"v_exp_f32 v25, v25\n"
"v_mfma_f32_16x16x32_bf16 v[244:247], v[220:223], v[228:231], 0\n"
"s_xor_b32 s98, s30, 4096\n"
"v_cvt_pk_bf16_f32 v207, v22, v23\n"
"v_cvt_pk_bf16_f32 v208, v24, v25\n"
"v_mfma_f32_16x16x32_bf16 v[248:251], v[224:227], v[228:231], 0\n"
"v_exp_f32 v26, v26\n"
"v_mfma_f32_16x16x32_bf16 v[32:35], a[148:151], a[84:87], 0\n"
"v_exp_f32 v27, v27\n"
"v_mfma_f32_16x16x32_bf16 v[32:35], a[152:155], a[88:91], v[32:35]\n"
"buffer_load_dwordx4 a[228:231], v0, s[64:67], s98 offen offset:0 sc1\n"
"v_cvt_pk_bf16_f32 v209, v26, v27\n"
"v_mfma_f32_16x16x32_bf16 v[32:35], a[156:159], a[92:95], v[32:35]\n"
"ds_read_b128 a[192:195], v7 offset:53248\n"
"v_exp_f32 v28, v28\n"
"v_mfma_f32_16x16x32_bf16 v[32:35], a[160:163], a[96:99], v[32:35]\n"
"ds_read_b64_tr_b16 a[204:205], v8 offset:53248\n"
"v_exp_f32 v29, v29\n"
"v_mfma_f32_16x16x32_bf16 v[36:39], a[176:179], a[96:99], 0\n"
"v_exp_f32 v30, v30\n"
"v_mfma_f32_16x16x32_bf16 v[36:39], a[172:175], a[92:95], v[36:39]\n"
"v_exp_f32 v31, v31\n"
"v_mfma_f32_16x16x32_bf16 v[36:39], a[168:171], a[88:91], v[36:39]\n"
"buffer_store_dwordx4 v[236:239], v234, s[64:67], s27 offen offset:0 sc1\n"
"v_cvt_pk_bf16_f32 v210, v28, v29\n"
"v_mfma_f32_16x16x32_bf16 v[36:39], a[164:167], a[84:87], v[36:39]\n"
"buffer_load_dwordx4 a[232:235], v0, s[64:67], s98 offen offset:1024 sc1\n"
"v_cvt_pk_bf16_f32 v211, v30, v31\n"
"v_mfma_f32_16x16x32_bf16 a[20:23], a[196:199], v[204:207], a[20:23]\n"
"s_waitcnt lgkmcnt(2)\n"
"v_exp_f32 v32, v32\n"
"v_mfma_f32_16x16x32_bf16 a[36:39], a[200:203], v[204:207], a[36:39]\n"
"v_exp_f32 v33, v33\n"
"v_mfma_f32_16x16x32_bf16 a[4:7], a[0:3], v[204:207], a[4:7]\n"
"v_exp_f32 v34, v34\n"
"v_mfma_f32_16x16x32_bf16 v[16:19], a[148:151], a[100:103], 0\n"
"ds_read_b128 a[148:151], v7 offset:55296\n"
"buffer_store_dwordx4 v[240:243], v234, s[64:67], s27 offen offset:128 sc1\n"
"v_mfma_f32_16x16x32_bf16 v[16:19], a[152:155], a[104:107], v[16:19]\n"
"ds_read_b128 a[152:155], v6 offset:55296\n"
"v_cvt_pk_bf16_f32 v212, v32, v33\n"
"v_mfma_f32_16x16x32_bf16 v[16:19], a[156:159], a[108:111], v[16:19]\n"
"v_exp_f32 v35, v35\n"
"v_mfma_f32_16x16x32_bf16 v[16:19], a[160:163], a[112:115], v[16:19]\n"
"v_exp_f32 v36, v36\n"
"v_mfma_f32_16x16x32_bf16 v[20:23], a[176:179], a[112:115], 0\n"
"ds_read_b128 a[156:159], v5 offset:55296\n"
"buffer_load_dwordx4 a[236:239], v0, s[64:67], s98 offen offset:2048 sc1\n"
"v_mfma_f32_16x16x32_bf16 v[20:23], a[172:175], a[108:111], v[20:23]\n"
"v_exp_f32 v37, v37\n"
"v_mfma_f32_16x16x32_bf16 v[20:23], a[168:171], a[104:107], v[20:23]\n"
"ds_read_b128 a[160:163], v4 offset:55296\n"
"v_cvt_pk_bf16_f32 v213, v34, v35\n"
"v_mfma_f32_16x16x32_bf16 v[20:23], a[164:167], a[100:103], v[20:23]\n"
"v_exp_f32 v38, v38\n"
"v_mfma_f32_16x16x32_bf16 a[24:27], a[196:199], v[208:211], a[24:27]\n"
"buffer_store_dwordx4 v[244:247], v234, s[64:67], s27 offen offset:256 sc1\n"
"v_cvt_pk_bf16_f32 v214, v36, v37\n"
"v_mfma_f32_16x16x32_bf16 a[40:43], a[200:203], v[208:211], a[40:43]\n"
"v_exp_f32 v39, v39\n"
"v_mfma_f32_16x16x32_bf16 a[8:11], a[0:3], v[208:211], a[8:11]\n"
"v_exp_f32 v16, v16\n"
"v_mfma_f32_16x16x32_bf16 v[24:27], a[180:183], a[52:55], 0\n"
"ds_read_b64_tr_b16 a[206:207], v8 offset:55296\n"
"v_cvt_pk_bf16_f32 v215, v38, v39\n"
"v_mfma_f32_16x16x32_bf16 v[24:27], a[184:187], a[56:59], v[24:27]\n"
"s_waitcnt vmcnt(7) lgkmcnt(1)\n"
"v_exp_f32 v17, v17\n"
"v_mfma_f32_16x16x32_bf16 v[24:27], a[188:191], a[60:63], v[24:27]\n"
"s_barrier\n"
"v_exp_f32 v18, v18\n"
"v_mfma_f32_16x16x32_bf16 v[24:27], a[192:195], a[64:67], v[24:27]\n"
"buffer_store_dwordx4 v[248:251], v234, s[64:67], s27 offen offset:384 sc1\n"
"v_cvt_pk_bf16_f32 v216, v16, v17\n"
"v_mfma_f32_16x16x32_bf16 v[28:31], a[148:151], a[64:67], 0\n"
"v_exp_f32 v19, v19\n"
"v_mfma_f32_16x16x32_bf16 v[28:31], a[152:155], a[60:63], v[28:31]\n"
"v_exp_f32 v20, v20\n"
"v_mfma_f32_16x16x32_bf16 v[28:31], a[156:159], a[56:59], v[28:31]\n"
"v_exp_f32 v21, v21\n"
"v_mfma_f32_16x16x32_bf16 v[28:31], a[160:163], a[52:55], v[28:31]\n"
"ds_read_b64_tr_b16 a[208:209], v9 offset:53248\n"
"v_cvt_pk_bf16_f32 v217, v18, v19\n"
"v_mfma_f32_16x16x32_bf16 a[28:31], a[196:199], v[212:215], a[28:31]\n"
"v_exp_f32 v22, v22\n"
"v_mfma_f32_16x16x32_bf16 a[44:47], a[200:203], v[212:215], a[44:47]\n"
"v_exp_f32 v23, v23\n"
"v_mfma_f32_16x16x32_bf16 a[12:15], a[0:3], v[212:215], a[12:15]\n"
"ds_read_b128 a[176:179], v4 offset:32768\n"
"v_cvt_pk_bf16_f32 v218, v20, v21\n"
"v_mfma_f32_16x16x32_bf16 v[32:35], a[180:183], a[68:71], 0\n"
"buffer_load_dwordx4 a[240:243], v0, s[64:67], s98 offen offset:3072 sc1\n"
"v_cvt_pk_bf16_f32 v219, v22, v23\n"
"v_mfma_f32_16x16x32_bf16 v[32:35], a[184:187], a[72:75], v[32:35]\n"
"ds_read_b64_tr_b16 a[210:211], v9 offset:55296\n"
"v_exp_f32 v24, v24\n"
"v_mfma_f32_16x16x32_bf16 v[32:35], a[188:191], a[76:79], v[32:35]\n"
"ds_read_b128 a[172:175], v5 offset:32768\n"
"v_exp_f32 v25, v25\n"
"v_mfma_f32_16x16x32_bf16 v[32:35], a[192:195], a[80:83], v[32:35]\n"
"v_exp_f32 v26, v26\n"
"v_mfma_f32_16x16x32_bf16 v[36:39], a[148:151], a[80:83], 0\n"
"s_xor_b32 s98, s30, 8192\n"
"v_exp_f32 v27, v27\n"
"v_mfma_f32_16x16x32_bf16 v[36:39], a[152:155], a[76:79], v[36:39]\n"
".Lpg7:\n"
"s_cbranch_execz .Lpg11\n"
"v_exp_f32 v28, v28\n"
"v_mfma_f32_16x16x32_bf16 v[36:39], a[156:159], a[72:75], v[36:39]\n"
"v_exp_f32 v29, v29\n"
"v_mfma_f32_16x16x32_bf16 v[36:39], a[160:163], a[68:71], v[36:39]\n"
"buffer_load_dwordx4 a[244:247], v0, s[64:67], s98 offen offset:0 sc1\n"
"v_cvt_pk_bf16_f32 v220, v24, v25\n"
"v_mfma_f32_16x16x32_bf16 a[32:35], a[196:199], v[216:219], a[32:35]\n"
"v_exp_f32 v30, v30\n"
"v_mfma_f32_16x16x32_bf16 a[48:51], a[200:203], v[216:219], a[48:51]\n"
"v_cvt_pk_bf16_f32 v221, v26, v27\n"
"v_cvt_pk_bf16_f32 v222, v28, v29\n"
"v_mfma_f32_16x16x32_bf16 a[16:19], a[0:3], v[216:219], a[16:19]\n"
"ds_read_b128 a[168:171], v6 offset:32768\n"
"v_exp_f32 v31, v31\n"
"v_mfma_f32_16x16x32_bf16 v[16:19], a[180:183], a[84:87], 0\n"
"v_exp_f32 v32, v32\n"
"v_mfma_f32_16x16x32_bf16 v[16:19], a[184:187], a[88:91], v[16:19]\n"
"v_exp_f32 v33, v33\n"
"v_mfma_f32_16x16x32_bf16 v[16:19], a[188:191], a[92:95], v[16:19]\n"
"buffer_load_dwordx4 a[248:251], v0, s[64:67], s98 offen offset:1024 sc1\n"
"v_cvt_pk_bf16_f32 v223, v30, v31\n"
"v_mfma_f32_16x16x32_bf16 v[16:19], a[192:195], a[96:99], v[16:19]\n"
"v_exp_f32 v34, v34\n"
"v_mfma_f32_16x16x32_bf16 v[20:23], a[148:151], a[96:99], 0\n"
"v_exp_f32 v35, v35\n"
"v_mfma_f32_16x16x32_bf16 v[20:23], a[152:155], a[92:95], v[20:23]\n"
"v_exp_f32 v36, v36\n"
"v_mfma_f32_16x16x32_bf16 v[20:23], a[156:159], a[88:91], v[20:23]\n"
"ds_read_b128 a[164:167], v7 offset:32768\n"
"v_cvt_pk_bf16_f32 v224, v32, v33\n"
"v_mfma_f32_16x16x32_bf16 v[20:23], a[160:163], a[84:87], v[20:23]\n"
"s_waitcnt lgkmcnt(2)\n"
"v_exp_f32 v37, v37\n"
"v_cvt_pk_bf16_f32 v225, v34, v35\n"
"v_mfma_f32_16x16x32_bf16 a[20:23], a[204:207], v[220:223], a[20:23]\n"
"v_exp_f32 v38, v38\n"
"v_mfma_f32_16x16x32_bf16 a[36:39], a[208:211], v[220:223], a[36:39]\n"
"v_exp_f32 v39, v39\n"
"v_mfma_f32_16x16x32_bf16 a[4:7], a[0:3], v[220:223], a[4:7]\n"
"buffer_load_dwordx4 a[252:255], v0, s[64:67], s98 offen offset:2048 sc1\n"
"v_cvt_pk_bf16_f32 v226, v36, v37\n"
"v_mfma_f32_16x16x32_bf16 v[24:27], a[180:183], a[100:103], 0\n"
"v_exp_f32 v16, v16\n"
"v_mfma_f32_16x16x32_bf16 v[24:27], a[184:187], a[104:107], v[24:27]\n"
"v_exp_f32 v17, v17\n"
"v_mfma_f32_16x16x32_bf16 v[24:27], a[188:191], a[108:111], v[24:27]\n"
"ds_read_b128 a[180:183], v7 offset:34816\n"
"v_cvt_pk_bf16_f32 v227, v38, v39\n"
"v_mfma_f32_16x16x32_bf16 v[24:27], a[192:195], a[112:115], v[24:27]\n"
"v_exp_f32 v18, v18\n"
"v_mfma_f32_16x16x32_bf16 v[28:31], a[148:151], a[112:115], 0\n"
"ds_read_b128 a[184:187], v6 offset:34816\n"
"buffer_load_dwordx4 a[144:147], v0, s[64:67], s98 offen offset:3072 sc1\n"
"v_mfma_f32_16x16x32_bf16 v[28:31], a[152:155], a[108:111], v[28:31]\n"
"ds_read_b64_tr_b16 a[196:197], v8 offset:57344\n"
"v_exp_f32 v19, v19\n"
"v_mfma_f32_16x16x32_bf16 v[28:31], a[156:159], a[104:107], v[28:31]\n"
"ds_read_b128 a[188:191], v5 offset:34816\n"
"v_exp_f32 v20, v20\n"
"v_mfma_f32_16x16x32_bf16 v[28:31], a[160:163], a[100:103], v[28:31]\n"
"v_exp_f32 v21, v21\n"
"v_cvt_pk_bf16_f32 v228, v16, v17\n"
"v_mfma_f32_16x16x32_bf16 a[24:27], a[204:207], v[224:227], a[24:27]\n"
"ds_read_b128 a[192:195], v4 offset:34816\n"
"v_cvt_pk_bf16_f32 v229, v18, v19\n"
"v_mfma_f32_16x16x32_bf16 a[40:43], a[208:211], v[224:227], a[40:43]\n"
"v_exp_f32 v22, v22\n"
"v_cvt_pk_bf16_f32 v230, v20, v21\n"
"v_mfma_f32_16x16x32_bf16 a[8:11], a[0:3], v[224:227], a[8:11]\n"
"s_add_u32 m0, s43, 32768\n"
"ds_read_b64_tr_b16 a[198:199], v8 offset:59392\n"
"v_exp_f32 v23, v23\n"
"v_mfma_f32_16x16x32_bf16 v[32:35], a[176:179], a[52:55], 0\n"
"s_add_u32 s96, s36, 458752\n"
"v_exp_f32 v24, v24\n"
"v_mfma_f32_16x16x32_bf16 v[32:35], a[172:175], a[56:59], v[32:35]\n"
"s_waitcnt lgkmcnt(6)\n"
"v_exp_f32 v25, v25\n"
"v_mfma_f32_16x16x32_bf16 v[32:35], a[168:171], a[60:63], v[32:35]\n"
"buffer_load_dwordx4 v13, s[60:63], s96 offen offset:0 lds\n"
"v_cvt_pk_bf16_f32 v231, v22, v23\n"
"v_mfma_f32_16x16x32_bf16 v[32:35], a[164:167], a[64:67], v[32:35]\n"
"s_waitcnt lgkmcnt(5)\n"
"v_exp_f32 v26, v26\n"
"v_mfma_f32_16x16x32_bf16 v[36:39], a[180:183], a[64:67], 0\n"
"s_waitcnt lgkmcnt(4)\n"
"v_exp_f32 v27, v27\n"
"v_mfma_f32_16x16x32_bf16 v[36:39], a[184:187], a[60:63], v[36:39]\n"
"s_waitcnt lgkmcnt(2)\n"
"v_exp_f32 v28, v28\n"
"v_mfma_f32_16x16x32_bf16 v[36:39], a[188:191], a[56:59], v[36:39]\n"
"s_waitcnt lgkmcnt(1)\n"
"s_add_u32 s94, s36, 465920\n"
"v_exp_f32 v29, v29\n"
"v_mfma_f32_16x16x32_bf16 v[36:39], a[192:195], a[52:55], v[36:39]\n"
"ds_read_b64_tr_b16 a[200:201], v9 offset:57344\n"
"v_cvt_pk_bf16_f32 v232, v24, v25\n"
"v_mfma_f32_16x16x32_bf16 a[28:31], a[204:207], v[228:231], a[28:31]\n"
"v_exp_f32 v30, v30\n"
"v_mfma_f32_16x16x32_bf16 a[44:47], a[208:211], v[228:231], a[44:47]\n"
"buffer_load_dwordx4 v13, s[60:63], s94 offen offset:1024 lds\n"
"v_cvt_pk_bf16_f32 v233, v26, v27\n"
"v_mfma_f32_16x16x32_bf16 a[12:15], a[0:3], v[228:231], a[12:15]\n"
"ds_read_b128 a[148:151], v4 offset:36864\n"
"v_exp_f32 v31, v31\n"
"v_mfma_f32_16x16x32_bf16 v[16:19], a[176:179], a[68:71], 0\n"
"v_cvt_pk_bf16_f32 v234, v28, v29\n"
"v_cvt_pk_bf16_f32 v235, v30, v31\n"
"v_mfma_f32_16x16x32_bf16 v[16:19], a[172:175], a[72:75], v[16:19]\n"
"ds_read_b64_tr_b16 a[202:203], v9 offset:59392\n"
"v_exp_f32 v32, v32\n"
"v_mfma_f32_16x16x32_bf16 v[16:19], a[168:171], a[76:79], v[16:19]\n"
"s_add_u32 s95, s36, 473088\n"
"ds_read_b128 a[152:155], v5 offset:36864\n"
"v_exp_f32 v33, v33\n"
"v_mfma_f32_16x16x32_bf16 v[16:19], a[164:167], a[80:83], v[16:19]\n"
"v_exp_f32 v34, v34\n"
"v_mfma_f32_16x16x32_bf16 v[20:23], a[180:183], a[80:83], 0\n"
"v_exp_f32 v35, v35\n"
"v_mfma_f32_16x16x32_bf16 v[20:23], a[184:187], a[76:79], v[20:23]\n"
"buffer_load_dwordx4 v13, s[60:63], s95 offen offset:2048 lds\n"
"v_cvt_pk_bf16_f32 v236, v32, v33\n"
"v_mfma_f32_16x16x32_bf16 v[20:23], a[188:191], a[72:75], v[20:23]\n"
"v_exp_f32 v36, v36\n"
"v_cvt_pk_bf16_f32 v237, v34, v35\n"
"v_mfma_f32_16x16x32_bf16 v[20:23], a[192:195], a[68:71], v[20:23]\n"
"ds_read_b128 a[156:159], v6 offset:36864\n"
"v_exp_f32 v37, v37\n"
"v_mfma_f32_16x16x32_bf16 a[32:35], a[204:207], v[232:235], a[32:35]\n"
"v_exp_f32 v38, v38\n"
"v_mfma_f32_16x16x32_bf16 a[48:51], a[208:211], v[232:235], a[48:51]\n"
"s_add_u32 s96, s36, 480256\n"
"v_exp_f32 v39, v39\n"
"v_mfma_f32_16x16x32_bf16 a[16:19], a[0:3], v[232:235], a[16:19]\n"
"buffer_load_dwordx4 v13, s[60:63], s96 offen offset:3072 lds\n"
"v_cvt_pk_bf16_f32 v238, v36, v37\n"
"v_mfma_f32_16x16x32_bf16 v[24:27], a[176:179], a[84:87], 0\n"
"v_exp_f32 v16, v16\n"
"v_mfma_f32_16x16x32_bf16 v[24:27], a[172:175], a[88:91], v[24:27]\n"
"v_exp_f32 v17, v17\n"
"v_mfma_f32_16x16x32_bf16 v[24:27], a[168:171], a[92:95], v[24:27]\n"
"ds_read_b128 a[160:163], v7 offset:36864\n"
"v_cvt_pk_bf16_f32 v239, v38, v39\n"
"v_mfma_f32_16x16x32_bf16 v[24:27], a[164:167], a[96:99], v[24:27]\n"
"v_exp_f32 v18, v18\n"
"v_mfma_f32_16x16x32_bf16 v[28:31], a[180:183], a[96:99], 0\n"
"v_exp_f32 v19, v19\n"
"v_mfma_f32_16x16x32_bf16 v[28:31], a[184:187], a[92:95], v[28:31]\n"
"v_exp_f32 v20, v20\n"
"v_mfma_f32_16x16x32_bf16 v[28:31], a[188:191], a[88:91], v[28:31]\n"
"v_exp_f32 v21, v21\n"
"v_mfma_f32_16x16x32_bf16 v[28:31], a[192:195], a[84:87], v[28:31]\n"
"s_waitcnt lgkmcnt(2)\n"
"v_cvt_pk_bf16_f32 v240, v16, v17\n"
"v_cvt_pk_bf16_f32 v241, v18, v19\n"
"v_mfma_f32_16x16x32_bf16 a[20:23], a[196:199], v[236:239], a[20:23]\n"
"v_exp_f32 v22, v22\n"
"v_mfma_f32_16x16x32_bf16 a[36:39], a[200:203], v[236:239], a[36:39]\n"
"v_exp_f32 v23, v23\n"
"v_mfma_f32_16x16x32_bf16 a[4:7], a[0:3], v[236:239], a[4:7]\n"
"v_cvt_pk_bf16_f32 v242, v20, v21\n"
"v_cvt_pk_bf16_f32 v243, v22, v23\n"
"v_mfma_f32_16x16x32_bf16 v[32:35], a[176:179], a[100:103], 0\n"
"v_exp_f32 v24, v24\n"
"v_mfma_f32_16x16x32_bf16 v[32:35], a[172:175], a[104:107], v[32:35]\n"
"v_exp_f32 v25, v25\n"
"v_mfma_f32_16x16x32_bf16 v[32:35], a[168:171], a[108:111], v[32:35]\n"
"ds_read_b128 a[176:179], v7 offset:38912\n"
"v_cvt_pk_bf16_f32 v244, v24, v25\n"
"v_mfma_f32_16x16x32_bf16 v[32:35], a[164:167], a[112:115], v[32:35]\n"
"v_exp_f32 v26, v26\n"
"v_mfma_f32_16x16x32_bf16 v[36:39], a[180:183], a[112:115], 0\n"
"s_xor_b32 s98, s30, 12288\n"
"ds_read_b128 a[172:175], v6 offset:38912\n"
"v_mfma_f32_16x16x32_bf16 v[36:39], a[184:187], a[108:111], v[36:39]\n"
"ds_read_b64_tr_b16 a[204:205], v8 offset:61440\n"
"v_exp_f32 v27, v27\n"
"v_mfma_f32_16x16x32_bf16 v[36:39], a[188:191], a[104:107], v[36:39]\n"
"ds_read_b128 a[168:171], v5 offset:38912\n"
"v_exp_f32 v28, v28\n"
"v_mfma_f32_16x16x32_bf16 v[36:39], a[192:195], a[100:103], v[36:39]\n"
"ds_read_b128 a[164:167], v4 offset:38912\n"
"v_exp_f32 v29, v29\n"
"v_mfma_f32_16x16x32_bf16 a[24:27], a[196:199], v[240:243], a[24:27]\n"
"v_exp_f32 v30, v30\n"
"v_mfma_f32_16x16x32_bf16 a[40:43], a[200:203], v[240:243], a[40:43]\n"
"v_cvt_pk_bf16_f32 v245, v26, v27\n"
"v_cvt_pk_bf16_f32 v246, v28, v29\n"
"v_mfma_f32_16x16x32_bf16 a[8:11], a[0:3], v[240:243], a[8:11]\n"
"ds_read_b64_tr_b16 a[206:207], v8 offset:63488\n"
"v_exp_f32 v31, v31\n"
"v_mfma_f32_16x16x32_bf16 v[16:19], a[148:151], a[52:55], 0\n"
"v_exp_f32 v32, v32\n"
"v_mfma_f32_16x16x32_bf16 v[16:19], a[152:155], a[56:59], v[16:19]\n"
"s_waitcnt lgkmcnt(6)\n"
"buffer_load_dwordx4 a[180:183], v0, s[64:67], s98 offen offset:0 sc1\n"
"v_cvt_pk_bf16_f32 v247, v30, v31\n"
"v_mfma_f32_16x16x32_bf16 v[16:19], a[156:159], a[60:63], v[16:19]\n"
"ds_read_b64_tr_b16 a[208:209], v9 offset:61440\n"
"v_exp_f32 v33, v33\n"
"v_mfma_f32_16x16x32_bf16 v[16:19], a[160:163], a[64:67], v[16:19]\n"
"s_waitcnt lgkmcnt(6)\n"
"v_exp_f32 v34, v34\n"
"v_mfma_f32_16x16x32_bf16 v[20:23], a[176:179], a[64:67], 0\n"
"s_waitcnt lgkmcnt(5)\n"
"v_exp_f32 v35, v35\n"
"v_mfma_f32_16x16x32_bf16 v[20:23], a[172:175], a[60:63], v[20:23]\n"
"s_waitcnt lgkmcnt(3)\n"
"v_exp_f32 v36, v36\n"
"v_mfma_f32_16x16x32_bf16 v[20:23], a[168:171], a[56:59], v[20:23]\n"
"s_waitcnt lgkmcnt(2)\n"
"buffer_load_dwordx4 a[184:187], v0, s[64:67], s98 offen offset:1024 sc1\n"
"v_cvt_pk_bf16_f32 v248, v32, v33\n"
"v_mfma_f32_16x16x32_bf16 v[20:23], a[164:167], a[52:55], v[20:23]\n"
"v_exp_f32 v37, v37\n"
"v_cvt_pk_bf16_f32 v249, v34, v35\n"
"v_mfma_f32_16x16x32_bf16 a[28:31], a[196:199], v[244:247], a[28:31]\n"
"v_exp_f32 v38, v38\n"
"v_mfma_f32_16x16x32_bf16 a[44:47], a[200:203], v[244:247], a[44:47]\n"
"v_exp_f32 v39, v39\n"
"v_mfma_f32_16x16x32_bf16 a[12:15], a[0:3], v[244:247], a[12:15]\n"
"v_cvt_pk_bf16_f32 v250, v36, v37\n"
"v_cvt_pk_bf16_f32 v251, v38, v39\n"
"v_mfma_f32_16x16x32_bf16 v[24:27], a[148:151], a[68:71], 0\n"
"buffer_load_dwordx4 a[188:191], v0, s[64:67], s98 offen offset:2048 sc1\n"
"ds_read_b64_tr_b16 a[210:211], v9 offset:63488\n"
"v_mfma_f32_16x16x32_bf16 v[24:27], a[152:155], a[72:75], v[24:27]\n"
"v_exp_f32 v16, v16\n"
"v_mfma_f32_16x16x32_bf16 v[24:27], a[156:159], a[76:79], v[24:27]\n"
"v_exp_f32 v17, v17\n"
"v_mfma_f32_16x16x32_bf16 v[24:27], a[160:163], a[80:83], v[24:27]\n"
"v_exp_f32 v18, v18\n"
"v_mfma_f32_16x16x32_bf16 v[28:31], a[176:179], a[80:83], 0\n"
"v_exp_f32 v19, v19\n"
"v_mfma_f32_16x16x32_bf16 v[28:31], a[172:175], a[76:79], v[28:31]\n"
"buffer_load_dwordx4 a[192:195], v0, s[64:67], s98 offen offset:3072 sc1\n"
"v_cvt_pk_bf16_f32 v252, v16, v17\n"
"v_mfma_f32_16x16x32_bf16 v[28:31], a[168:171], a[72:75], v[28:31]\n"
"v_exp_f32 v20, v20\n"
"v_cvt_pk_bf16_f32 v253, v18, v19\n"
"v_mfma_f32_16x16x32_bf16 v[28:31], a[164:167], a[68:71], v[28:31]\n"
"v_exp_f32 v21, v21\n"
"v_mfma_f32_16x16x32_bf16 a[32:35], a[196:199], v[248:251], a[32:35]\n"
"v_exp_f32 v22, v22\n"
"v_mfma_f32_16x16x32_bf16 a[48:51], a[200:203], v[248:251], a[48:51]\n"
"v_exp_f32 v23, v23\n"
"v_mfma_f32_16x16x32_bf16 a[16:19], a[0:3], v[248:251], a[16:19]\n"
"v_cvt_pk_bf16_f32 v254, v20, v21\n"
"v_cvt_pk_bf16_f32 v255, v22, v23\n"
"v_mfma_f32_16x16x32_bf16 v[32:35], a[148:151], a[84:87], 0\n"
"v_exp_f32 v24, v24\n"
"v_mfma_f32_16x16x32_bf16 v[32:35], a[152:155], a[88:91], v[32:35]\n"
"v_exp_f32 v25, v25\n"
"v_mfma_f32_16x16x32_bf16 v[32:35], a[156:159], a[92:95], v[32:35]\n"
"v_or_b32 v1, s80, v14\n"
"v_cvt_pk_bf16_f32 v24, v24, v25\n"
"v_mfma_f32_16x16x32_bf16 v[32:35], a[160:163], a[96:99], v[32:35]\n"
"s_waitcnt vmcnt(13)\n"
"v_exp_f32 v26, v26\n"
"v_mfma_f32_16x16x32_bf16 v[36:39], a[176:179], a[96:99], 0\n"
"v_exp_f32 v27, v27\n"
"v_mfma_f32_16x16x32_bf16 v[36:39], a[172:175], a[92:95], v[36:39]\n"
"v_exp_f32 v28, v28\n"
"v_mfma_f32_16x16x32_bf16 v[36:39], a[168:171], a[88:91], v[36:39]\n"
"buffer_store_dword v12, v1, s[64:67], s12 offen sc1\n"
"v_cvt_pk_bf16_f32 v25, v26, v27\n"
"v_mfma_f32_16x16x32_bf16 v[36:39], a[164:167], a[84:87], v[36:39]\n"
"s_waitcnt lgkmcnt(0)\n"
"v_exp_f32 v29, v29\n"
"v_mfma_f32_16x16x32_bf16 a[20:23], a[204:207], v[252:255], a[20:23]\n"
"v_exp_f32 v30, v30\n"
"v_cvt_pk_bf16_f32 v26, v28, v29\n"
"v_mfma_f32_16x16x32_bf16 a[36:39], a[208:211], v[252:255], a[36:39]\n"
"v_exp_f32 v31, v31\n"
"v_mfma_f32_16x16x32_bf16 a[4:7], a[0:3], v[252:255], a[4:7]\n"
"v_exp_f32 v32, v32\n"
"v_exp_f32 v33, v33\n"
"v_mfma_f32_16x16x32_bf16 v[16:19], a[148:151], a[100:103], 0\n"
"s_xor_b32 s98, s30, 131072\n"
"v_exp_f32 v34, v34\n"
"v_exp_f32 v35, v35\n"
"v_mfma_f32_16x16x32_bf16 v[16:19], a[152:155], a[104:107], v[16:19]\n"
"v_exp_f32 v36, v36\n"
"v_cvt_pk_bf16_f32 v27, v30, v31\n"
"v_mfma_f32_16x16x32_bf16 v[16:19], a[156:159], a[108:111], v[16:19]\n"
"v_exp_f32 v37, v37\n"
"v_cvt_pk_bf16_f32 v32, v32, v33\n"
"v_mfma_f32_16x16x32_bf16 v[16:19], a[160:163], a[112:115], v[16:19]\n"
"v_exp_f32 v38, v38\n"
"v_exp_f32 v39, v39\n"
"v_mfma_f32_16x16x32_bf16 v[20:23], a[176:179], a[112:115], 0\n"
"v_cvt_pk_bf16_f32 v33, v34, v35\n"
"v_cvt_pk_bf16_f32 v34, v36, v37\n"
"v_mfma_f32_16x16x32_bf16 v[20:23], a[172:175], a[108:111], v[20:23]\n"
"buffer_load_dwordx4 a[148:151], v0, s[64:67], s98 offen offset:0 sc1\n"
"v_cvt_pk_bf16_f32 v35, v38, v39\n"
"v_mfma_f32_16x16x32_bf16 v[20:23], a[168:171], a[104:107], v[20:23]\n"
"v_mfma_f32_16x16x32_bf16 v[20:23], a[164:167], a[100:103], v[20:23]\n"
"v_exp_f32 v16, v16\n"
"v_mfma_f32_16x16x32_bf16 a[24:27], a[204:207], v[24:27], a[24:27]\n"
"v_exp_f32 v17, v17\n"
"v_mfma_f32_16x16x32_bf16 a[40:43], a[208:211], v[24:27], a[40:43]\n"
"v_exp_f32 v18, v18\n"
"v_cvt_pk_bf16_f32 v16, v16, v17\n"
"v_mfma_f32_16x16x32_bf16 a[8:11], a[0:3], v[24:27], a[8:11]\n"
"buffer_load_dwordx4 a[152:155], v0, s[64:67], s98 offen offset:1024 sc1\n"
"v_exp_f32 v19, v19\n"
"v_mfma_f32_16x16x32_bf16 a[28:31], a[204:207], v[32:35], a[28:31]\n"
"v_exp_f32 v20, v20\n"
"v_cvt_pk_bf16_f32 v17, v18, v19\n"
"v_mfma_f32_16x16x32_bf16 a[44:47], a[208:211], v[32:35], a[44:47]\n"
"v_exp_f32 v21, v21\n"
"v_exp_f32 v22, v22\n"
"v_exp_f32 v23, v23\n"
"v_cvt_pk_bf16_f32 v18, v20, v21\n"
"v_cvt_pk_bf16_f32 v19, v22, v23\n"
"v_mfma_f32_16x16x32_bf16 a[12:15], a[0:3], v[32:35], a[12:15]\n"
"s_nop 0\n"
"v_mfma_f32_16x16x32_bf16 a[32:35], a[204:207], v[16:19], a[32:35]\n"
"v_mfma_f32_16x16x32_bf16 a[48:51], a[208:211], v[16:19], a[48:51]\n"
"v_mfma_f32_16x16x32_bf16 a[16:19], a[0:3], v[16:19], a[16:19]\n"
"s_nop 0\n"
"v_accvgpr_read_b32 v4, a4\n"
"v_accvgpr_read_b32 v5, a8\n"
"s_nop 1\n"
"v_accvgpr_read_b32 v6, a12\n"
"s_nop 2\n"
"v_accvgpr_read_b32 v7, a16\n"
"v_add_f32 v1, v4, v5\n"
"v_min3_f32 v2, v4, v5, v6\n"
"v_add_f32 v3, v6, v7\n"
"v_min_f32 v2, v2, v7\n"
"v_add_f32 v1, v1, v3\n"
"v_cmp_nle_f32 vcc, 0x30800000, v2\n"
"v_mov_b32 v3, 0x53800000\n"
"v_cmp_nle_f32 s[28:29], v1, v3\n"
"s_nop 1\n"
"s_or_b64 vcc, vcc, s[28:29]\n"
"s_cbranch_vccnz .Lfallback\n"
".Lfbdone:\n"
"v_rcp_f32 v4, v4\n"
"v_rcp_f32 v5, v5\n"
"v_rcp_f32 v6, v6\n"
"v_rcp_f32 v7, v7\n"
"ds_read_b64_tr_b16 a[164:165], v10 offset:0\n"
"ds_read_b64_tr_b16 a[166:167], v10 offset:2048\n"
"ds_read_b64_tr_b16 a[168:169], v10 offset:4096\n"
"ds_read_b64_tr_b16 a[170:171], v10 offset:6144\n"
"ds_read_b64_tr_b16 a[172:173], v10 offset:8192\n"
"ds_read_b64_tr_b16 a[174:175], v10 offset:10240\n"
"ds_read_b64_tr_b16 a[176:177], v10 offset:12288\n"
"ds_read_b64_tr_b16 a[178:179], v10 offset:14336\n"
"s_add_u32 s99, s33, 0\n"
"v_accvgpr_read_b32 v36, a20\n"
"v_accvgpr_read_b32 v37, a21\n"
"v_accvgpr_read_b32 v38, a22\n"
"v_accvgpr_read_b32 v39, a23\n"
"v_pk_mul_f32 v[36:37], v[36:37], v[4:5] op_sel_hi:[1,0]\n"
"v_pk_mul_f32 v[38:39], v[38:39], v[4:5] op_sel_hi:[1,0]\n"
"v_cvt_pk_bf16_f32 v20, v36, v37\n"
"v_cvt_pk_bf16_f32 v21, v38, v39\n"
"v_accvgpr_read_b32 v36, a36\n"
"v_accvgpr_read_b32 v37, a37\n"
"v_accvgpr_read_b32 v38, a38\n"
"v_accvgpr_read_b32 v39, a39\n"
"v_pk_mul_f32 v[36:37], v[36:37], v[4:5] op_sel_hi:[1,0]\n"
"v_pk_mul_f32 v[38:39], v[38:39], v[4:5] op_sel_hi:[1,0]\n"
"v_cvt_pk_bf16_f32 v22, v36, v37\n"
"v_cvt_pk_bf16_f32 v23, v38, v39\n"
"buffer_store_dwordx4 v[20:23], v0, s[64:67], s99 offen offset:0 sc1\n"
"v_accvgpr_read_b32 v36, a24\n"
"v_accvgpr_read_b32 v37, a25\n"
"v_accvgpr_read_b32 v38, a26\n"
"v_accvgpr_read_b32 v39, a27\n"
"v_pk_mul_f32 v[36:37], v[36:37], v[4:5] op_sel:[0,1] op_sel_hi:[1,1]\n"
"v_pk_mul_f32 v[38:39], v[38:39], v[4:5] op_sel:[0,1] op_sel_hi:[1,1]\n"
"v_cvt_pk_bf16_f32 v28, v36, v37\n"
"v_cvt_pk_bf16_f32 v29, v38, v39\n"
"v_accvgpr_read_b32 v36, a40\n"
"v_accvgpr_read_b32 v37, a41\n"
"v_accvgpr_read_b32 v38, a42\n"
"v_accvgpr_read_b32 v39, a43\n"
"v_pk_mul_f32 v[36:37], v[36:37], v[4:5] op_sel:[0,1] op_sel_hi:[1,1]\n"
"v_pk_mul_f32 v[38:39], v[38:39], v[4:5] op_sel:[0,1] op_sel_hi:[1,1]\n"
"v_cvt_pk_bf16_f32 v30, v36, v37\n"
"v_cvt_pk_bf16_f32 v31, v38, v39\n"
"buffer_store_dwordx4 v[28:31], v0, s[64:67], s99 offen offset:1024 sc1\n"
"s_waitcnt lgkmcnt(5)\n"
"v_mfma_f32_16x16x32_bf16 a[52:55], a[164:167], a[116:119], 0\n"
"s_waitcnt lgkmcnt(3)\n"
"buffer_load_dwordx4 a[156:159], v0, s[64:67], s98 offen offset:2048 sc1\n"
"v_accvgpr_read_b32 v36, a28\n"
"v_mfma_f32_16x16x32_bf16 a[52:55], a[168:171], a[132:135], a[52:55]\n"
"s_barrier\n"
"v_accvgpr_read_b32 v37, a29\n"
"v_accvgpr_read_b32 v38, a30\n"
"s_waitcnt lgkmcnt(1)\n"
"v_mfma_f32_16x16x32_bf16 a[52:55], a[172:175], v[44:47], a[52:55]\n"
"s_waitcnt lgkmcnt(0)\n"
"v_accvgpr_read_b32 v39, a31\n"
"v_mul_f32 v36, v6, v36\n"
"v_mfma_f32_16x16x32_bf16 a[52:55], a[176:179], v[60:63], a[52:55]\n"
"ds_read_b64_tr_b16 a[196:197], v11 offset:0\n"
"v_mul_f32 v37, v6, v37\n"
"v_mfma_f32_16x16x32_bf16 a[56:59], a[176:179], v[64:67], 0\n"
"ds_read_b64_tr_b16 a[198:199], v11 offset:2048\n"
"v_mul_f32 v38, v6, v38\n"
"v_mfma_f32_16x16x32_bf16 a[56:59], a[172:175], v[48:51], a[56:59]\n"
"buffer_load_dwordx4 a[160:163], v0, s[64:67], s98 offen offset:3072 sc1\n"
"ds_read_b64_tr_b16 a[200:201], v11 offset:4096\n"
"v_mfma_f32_16x16x32_bf16 a[56:59], a[168:171], a[136:139], a[56:59]\n"
"ds_read_b64_tr_b16 a[202:203], v11 offset:6144\n"
"v_mul_f32 v39, v6, v39\n"
"v_mfma_f32_16x16x32_bf16 a[56:59], a[164:167], a[120:123], a[56:59]\n"
"ds_read_b64_tr_b16 a[204:205], v11 offset:8192\n"
"v_cvt_pk_bf16_f32 v20, v36, v37\n"
"v_mfma_f32_16x16x32_bf16 a[60:63], a[164:167], a[124:127], 0\n"
"s_xor_b32 s98, s30, 135168\n"
"ds_read_b64_tr_b16 a[206:207], v11 offset:10240\n"
"v_cvt_pk_bf16_f32 v21, v38, v39\n"
"v_mfma_f32_16x16x32_bf16 a[60:63], a[168:171], a[140:143], a[60:63]\n"
"ds_read_b64_tr_b16 a[208:209], v11 offset:12288\n"
"v_accvgpr_read_b32 v36, a44\n"
"v_mfma_f32_16x16x32_bf16 a[60:63], a[172:175], v[52:55], a[60:63]\n"
"buffer_load_dwordx4 a[84:87], v0, s[64:67], s98 offen offset:0 sc1\n"
"ds_read_b64_tr_b16 a[210:211], v11 offset:14336\n"
"v_mfma_f32_16x16x32_bf16 a[60:63], a[176:179], v[68:71], a[60:63]\n"
"v_accvgpr_read_b32 v37, a45\n"
"v_accvgpr_read_b32 v38, a46\n"
"v_mfma_f32_16x16x32_bf16 a[64:67], a[176:179], v[72:75], 0\n"
"v_accvgpr_read_b32 v39, a47\n"
"v_mul_f32 v36, v6, v36\n"
"v_mfma_f32_16x16x32_bf16 a[64:67], a[172:175], v[56:59], a[64:67]\n"
"v_mul_f32 v37, v6, v37\n"
"v_mul_f32 v38, v6, v38\n"
"v_mfma_f32_16x16x32_bf16 a[64:67], a[168:171], v[40:43], a[64:67]\n"
"v_mul_f32 v39, v6, v39\n"
"v_cvt_pk_bf16_f32 v22, v36, v37\n"
"v_mfma_f32_16x16x32_bf16 a[64:67], a[164:167], a[128:131], a[64:67]\n"
"s_waitcnt lgkmcnt(6)\n"
"buffer_load_dwordx4 a[88:91], v0, s[64:67], s98 offen offset:1024 sc1\n"
"v_cvt_pk_bf16_f32 v23, v38, v39\n"
"v_mfma_f32_16x16x32_bf16 a[80:83], a[196:199], a[128:131], 0\n"
"s_waitcnt lgkmcnt(4)\n"
"v_accvgpr_read_b32 v36, a32\n"
"v_accvgpr_read_b32 v37, a33\n"
"v_mfma_f32_16x16x32_bf16 a[80:83], a[200:203], v[40:43], a[80:83]\n"
"s_waitcnt lgkmcnt(2)\n"
"v_accvgpr_read_b32 v38, a34\n"
"v_accvgpr_read_b32 v39, a35\n"
"v_mfma_f32_16x16x32_bf16 a[80:83], a[204:207], v[56:59], a[80:83]\n"
"s_waitcnt lgkmcnt(0)\n"
"v_mul_f32 v36, v7, v36\n"
"v_mul_f32 v37, v7, v37\n"
"v_mfma_f32_16x16x32_bf16 a[80:83], a[208:211], v[72:75], a[80:83]\n"
"v_mul_f32 v38, v7, v38\n"
"v_mul_f32 v39, v7, v39\n"
"v_mfma_f32_16x16x32_bf16 a[76:79], a[208:211], v[68:71], 0\n"
"buffer_store_dwordx4 v[20:23], v0, s[64:67], s99 offen offset:2048 sc1\n"
"v_cvt_pk_bf16_f32 v28, v36, v37\n"
"v_mfma_f32_16x16x32_bf16 a[76:79], a[204:207], v[52:55], a[76:79]\n"
"ds_read_b64_tr_b16 a[176:177], v10 offset:16384\n"
"v_cvt_pk_bf16_f32 v29, v38, v39\n"
"v_mfma_f32_16x16x32_bf16 a[76:79], a[200:203], a[140:143], a[76:79]\n"
"ds_read_b64_tr_b16 a[178:179], v10 offset:18432\n"
"v_accvgpr_read_b32 v36, a48\n"
"v_mfma_f32_16x16x32_bf16 a[76:79], a[196:199], a[124:127], a[76:79]\n"
"ds_read_b64_tr_b16 a[172:173], v10 offset:20480\n"
"v_accvgpr_read_b32 v37, a49\n"
"v_mfma_f32_16x16x32_bf16 a[72:75], a[196:199], a[120:123], 0\n"
"ds_read_b64_tr_b16 a[174:175], v10 offset:22528\n"
"v_accvgpr_read_b32 v38, a50\n"
"v_mfma_f32_16x16x32_bf16 a[72:75], a[200:203], a[136:139], a[72:75]\n"
"ds_read_b64_tr_b16 a[168:169], v10 offset:24576\n"
"v_accvgpr_read_b32 v39, a51\n"
"v_mfma_f32_16x16x32_bf16 a[72:75], a[204:207], v[48:51], a[72:75]\n"
"buffer_load_dwordx4 a[92:95], v0, s[64:67], s98 offen offset:2048 sc1\n"
"ds_read_b64_tr_b16 a[170:171], v10 offset:26624\n"
"v_mfma_f32_16x16x32_bf16 a[72:75], a[208:211], v[64:67], a[72:75]\n"
"ds_read_b64_tr_b16 a[164:165], v10 offset:28672\n"
"v_mul_f32 v36, v7, v36\n"
"v_mfma_f32_16x16x32_bf16 a[68:71], a[208:211], v[60:63], 0\n"
"ds_read_b64_tr_b16 a[166:167], v10 offset:30720\n"
"v_mul_f32 v37, v7, v37\n"
"v_mfma_f32_16x16x32_bf16 a[68:71], a[204:207], v[44:47], a[68:71]\n"
"v_mul_f32 v38, v7, v38\n"
"v_mul_f32 v39, v7, v39\n"
"v_mfma_f32_16x16x32_bf16 a[68:71], a[200:203], a[132:135], a[68:71]\n"
"v_cvt_pk_bf16_f32 v30, v36, v37\n"
"v_cvt_pk_bf16_f32 v31, v38, v39\n"
"v_mfma_f32_16x16x32_bf16 a[68:71], a[196:199], a[116:119], a[68:71]\n"
"s_waitcnt lgkmcnt(6)\n"
"buffer_load_dwordx4 a[96:99], v0, s[64:67], s98 offen offset:3072 sc1\n"
"ds_read_b64_tr_b16 a[208:209], v11 offset:16384\n"
"v_mfma_f32_16x16x32_bf16 a[52:55], a[176:179], v[76:79], a[52:55]\n"
"s_waitcnt lgkmcnt(5)\n"
"ds_read_b64_tr_b16 a[210:211], v11 offset:18432\n"
"v_mfma_f32_16x16x32_bf16 a[52:55], a[172:175], v[92:95], a[52:55]\n"
"s_waitcnt lgkmcnt(4)\n"
"ds_read_b64_tr_b16 a[204:205], v11 offset:20480\n"
"v_mfma_f32_16x16x32_bf16 a[52:55], a[168:171], v[108:111], a[52:55]\n"
"s_waitcnt lgkmcnt(3)\n"
"ds_read_b64_tr_b16 a[206:207], v11 offset:22528\n"
"v_mfma_f32_16x16x32_bf16 a[52:55], a[164:167], v[124:127], a[52:55]\n"
"ds_read_b64_tr_b16 a[200:201], v11 offset:24576\n"
"v_mfma_f32_16x16x32_bf16 a[56:59], a[164:167], v[128:131], a[56:59]\n"
"buffer_store_dwordx4 v[28:31], v0, s[64:67], s99 offen offset:3072 sc1\n"
"ds_read_b64_tr_b16 a[202:203], v11 offset:26624\n"
"v_mfma_f32_16x16x32_bf16 a[56:59], a[168:171], v[112:115], a[56:59]\n"
"ds_read_b64_tr_b16 a[196:197], v11 offset:28672\n"
"v_mfma_f32_16x16x32_bf16 a[56:59], a[172:175], v[96:99], a[56:59]\n"
"ds_read_b64_tr_b16 a[198:199], v11 offset:30720\n"
"v_mfma_f32_16x16x32_bf16 a[56:59], a[176:179], v[80:83], a[56:59]\n"
"s_xor_b32 s98, s30, 139264\n"
"v_mfma_f32_16x16x32_bf16 a[60:63], a[176:179], v[84:87], a[60:63]\n"
"v_mfma_f32_16x16x32_bf16 a[60:63], a[172:175], v[100:103], a[60:63]\n"
"buffer_load_dwordx4 a[100:103], v0, s[64:67], s98 offen offset:0 sc1\n"
"v_mfma_f32_16x16x32_bf16 a[60:63], a[168:171], v[116:119], a[60:63]\n"
"v_mfma_f32_16x16x32_bf16 a[60:63], a[164:167], v[132:135], a[60:63]\n"
"v_mfma_f32_16x16x32_bf16 a[64:67], a[164:167], v[136:139], a[64:67]\n"
"ds_read_b64_tr_b16 a[164:165], v10 offset:32768\n"
"v_mfma_f32_16x16x32_bf16 a[64:67], a[168:171], v[120:123], a[64:67]\n"
"ds_read_b64_tr_b16 a[166:167], v10 offset:34816\n"
"v_mfma_f32_16x16x32_bf16 a[64:67], a[172:175], v[104:107], a[64:67]\n"
"buffer_load_dwordx4 a[104:107], v0, s[64:67], s98 offen offset:1024 sc1\n"
"ds_read_b64_tr_b16 a[168:169], v10 offset:36864\n"
"v_mfma_f32_16x16x32_bf16 a[64:67], a[176:179], v[88:91], a[64:67]\n"
"s_waitcnt lgkmcnt(6)\n"
"ds_read_b64_tr_b16 a[170:171], v10 offset:38912\n"
"v_mfma_f32_16x16x32_bf16 a[80:83], a[208:211], v[88:91], a[80:83]\n"
"ds_read_b64_tr_b16 a[172:173], v10 offset:40960\n"
"v_mfma_f32_16x16x32_bf16 a[80:83], a[204:207], v[104:107], a[80:83]\n"
"s_waitcnt lgkmcnt(6)\n"
"ds_read_b64_tr_b16 a[174:175], v10 offset:43008\n"
"v_mfma_f32_16x16x32_bf16 a[80:83], a[200:203], v[120:123], a[80:83]\n"
"s_waitcnt lgkmcnt(6)\n"
"ds_read_b64_tr_b16 a[176:177], v10 offset:45056\n"
"v_mfma_f32_16x16x32_bf16 a[80:83], a[196:199], v[136:139], a[80:83]\n"
"ds_read_b64_tr_b16 a[178:179], v10 offset:47104\n"
"v_mfma_f32_16x16x32_bf16 a[76:79], a[196:199], v[132:135], a[76:79]\n"
"buffer_load_dwordx4 a[108:111], v0, s[64:67], s98 offen offset:2048 sc1\n"
"v_mfma_f32_16x16x32_bf16 a[76:79], a[200:203], v[116:119], a[76:79]\n"
"v_mfma_f32_16x16x32_bf16 a[76:79], a[204:207], v[100:103], a[76:79]\n"
"v_mfma_f32_16x16x32_bf16 a[76:79], a[208:211], v[84:87], a[76:79]\n"
"v_mfma_f32_16x16x32_bf16 a[72:75], a[208:211], v[80:83], a[72:75]\n"
"v_mfma_f32_16x16x32_bf16 a[72:75], a[204:207], v[96:99], a[72:75]\n"
"buffer_load_dwordx4 a[112:115], v0, s[64:67], s98 offen offset:3072 sc1\n"
"v_mfma_f32_16x16x32_bf16 a[72:75], a[200:203], v[112:115], a[72:75]\n"
"v_mfma_f32_16x16x32_bf16 a[72:75], a[196:199], v[128:131], a[72:75]\n"
"v_mfma_f32_16x16x32_bf16 a[68:71], a[196:199], v[124:127], a[68:71]\n"
"s_xor_b32 s98, s30, 143360\n"
"ds_read_b64_tr_b16 a[196:197], v11 offset:32768\n"
"v_mfma_f32_16x16x32_bf16 a[68:71], a[200:203], v[108:111], a[68:71]\n"
"ds_read_b64_tr_b16 a[198:199], v11 offset:34816\n"
"v_mfma_f32_16x16x32_bf16 a[68:71], a[204:207], v[92:95], a[68:71]\n"
"buffer_load_dwordx4 a[0:3], v0, s[64:67], s98 offen offset:0 sc1\n"
"ds_read_b64_tr_b16 a[200:201], v11 offset:36864\n"
"v_mfma_f32_16x16x32_bf16 a[68:71], a[208:211], v[76:79], a[68:71]\n"
"s_waitcnt lgkmcnt(3)\n"
"ds_read_b64_tr_b16 a[202:203], v11 offset:38912\n"
"v_mfma_f32_16x16x32_bf16 a[52:55], a[164:167], v[140:143], a[52:55]\n"
"ds_read_b64_tr_b16 a[204:205], v11 offset:40960\n"
"v_mfma_f32_16x16x32_bf16 a[52:55], a[168:171], v[156:159], a[52:55]\n"
"ds_read_b64_tr_b16 a[206:207], v11 offset:43008\n"
"v_mfma_f32_16x16x32_bf16 a[52:55], a[172:175], v[172:175], a[52:55]\n"
"ds_read_b64_tr_b16 a[208:209], v11 offset:45056\n"
"v_mfma_f32_16x16x32_bf16 a[52:55], a[176:179], v[188:191], a[52:55]\n"
"buffer_load_dwordx4 a[4:7], v0, s[64:67], s98 offen offset:1024 sc1\n"
"ds_read_b64_tr_b16 a[210:211], v11 offset:47104\n"
"v_mfma_f32_16x16x32_bf16 a[56:59], a[176:179], v[192:195], a[56:59]\n"
"v_mfma_f32_16x16x32_bf16 a[56:59], a[172:175], v[176:179], a[56:59]\n"
"v_mfma_f32_16x16x32_bf16 a[56:59], a[168:171], v[160:163], a[56:59]\n"
"v_mfma_f32_16x16x32_bf16 a[56:59], a[164:167], v[144:147], a[56:59]\n"
"v_mfma_f32_16x16x32_bf16 a[60:63], a[164:167], v[148:151], a[60:63]\n"
"buffer_load_dwordx4 a[8:11], v0, s[64:67], s98 offen offset:2048 sc1\n"
"v_mfma_f32_16x16x32_bf16 a[60:63], a[168:171], v[164:167], a[60:63]\n"
"v_mfma_f32_16x16x32_bf16 a[60:63], a[172:175], v[180:183], a[60:63]\n"
"v_mfma_f32_16x16x32_bf16 a[60:63], a[176:179], v[196:199], a[60:63]\n"
"v_mfma_f32_16x16x32_bf16 a[64:67], a[176:179], v[200:203], a[64:67]\n"
"ds_read_b64_tr_b16 a[176:177], v10 offset:49152\n"
"v_mfma_f32_16x16x32_bf16 a[64:67], a[172:175], v[184:187], a[64:67]\n"
"ds_read_b64_tr_b16 a[178:179], v10 offset:51200\n"
"v_mfma_f32_16x16x32_bf16 a[64:67], a[168:171], v[168:171], a[64:67]\n"
"buffer_load_dwordx4 a[12:15], v0, s[64:67], s98 offen offset:3072 sc1\n"
"ds_read_b64_tr_b16 a[172:173], v10 offset:53248\n"
"v_mfma_f32_16x16x32_bf16 a[64:67], a[164:167], v[152:155], a[64:67]\n"
"s_waitcnt lgkmcnt(4)\n"
"ds_read_b64_tr_b16 a[174:175], v10 offset:55296\n"
"v_mfma_f32_16x16x32_bf16 a[80:83], a[196:199], v[152:155], a[80:83]\n"
".Lpg8:\n"
"s_cbranch_execz .Lpg12\n"
"ds_read_b64_tr_b16 a[168:169], v10 offset:57344\n"
"v_mfma_f32_16x16x32_bf16 a[80:83], a[200:203], v[168:171], a[80:83]\n"
"s_xor_b32 s98, s30, 262144\n"
"ds_read_b64_tr_b16 a[170:171], v10 offset:59392\n"
"v_mfma_f32_16x16x32_bf16 a[80:83], a[204:207], v[184:187], a[80:83]\n"
"s_waitcnt lgkmcnt(6)\n"
"ds_read_b64_tr_b16 a[164:165], v10 offset:61440\n"
"v_mfma_f32_16x16x32_bf16 a[80:83], a[208:211], v[200:203], a[80:83]\n"
"buffer_load_dwordx4 a[16:19], v0, s[64:67], s98 offen offset:0 sc1\n"
"ds_read_b64_tr_b16 a[166:167], v10 offset:63488\n"
"v_mfma_f32_16x16x32_bf16 a[76:79], a[208:211], v[196:199], a[76:79]\n"
"v_mfma_f32_16x16x32_bf16 a[76:79], a[204:207], v[180:183], a[76:79]\n"
"v_mfma_f32_16x16x32_bf16 a[76:79], a[200:203], v[164:167], a[76:79]\n"
"s_add_u32 m0, s43, 16384\n"
"v_mfma_f32_16x16x32_bf16 a[76:79], a[196:199], v[148:151], a[76:79]\n"
"s_add_u32 s94, s36, 327680\n"
"v_mfma_f32_16x16x32_bf16 a[72:75], a[196:199], v[144:147], a[72:75]\n"
"buffer_load_dwordx4 v13, s[60:63], s94 offen offset:0 lds\n"
"v_mfma_f32_16x16x32_bf16 a[72:75], a[200:203], v[160:163], a[72:75]\n"
"v_mfma_f32_16x16x32_bf16 a[72:75], a[204:207], v[176:179], a[72:75]\n"
"v_mfma_f32_16x16x32_bf16 a[72:75], a[208:211], v[192:195], a[72:75]\n"
"s_add_u32 s95, s36, 334848\n"
"v_mfma_f32_16x16x32_bf16 a[68:71], a[208:211], v[188:191], a[68:71]\n"
"ds_read_b64_tr_b16 a[208:209], v11 offset:49152\n"
"v_mfma_f32_16x16x32_bf16 a[68:71], a[204:207], v[172:175], a[68:71]\n"
"buffer_load_dwordx4 v13, s[60:63], s95 offen offset:1024 lds\n"
"ds_read_b64_tr_b16 a[210:211], v11 offset:51200\n"
"v_mfma_f32_16x16x32_bf16 a[68:71], a[200:203], v[156:159], a[68:71]\n"
"ds_read_b64_tr_b16 a[204:205], v11 offset:53248\n"
"v_mfma_f32_16x16x32_bf16 a[68:71], a[196:199], v[140:143], a[68:71]\n"
"s_waitcnt lgkmcnt(4)\n"
"ds_read_b64_tr_b16 a[206:207], v11 offset:55296\n"
"v_mfma_f32_16x16x32_bf16 a[52:55], a[176:179], v[204:207], a[52:55]\n"
"s_waitcnt vmcnt(11)\n"
"ds_read_b64_tr_b16 a[200:201], v11 offset:57344\n"
"v_mfma_f32_16x16x32_bf16 a[52:55], a[172:175], v[220:223], a[52:55]\n"
"s_waitcnt lgkmcnt(5)\n"
"s_add_u32 s24, s34, 0\n"
"ds_read_b64_tr_b16 a[202:203], v11 offset:59392\n"
"v_mfma_f32_16x16x32_bf16 a[52:55], a[168:171], v[236:239], a[52:55]\n"
"buffer_store_dword v12, v14, s[64:67], s24 offen sc1\n"
"ds_read_b64_tr_b16 a[196:197], v11 offset:61440\n"
"v_mfma_f32_16x16x32_bf16 a[52:55], a[164:167], v[252:255], a[52:55]\n"
"ds_read_b64_tr_b16 a[198:199], v11 offset:63488\n"
"v_subrev_u32 v8, 65536, v8\n"
"v_mfma_f32_16x16x32_bf16 a[56:59], a[164:167], v[24:27], a[56:59]\n"
"v_subrev_u32 v9, 65536, v9\n"
"v_subrev_u32 v10, 65536, v10\n"
"v_mfma_f32_16x16x32_bf16 a[56:59], a[168:171], v[240:243], a[56:59]\n"
"v_subrev_u32 v11, 65536, v11\n"
"v_mfma_f32_16x16x32_bf16 a[56:59], a[172:175], v[224:227], a[56:59]\n"
"s_add_u32 s96, s36, 342016\n"
"v_mfma_f32_16x16x32_bf16 a[56:59], a[176:179], v[208:211], a[56:59]\n"
"v_mfma_f32_16x16x32_bf16 a[60:63], a[176:179], v[212:215], a[60:63]\n"
"buffer_load_dwordx4 v13, s[60:63], s96 offen offset:2048 lds\n"
"v_mfma_f32_16x16x32_bf16 a[60:63], a[172:175], v[228:231], a[60:63]\n"
"v_mfma_f32_16x16x32_bf16 a[60:63], a[168:171], v[244:247], a[60:63]\n"
"v_mfma_f32_16x16x32_bf16 a[60:63], a[164:167], v[32:35], a[60:63]\n"
"s_add_u32 s94, s36, 349184\n"
"v_mfma_f32_16x16x32_bf16 a[64:67], a[164:167], v[16:19], a[64:67]\n"
"ds_read_b64_tr_b16 a[164:165], v8 offset:0\n"
"v_mfma_f32_16x16x32_bf16 a[64:67], a[168:171], v[248:251], a[64:67]\n"
"buffer_load_dwordx4 v13, s[60:63], s94 offen offset:3072 lds\n"
"ds_read_b64_tr_b16 a[166:167], v8 offset:2048\n"
"v_mfma_f32_16x16x32_bf16 a[64:67], a[172:175], v[232:235], a[64:67]\n"
"ds_read_b64_tr_b16 a[168:169], v8 offset:4096\n"
"v_mfma_f32_16x16x32_bf16 a[64:67], a[176:179], v[216:219], a[64:67]\n"
"s_waitcnt lgkmcnt(3)\n"
"ds_read_b64_tr_b16 a[170:171], v8 offset:6144\n"
"v_mfma_f32_16x16x32_bf16 a[80:83], a[208:211], v[216:219], a[80:83]\n"
"ds_read_b64_tr_b16 a[172:173], v8 offset:8192\n"
"v_mfma_f32_16x16x32_bf16 a[80:83], a[204:207], v[232:235], a[80:83]\n"
"ds_read_b64_tr_b16 a[174:175], v8 offset:10240\n"
"v_mfma_f32_16x16x32_bf16 a[80:83], a[200:203], v[248:251], a[80:83]\n"
"ds_read_b64_tr_b16 a[176:177], v8 offset:12288\n"
"v_mfma_f32_16x16x32_bf16 a[80:83], a[196:199], v[16:19], a[80:83]\n"
"ds_read_b64_tr_b16 a[178:179], v8 offset:14336\n"
"v_mfma_f32_16x16x32_bf16 a[76:79], a[196:199], v[32:35], a[76:79]\n"
"v_mfma_f32_16x16x32_bf16 a[76:79], a[200:203], v[244:247], a[76:79]\n"
"s_barrier\n"
"v_mfma_f32_16x16x32_bf16 a[76:79], a[204:207], v[228:231], a[76:79]\n"
"v_mfma_f32_16x16x32_bf16 a[76:79], a[208:211], v[212:215], a[76:79]\n"
"v_mfma_f32_16x16x32_bf16 a[72:75], a[208:211], v[208:211], a[72:75]\n"
"v_accvgpr_read_b32 v36, a64\n"
"v_accvgpr_read_b32 v37, a65\n"
"v_mfma_f32_16x16x32_bf16 a[72:75], a[204:207], v[224:227], a[72:75]\n"
"v_accvgpr_read_b32 v38, a66\n"
"v_accvgpr_read_b32 v39, a67\n"
"v_mfma_f32_16x16x32_bf16 a[72:75], a[200:203], v[240:243], a[72:75]\n"
"v_mul_f32 v36, v7, v36\n"
"v_mul_f32 v37, v7, v37\n"
"v_mfma_f32_16x16x32_bf16 a[72:75], a[196:199], v[24:27], a[72:75]\n"
"v_mul_f32 v38, v7, v38\n"
"v_mul_f32 v39, v7, v39\n"
"v_mfma_f32_16x16x32_bf16 a[68:71], a[196:199], v[252:255], a[68:71]\n"
"v_cvt_pk_bf16_f32 v28, v36, v37\n"
"v_cvt_pk_bf16_f32 v29, v38, v39\n"
"v_mfma_f32_16x16x32_bf16 a[68:71], a[200:203], v[236:239], a[68:71]\n"
"s_waitcnt lgkmcnt(3)\n"
"v_accvgpr_read_b32 v36, a80\n"
"v_accvgpr_read_b32 v37, a81\n"
"v_mfma_f32_16x16x32_bf16 a[68:71], a[204:207], v[220:223], a[68:71]\n"
"s_add_u32 m0, s43, -49152\n"
"v_accvgpr_read_b32 v38, a82\n"
"v_accvgpr_read_b32 v39, a83\n"
"v_mfma_f32_16x16x32_bf16 a[68:71], a[208:211], v[204:207], a[68:71]\n"
"s_add_u32 s95, s36, 98304\n"
"v_mul_f32 v36, v7, v36\n"
"v_mul_f32 v37, v7, v37\n"
"v_mfma_f32_16x16x32_bf16 a[20:23], a[164:167], a[116:119], 0\n"
"buffer_load_dwordx4 v13, s[60:63], s95 offen offset:0 lds\n"
"v_mul_f32 v38, v7, v38\n"
"v_mfma_f32_16x16x32_bf16 a[20:23], a[168:171], a[132:135], a[20:23]\n"
"s_waitcnt lgkmcnt(0)\n"
"v_mul_f32 v39, v7, v39\n"
"v_cvt_pk_bf16_f32 v30, v36, v37\n"
"v_mfma_f32_16x16x32_bf16 a[20:23], a[172:175], v[44:47], a[20:23]\n"
"v_cvt_pk_bf16_f32 v31, v38, v39\n"
"v_accvgpr_read_b32 v36, a60\n"
"v_mfma_f32_16x16x32_bf16 a[20:23], a[176:179], v[60:63], a[20:23]\n"
"s_add_u32 s96, s36, 105472\n"
"v_accvgpr_read_b32 v37, a61\n"
"v_accvgpr_read_b32 v38, a62\n"
"v_mfma_f32_16x16x32_bf16 a[24:27], a[176:179], v[64:67], 0\n"
"v_accvgpr_read_b32 v39, a63\n"
"v_mul_f32 v36, v6, v36\n"
"v_mfma_f32_16x16x32_bf16 a[24:27], a[172:175], v[48:51], a[24:27]\n"
"buffer_load_dwordx4 v13, s[60:63], s96 offen offset:1024 lds\n"
"ds_read_b64_tr_b16 a[196:197], v9 offset:0\n"
"v_mfma_f32_16x16x32_bf16 a[24:27], a[168:171], a[136:139], a[24:27]\n"
"ds_read_b64_tr_b16 a[198:199], v9 offset:2048\n"
"v_mul_f32 v37, v6, v37\n"
"v_mfma_f32_16x16x32_bf16 a[24:27], a[164:167], a[120:123], a[24:27]\n"
"ds_read_b64_tr_b16 a[200:201], v9 offset:4096\n"
"v_mul_f32 v38, v6, v38\n"
"v_mfma_f32_16x16x32_bf16 a[28:31], a[164:167], a[124:127], 0\n"
"s_add_u32 s99, s33, 262144\n"
"ds_read_b64_tr_b16 a[202:203], v9 offset:6144\n"
"v_mul_f32 v39, v6, v39\n"
"v_mfma_f32_16x16x32_bf16 a[28:31], a[168:171], a[140:143], a[28:31]\n"
"ds_read_b64_tr_b16 a[204:205], v9 offset:8192\n"
"v_cvt_pk_bf16_f32 v20, v36, v37\n"
"v_mfma_f32_16x16x32_bf16 a[28:31], a[172:175], v[52:55], a[28:31]\n"
"buffer_store_dwordx4 v[28:31], v0, s[64:67], s99 offen offset:3072 sc1\n"
"ds_read_b64_tr_b16 a[206:207], v9 offset:10240\n"
"v_mfma_f32_16x16x32_bf16 a[28:31], a[176:179], v[68:71], a[28:31]\n"
"ds_read_b64_tr_b16 a[208:209], v9 offset:12288\n"
"v_cvt_pk_bf16_f32 v21, v38, v39\n"
"v_mfma_f32_16x16x32_bf16 a[32:35], a[176:179], v[72:75], 0\n"
"ds_read_b64_tr_b16 a[210:211], v9 offset:14336\n"
"v_accvgpr_read_b32 v36, a76\n"
"v_mfma_f32_16x16x32_bf16 a[32:35], a[172:175], v[56:59], a[32:35]\n"
"v_accvgpr_read_b32 v37, a77\n"
"v_accvgpr_read_b32 v38, a78\n"
"v_mfma_f32_16x16x32_bf16 a[32:35], a[168:171], v[40:43], a[32:35]\n"
"s_add_u32 s94, s36, 112640\n"
"v_accvgpr_read_b32 v39, a79\n"
"v_mul_f32 v36, v6, v36\n"
"v_mfma_f32_16x16x32_bf16 a[32:35], a[164:167], a[128:131], a[32:35]\n"
"s_waitcnt lgkmcnt(6)\n"
"v_mul_f32 v37, v6, v37\n"
"v_mul_f32 v38, v6, v38\n"
"v_mfma_f32_16x16x32_bf16 a[48:51], a[196:199], a[128:131], 0\n"
"s_waitcnt lgkmcnt(4)\n"
"buffer_load_dwordx4 v13, s[60:63], s94 offen offset:2048 lds\n"
"v_mul_f32 v39, v6, v39\n"
"v_mfma_f32_16x16x32_bf16 a[48:51], a[200:203], v[40:43], a[48:51]\n"
"s_waitcnt lgkmcnt(2)\n"
"v_cvt_pk_bf16_f32 v22, v36, v37\n"
"v_cvt_pk_bf16_f32 v23, v38, v39\n"
"v_mfma_f32_16x16x32_bf16 a[48:51], a[204:207], v[56:59], a[48:51]\n"
"s_waitcnt lgkmcnt(0)\n"
"v_accvgpr_read_b32 v36, a56\n"
"v_accvgpr_read_b32 v37, a57\n"
"v_mfma_f32_16x16x32_bf16 a[48:51], a[208:211], v[72:75], a[48:51]\n"
"s_add_u32 s95, s36, 119808\n"
"v_accvgpr_read_b32 v38, a58\n"
"v_accvgpr_read_b32 v39, a59\n"
"v_mfma_f32_16x16x32_bf16 a[44:47], a[208:211], v[68:71], 0\n"
"ds_read_b64_tr_b16 a[176:177], v8 offset:16384\n"
"v_mul_f32 v36, v5, v36\n"
"v_mfma_f32_16x16x32_bf16 a[44:47], a[204:207], v[52:55], a[44:47]\n"
"buffer_load_dwordx4 v13, s[60:63], s95 offen offset:3072 lds\n"
"ds_read_b64_tr_b16 a[178:179], v8 offset:18432\n"
"v_mfma_f32_16x16x32_bf16 a[44:47], a[200:203], a[140:143], a[44:47]\n"
"ds_read_b64_tr_b16 a[172:173], v8 offset:20480\n"
"v_mul_f32 v37, v5, v37\n"
"v_mfma_f32_16x16x32_bf16 a[44:47], a[196:199], a[124:127], a[44:47]\n"
"ds_read_b64_tr_b16 a[174:175], v8 offset:22528\n"
"v_mul_f32 v38, v5, v38\n"
"v_mfma_f32_16x16x32_bf16 a[40:43], a[196:199], a[120:123], 0\n"
"ds_read_b64_tr_b16 a[168:169], v8 offset:24576\n"
"v_mul_f32 v39, v5, v39\n"
"v_mfma_f32_16x16x32_bf16 a[40:43], a[200:203], a[136:139], a[40:43]\n"
"ds_read_b64_tr_b16 a[170:171], v8 offset:26624\n"
"v_cvt_pk_bf16_f32 v28, v36, v37\n"
"v_mfma_f32_16x16x32_bf16 a[40:43], a[204:207], v[48:51], a[40:43]\n"
"buffer_store_dwordx4 v[20:23], v0, s[64:67], s99 offen offset:2048 sc1\n"
"ds_read_b64_tr_b16 a[164:165], v8 offset:28672\n"
"v_mfma_f32_16x16x32_bf16 a[40:43], a[208:211], v[64:67], a[40:43]\n"
"ds_read_b64_tr_b16 a[166:167], v8 offset:30720\n"
"v_cvt_pk_bf16_f32 v29, v38, v39\n"
"v_mfma_f32_16x16x32_bf16 a[36:39], a[208:211], v[60:63], 0\n"
"v_accvgpr_read_b32 v36, a72\n"
"v_lshlrev_b32 v1, 3, v0\n"
"v_mfma_f32_16x16x32_bf16 a[36:39], a[204:207], v[44:47], a[36:39]\n"
"s_add_u32 m0, s43, -32768\n"
"v_accvgpr_read_b32 v37, a73\n"
"v_accvgpr_read_b32 v38, a74\n"
"v_mfma_f32_16x16x32_bf16 a[36:39], a[200:203], a[132:135], a[36:39]\n"
"s_add_u32 s96, s36, 229376\n"
"v_accvgpr_read_b32 v39, a75\n"
"v_mul_f32 v36, v5, v36\n"
"v_mfma_f32_16x16x32_bf16 a[36:39], a[196:199], a[116:119], a[36:39]\n"
"s_waitcnt lgkmcnt(6)\n"
"buffer_load_dwordx4 v13, s[60:63], s96 offen offset:0 lds\n"
"v_and_b32 v3, 0x200, v1\n"
"v_mfma_f32_16x16x32_bf16 a[20:23], a[176:179], v[76:79], a[20:23]\n"
"s_waitcnt lgkmcnt(4)\n"
"v_mul_f32 v37, v5, v37\n"
"v_mul_f32 v38, v5, v38\n"
"v_mfma_f32_16x16x32_bf16 a[20:23], a[172:175], v[92:95], a[20:23]\n"
"s_waitcnt lgkmcnt(2)\n"
"v_mul_f32 v39, v5, v39\n"
"v_cvt_pk_bf16_f32 v30, v36, v37\n"
"v_mfma_f32_16x16x32_bf16 a[20:23], a[168:171], v[108:111], a[20:23]\n"
"s_waitcnt lgkmcnt(0)\n"
"s_add_u32 s94, s36, 236544\n"
"v_cvt_pk_bf16_f32 v31, v38, v39\n"
"v_accvgpr_read_b32 v36, a52\n"
"v_mfma_f32_16x16x32_bf16 a[20:23], a[164:167], v[124:127], a[20:23]\n"
"v_accvgpr_read_b32 v37, a53\n"
"v_accvgpr_read_b32 v38, a54\n"
"v_mfma_f32_16x16x32_bf16 a[24:27], a[164:167], v[128:131], a[24:27]\n"
"buffer_load_dwordx4 v13, s[60:63], s94 offen offset:1024 lds\n"
"ds_read_b64_tr_b16 a[208:209], v9 offset:16384\n"
"v_mfma_f32_16x16x32_bf16 a[24:27], a[168:171], v[112:115], a[24:27]\n"
"ds_read_b64_tr_b16 a[210:211], v9 offset:18432\n"
"v_accvgpr_read_b32 v39, a55\n"
"v_mfma_f32_16x16x32_bf16 a[24:27], a[172:175], v[96:99], a[24:27]\n"
"ds_read_b64_tr_b16 a[204:205], v9 offset:20480\n"
"v_mul_f32 v36, v4, v36\n"
"v_mfma_f32_16x16x32_bf16 a[24:27], a[176:179], v[80:83], a[24:27]\n"
"ds_read_b64_tr_b16 a[206:207], v9 offset:22528\n"
"v_and_b32 v1, 0x180, v1\n"
"v_mfma_f32_16x16x32_bf16 a[28:31], a[176:179], v[84:87], a[28:31]\n"
"ds_read_b64_tr_b16 a[200:201], v9 offset:24576\n"
"v_mul_f32 v37, v4, v37\n"
"v_mfma_f32_16x16x32_bf16 a[28:31], a[172:175], v[100:103], a[28:31]\n"
"buffer_store_dwordx4 v[28:31], v0, s[64:67], s99 offen offset:1024 sc1\n"
"ds_read_b64_tr_b16 a[202:203], v9 offset:26624\n"
"v_mfma_f32_16x16x32_bf16 a[28:31], a[168:171], v[116:119], a[28:31]\n"
"ds_read_b64_tr_b16 a[196:197], v9 offset:28672\n"
"v_mul_f32 v38, v4, v38\n"
"v_mfma_f32_16x16x32_bf16 a[28:31], a[164:167], v[132:135], a[28:31]\n"
"ds_read_b64_tr_b16 a[198:199], v9 offset:30720\n"
"v_mul_f32 v39, v4, v39\n"
"v_mfma_f32_16x16x32_bf16 a[32:35], a[164:167], v[136:139], a[32:35]\n"
"v_cvt_pk_bf16_f32 v20, v36, v37\n"
"v_lshl_add_u32 v1, v3, 5, v1\n"
"v_mfma_f32_16x16x32_bf16 a[32:35], a[168:171], v[120:123], a[32:35]\n"
"s_add_u32 s95, s36, 243712\n"
"v_cvt_pk_bf16_f32 v21, v38, v39\n"
"v_accvgpr_read_b32 v36, a68\n"
"v_mfma_f32_16x16x32_bf16 a[32:35], a[172:175], v[104:107], a[32:35]\n"
"v_accvgpr_read_b32 v37, a69\n"
"v_accvgpr_read_b32 v38, a70\n"
"v_mfma_f32_16x16x32_bf16 a[32:35], a[176:179], v[88:91], a[32:35]\n"
"s_waitcnt lgkmcnt(6)\n"
"buffer_load_dwordx4 v13, s[60:63], s95 offen offset:2048 lds\n"
"v_accvgpr_read_b32 v39, a71\n"
"v_mfma_f32_16x16x32_bf16 a[48:51], a[208:211], v[88:91], a[48:51]\n"
"s_waitcnt lgkmcnt(4)\n"
"v_mul_f32 v36, v4, v36\n"
"v_mul_f32 v37, v4, v37\n"
"v_mfma_f32_16x16x32_bf16 a[48:51], a[204:207], v[104:107], a[48:51]\n"
"s_waitcnt lgkmcnt(2)\n"
"ds_read_b64_tr_b16 a[164:165], v8 offset:32768\n"
"v_mul_f32 v38, v4, v38\n"
"v_mfma_f32_16x16x32_bf16 a[48:51], a[200:203], v[120:123], a[48:51]\n"
"s_waitcnt lgkmcnt(1)\n"
"s_add_u32 s96, s36, 250880\n"
"ds_read_b64_tr_b16 a[166:167], v8 offset:34816\n"
"v_mul_f32 v39, v4, v39\n"
"v_mfma_f32_16x16x32_bf16 a[48:51], a[196:199], v[136:139], a[48:51]\n"
"ds_read_b64_tr_b16 a[168:169], v8 offset:36864\n"
"v_cvt_pk_bf16_f32 v22, v36, v37\n"
"v_mfma_f32_16x16x32_bf16 a[44:47], a[196:199], v[132:135], a[44:47]\n"
"buffer_load_dwordx4 v13, s[60:63], s96 offen offset:3072 lds\n"
"ds_read_b64_tr_b16 a[170:171], v8 offset:38912\n"
"v_mfma_f32_16x16x32_bf16 a[44:47], a[200:203], v[116:119], a[44:47]\n"
"ds_read_b64_tr_b16 a[172:173], v8 offset:40960\n"
"v_cvt_pk_bf16_f32 v23, v38, v39\n"
"v_mfma_f32_16x16x32_bf16 a[44:47], a[204:207], v[100:103], a[44:47]\n"
"ds_read_b64_tr_b16 a[174:175], v8 offset:43008\n"
"v_mfma_f32_16x16x32_bf16 a[44:47], a[208:211], v[84:87], a[44:47]\n"
"ds_read_b64_tr_b16 a[176:177], v8 offset:45056\n"
"v_mfma_f32_16x16x32_bf16 a[40:43], a[208:211], v[80:83], a[40:43]\n"
"ds_read_b64_tr_b16 a[178:179], v8 offset:47104\n"
"v_mfma_f32_16x16x32_bf16 a[40:43], a[204:207], v[96:99], a[40:43]\n"
"buffer_store_dwordx4 v[20:23], v0, s[64:67], s99 offen offset:0 sc1\n"
"v_mfma_f32_16x16x32_bf16 a[40:43], a[200:203], v[112:115], a[40:43]\n"
"v_mfma_f32_16x16x32_bf16 a[40:43], a[196:199], v[128:131], a[40:43]\n"
"v_mfma_f32_16x16x32_bf16 a[36:39], a[196:199], v[124:127], a[36:39]\n"
"s_add_u32 m0, s43, -16384\n"
"ds_read_b64_tr_b16 a[196:197], v9 offset:32768\n"
"v_mfma_f32_16x16x32_bf16 a[36:39], a[200:203], v[108:111], a[36:39]\n"
"s_add_u32 s94, s36, 360448\n"
"ds_read_b64_tr_b16 a[198:199], v9 offset:34816\n"
"v_mfma_f32_16x16x32_bf16 a[36:39], a[204:207], v[92:95], a[36:39]\n"
"buffer_load_dwordx4 v13, s[60:63], s94 offen offset:0 lds\n"
"ds_read_b64_tr_b16 a[200:201], v9 offset:36864\n"
"v_mfma_f32_16x16x32_bf16 a[36:39], a[208:211], v[76:79], a[36:39]\n"
"s_waitcnt lgkmcnt(8)\n"
"ds_read_b64_tr_b16 a[202:203], v9 offset:38912\n"
"v_mfma_f32_16x16x32_bf16 a[20:23], a[164:167], v[140:143], a[20:23]\n"
"s_waitcnt lgkmcnt(8)\n"
"ds_read_b64_tr_b16 a[204:205], v9 offset:40960\n"
"v_mfma_f32_16x16x32_bf16 a[20:23], a[168:171], v[156:159], a[20:23]\n"
"s_waitcnt lgkmcnt(7)\n"
"s_add_u32 s95, s36, 367616\n"
"ds_read_b64_tr_b16 a[206:207], v9 offset:43008\n"
"v_mfma_f32_16x16x32_bf16 a[20:23], a[172:175], v[172:175], a[20:23]\n"
"s_waitcnt lgkmcnt(6)\n"
"ds_read_b64_tr_b16 a[208:209], v9 offset:45056\n"
"v_mfma_f32_16x16x32_bf16 a[20:23], a[176:179], v[188:191], a[20:23]\n"
"buffer_load_dwordx4 v13, s[60:63], s95 offen offset:1024 lds\n"
"ds_read_b64_tr_b16 a[210:211], v9 offset:47104\n"
"v_mfma_f32_16x16x32_bf16 a[24:27], a[176:179], v[192:195], a[24:27]\n"
"v_mfma_f32_16x16x32_bf16 a[24:27], a[172:175], v[176:179], a[24:27]\n"
"v_mfma_f32_16x16x32_bf16 a[24:27], a[168:171], v[160:163], a[24:27]\n"
"v_mfma_f32_16x16x32_bf16 a[24:27], a[164:167], v[144:147], a[24:27]\n"
"s_add_u32 s96, s36, 374784\n"
"v_mfma_f32_16x16x32_bf16 a[28:31], a[164:167], v[148:151], a[28:31]\n"
"v_mfma_f32_16x16x32_bf16 a[28:31], a[168:171], v[164:167], a[28:31]\n"
"buffer_load_dwordx4 v13, s[60:63], s96 offen offset:2048 lds\n"
"v_mfma_f32_16x16x32_bf16 a[28:31], a[172:175], v[180:183], a[28:31]\n"
"v_mfma_f32_16x16x32_bf16 a[28:31], a[176:179], v[196:199], a[28:31]\n"
"v_mfma_f32_16x16x32_bf16 a[32:35], a[176:179], v[200:203], a[32:35]\n"
"s_add_u32 s94, s36, 381952\n"
"ds_read_b64_tr_b16 a[176:177], v10 offset:0\n"
"v_mfma_f32_16x16x32_bf16 a[32:35], a[172:175], v[184:187], a[32:35]\n"
"ds_read_b64_tr_b16 a[178:179], v10 offset:2048\n"
"v_mfma_f32_16x16x32_bf16 a[32:35], a[168:171], v[168:171], a[32:35]\n"
"buffer_load_dwordx4 v13, s[60:63], s94 offen offset:3072 lds\n"
"ds_read_b64_tr_b16 a[172:173], v10 offset:4096\n"
"v_mfma_f32_16x16x32_bf16 a[32:35], a[164:167], v[152:155], a[32:35]\n"
"s_waitcnt lgkmcnt(4)\n"
"ds_read_b64_tr_b16 a[174:175], v10 offset:6144\n"
"v_mfma_f32_16x16x32_bf16 a[48:51], a[196:199], v[152:155], a[48:51]\n"
"ds_read_b64_tr_b16 a[168:169], v10 offset:8192\n"
"v_mfma_f32_16x16x32_bf16 a[48:51], a[200:203], v[168:171], a[48:51]\n"
"s_add_u32 m0, s43, 0\n"
"ds_read_b64_tr_b16 a[170:171], v10 offset:10240\n"
"v_mfma_f32_16x16x32_bf16 a[48:51], a[204:207], v[184:187], a[48:51]\n"
"s_waitcnt lgkmcnt(6)\n"
"s_add_u32 s95, s36, 491520\n"
"ds_read_b64_tr_b16 a[164:165], v10 offset:12288\n"
"v_mfma_f32_16x16x32_bf16 a[48:51], a[208:211], v[200:203], a[48:51]\n"
"buffer_load_dwordx4 v13, s[60:63], s95 offen offset:0 lds\n"
"ds_read_b64_tr_b16 a[166:167], v10 offset:14336\n"
"v_mfma_f32_16x16x32_bf16 a[44:47], a[208:211], v[196:199], a[44:47]\n"
"v_mfma_f32_16x16x32_bf16 a[44:47], a[204:207], v[180:183], a[44:47]\n"
"v_mfma_f32_16x16x32_bf16 a[44:47], a[200:203], v[164:167], a[44:47]\n"
"s_add_u32 s96, s36, 498688\n"
"v_mfma_f32_16x16x32_bf16 a[44:47], a[196:199], v[148:151], a[44:47]\n"
"v_mfma_f32_16x16x32_bf16 a[40:43], a[196:199], v[144:147], a[40:43]\n"
"buffer_load_dwordx4 v13, s[60:63], s96 offen offset:1024 lds\n"
"v_mfma_f32_16x16x32_bf16 a[40:43], a[200:203], v[160:163], a[40:43]\n"
"v_mfma_f32_16x16x32_bf16 a[40:43], a[204:207], v[176:179], a[40:43]\n"
"v_mfma_f32_16x16x32_bf16 a[40:43], a[208:211], v[192:195], a[40:43]\n"
"s_add_u32 s94, s36, 505856\n"
"v_mfma_f32_16x16x32_bf16 a[36:39], a[208:211], v[188:191], a[36:39]\n"
"ds_read_b64_tr_b16 a[208:209], v11 offset:0\n"
"v_mfma_f32_16x16x32_bf16 a[36:39], a[204:207], v[172:175], a[36:39]\n"
"buffer_load_dwordx4 v13, s[60:63], s94 offen offset:2048 lds\n"
"ds_read_b64_tr_b16 a[210:211], v11 offset:2048\n"
"v_mfma_f32_16x16x32_bf16 a[36:39], a[200:203], v[156:159], a[36:39]\n"
"ds_read_b64_tr_b16 a[204:205], v11 offset:4096\n"
"v_mfma_f32_16x16x32_bf16 a[36:39], a[196:199], v[140:143], a[36:39]\n"
"s_waitcnt lgkmcnt(4)\n"
"ds_read_b64_tr_b16 a[206:207], v11 offset:6144\n"
"v_mfma_f32_16x16x32_bf16 a[52:55], a[176:179], a[116:119], 0\n"
"ds_read_b64_tr_b16 a[200:201], v11 offset:8192\n"
"v_mfma_f32_16x16x32_bf16 a[52:55], a[172:175], a[132:135], a[52:55]\n"
"s_add_u32 s25, s35, 0\n"
"ds_read_b64_tr_b16 a[202:203], v11 offset:10240\n"
"v_mfma_f32_16x16x32_bf16 a[52:55], a[168:171], v[44:47], a[52:55]\n"
"s_waitcnt lgkmcnt(6)\n"
"ds_read_b64_tr_b16 a[196:197], v11 offset:12288\n"
"v_mfma_f32_16x16x32_bf16 a[52:55], a[164:167], v[60:63], a[52:55]\n"
"buffer_load_dword v2, v1, s[64:67], s25 offen sc1\n"
"ds_read_b64_tr_b16 a[198:199], v11 offset:14336\n"
"v_mfma_f32_16x16x32_bf16 a[56:59], a[164:167], v[64:67], 0\n"
"v_mfma_f32_16x16x32_bf16 a[56:59], a[168:171], v[48:51], a[56:59]\n"
"v_mfma_f32_16x16x32_bf16 a[56:59], a[172:175], a[136:139], a[56:59]\n"
"s_add_u32 s95, s36, 513024\n"
"v_mfma_f32_16x16x32_bf16 a[56:59], a[176:179], a[120:123], a[56:59]\n"
"v_mfma_f32_16x16x32_bf16 a[60:63], a[176:179], a[124:127], 0\n"
"buffer_load_dwordx4 v13, s[60:63], s95 offen offset:3072 lds\n"
"v_mfma_f32_16x16x32_bf16 a[60:63], a[172:175], a[140:143], a[60:63]\n"
"v_mfma_f32_16x16x32_bf16 a[60:63], a[168:171], v[52:55], a[60:63]\n"
"v_mfma_f32_16x16x32_bf16 a[60:63], a[164:167], v[68:71], a[60:63]\n"
"v_mfma_f32_16x16x32_bf16 a[64:67], a[164:167], v[72:75], 0\n"
"ds_read_b64_tr_b16 a[164:165], v8 offset:49152\n"
"v_mfma_f32_16x16x32_bf16 a[64:67], a[168:171], v[56:59], a[64:67]\n"
"ds_read_b64_tr_b16 a[166:167], v8 offset:51200\n"
"v_mfma_f32_16x16x32_bf16 a[64:67], a[172:175], v[40:43], a[64:67]\n"
"ds_read_b64_tr_b16 a[168:169], v8 offset:53248\n"
"v_mfma_f32_16x16x32_bf16 a[64:67], a[176:179], a[128:131], a[64:67]\n"
"s_waitcnt lgkmcnt(4)\n"
"ds_read_b64_tr_b16 a[170:171], v8 offset:55296\n"
"v_mfma_f32_16x16x32_bf16 a[80:83], a[208:211], a[128:131], 0\n"
"buffer_load_dwordx4 a[128:131], v0, s[64:67], s98 offen offset:1024 sc1\n"
"ds_read_b64_tr_b16 a[172:173], v8 offset:57344\n"
"v_mfma_f32_16x16x32_bf16 a[80:83], a[204:207], v[40:43], a[80:83]\n"
"ds_read_b64_tr_b16 a[174:175], v8 offset:59392\n"
"v_mfma_f32_16x16x32_bf16 a[80:83], a[200:203], v[56:59], a[80:83]\n"
"s_waitcnt lgkmcnt(6)\n"
"ds_read_b64_tr_b16 a[176:177], v8 offset:61440\n"
"v_mfma_f32_16x16x32_bf16 a[80:83], a[196:199], v[72:75], a[80:83]\n"
"ds_read_b64_tr_b16 a[178:179], v8 offset:63488\n"
"v_mfma_f32_16x16x32_bf16 a[76:79], a[196:199], v[68:71], 0\n"
"v_mfma_f32_16x16x32_bf16 a[76:79], a[200:203], v[52:55], a[76:79]\n"
"buffer_load_dwordx4 v[40:43], v0, s[64:67], s98 offen offset:2048 sc1\n"
"v_mfma_f32_16x16x32_bf16 a[76:79], a[204:207], a[140:143], a[76:79]\n"
"v_mfma_f32_16x16x32_bf16 a[76:79], a[208:211], a[124:127], a[76:79]\n"
"v_mfma_f32_16x16x32_bf16 a[72:75], a[208:211], a[120:123], 0\n"
"s_waitcnt vmcnt(11)\n"
"v_mfma_f32_16x16x32_bf16 a[72:75], a[204:207], a[136:139], a[72:75]\n"
"s_add_u32 s24, s34, 4096\n"
"v_mfma_f32_16x16x32_bf16 a[72:75], a[200:203], v[48:51], a[72:75]\n"
"buffer_store_dword v12, v14, s[64:67], s24 offen sc1\n"
"v_mfma_f32_16x16x32_bf16 a[72:75], a[196:199], v[64:67], a[72:75]\n"
"v_mfma_f32_16x16x32_bf16 a[68:71], a[196:199], v[60:63], 0\n"
"ds_read_b64_tr_b16 a[196:197], v9 offset:49152\n"
"v_mfma_f32_16x16x32_bf16 a[68:71], a[200:203], v[44:47], a[68:71]\n"
"ds_read_b64_tr_b16 a[198:199], v9 offset:51200\n"
"v_mfma_f32_16x16x32_bf16 a[68:71], a[204:207], a[132:135], a[68:71]\n"
"ds_read_b64_tr_b16 a[200:201], v9 offset:53248\n"
"v_mfma_f32_16x16x32_bf16 a[68:71], a[208:211], a[116:119], a[68:71]\n"
"s_waitcnt lgkmcnt(3)\n"
"ds_read_b64_tr_b16 a[202:203], v9 offset:55296\n"
"v_mfma_f32_16x16x32_bf16 a[20:23], a[164:167], v[204:207], a[20:23]\n"
"buffer_load_dwordx4 v[56:59], v0, s[64:67], s98 offen offset:3072 sc1\n"
"ds_read_b64_tr_b16 a[204:205], v9 offset:57344\n"
"v_mfma_f32_16x16x32_bf16 a[20:23], a[168:171], v[220:223], a[20:23]\n"
"ds_read_b64_tr_b16 a[206:207], v9 offset:59392\n"
"v_mfma_f32_16x16x32_bf16 a[20:23], a[172:175], v[236:239], a[20:23]\n"
"ds_read_b64_tr_b16 a[208:209], v9 offset:61440\n"
"v_mfma_f32_16x16x32_bf16 a[20:23], a[176:179], v[252:255], a[20:23]\n"
"s_xor_b32 s98, s30, 266240\n"
"ds_read_b64_tr_b16 a[210:211], v9 offset:63488\n"
"v_mfma_f32_16x16x32_bf16 a[24:27], a[176:179], v[24:27], a[24:27]\n"
"v_mfma_f32_16x16x32_bf16 a[24:27], a[172:175], v[240:243], a[24:27]\n"
"buffer_load_dwordx4 v[72:75], v0, s[64:67], s98 offen offset:0 sc1\n"
"v_mfma_f32_16x16x32_bf16 a[24:27], a[168:171], v[224:227], a[24:27]\n"
"v_mfma_f32_16x16x32_bf16 a[24:27], a[164:167], v[208:211], a[24:27]\n"
"v_mfma_f32_16x16x32_bf16 a[28:31], a[164:167], v[212:215], a[28:31]\n"
"v_mfma_f32_16x16x32_bf16 a[28:31], a[168:171], v[228:231], a[28:31]\n"
"v_mfma_f32_16x16x32_bf16 a[28:31], a[172:175], v[244:247], a[28:31]\n"
"buffer_load_dwordx4 v[68:71], v0, s[64:67], s98 offen offset:1024 sc1\n"
"v_mfma_f32_16x16x32_bf16 a[28:31], a[176:179], v[32:35], a[28:31]\n"
"v_mfma_f32_16x16x32_bf16 a[32:35], a[176:179], v[16:19], a[32:35]\n"
"ds_read_b64_tr_b16 a[176:177], v10 offset:16384\n"
"v_mfma_f32_16x16x32_bf16 a[32:35], a[172:175], v[248:251], a[32:35]\n"
"ds_read_b64_tr_b16 a[178:179], v10 offset:18432\n"
"v_mfma_f32_16x16x32_bf16 a[32:35], a[168:171], v[232:235], a[32:35]\n"
"s_waitcnt lgkmcnt(4)\n"
"ds_read_b64_tr_b16 a[172:173], v10 offset:20480\n"
"v_mfma_f32_16x16x32_bf16 a[32:35], a[164:167], v[216:219], a[32:35]\n"
"buffer_load_dwordx4 v[52:55], v0, s[64:67], s98 offen offset:2048 sc1\n"
"ds_read_b64_tr_b16 a[174:175], v10 offset:22528\n"
"v_mfma_f32_16x16x32_bf16 a[48:51], a[196:199], v[216:219], a[48:51]\n"
"ds_read_b64_tr_b16 a[168:169], v10 offset:24576\n"
"v_mfma_f32_16x16x32_bf16 a[48:51], a[200:203], v[232:235], a[48:51]\n"
"ds_read_b64_tr_b16 a[170:171], v10 offset:26624\n"
"v_mfma_f32_16x16x32_bf16 a[48:51], a[204:207], v[248:251], a[48:51]\n"
"s_waitcnt lgkmcnt(6)\n"
"ds_read_b64_tr_b16 a[164:165], v10 offset:28672\n"
"v_mfma_f32_16x16x32_bf16 a[48:51], a[208:211], v[16:19], a[48:51]\n"
"ds_read_b64_tr_b16 a[166:167], v10 offset:30720\n"
"v_mfma_f32_16x16x32_bf16 a[44:47], a[208:211], v[32:35], a[44:47]\n"
"buffer_load_dwordx4 a[140:143], v0, s[64:67], s98 offen offset:3072 sc1\n"
"v_mfma_f32_16x16x32_bf16 a[44:47], a[204:207], v[244:247], a[44:47]\n"
"s_waitcnt vmcnt(9)\n"
"v_cmp_ne_u32 vcc, s92, v2\n"
"v_mfma_f32_16x16x32_bf16 a[44:47], a[200:203], v[228:231], a[44:47]\n"
"v_mfma_f32_16x16x32_bf16 a[44:47], a[196:199], v[212:215], a[44:47]\n"
"s_cbranch_vccnz .Lrt4\n"
".Lbk4:\n"
"v_mfma_f32_16x16x32_bf16 a[40:43], a[196:199], v[208:211], a[40:43]\n"
"s_xor_b32 s98, s31, 132096\n"
"v_accvgpr_read_b32 v36, a32\n"
"v_accvgpr_read_b32 v37, a33\n"
"v_mfma_f32_16x16x32_bf16 a[40:43], a[200:203], v[224:227], a[40:43]\n"
"buffer_load_dwordx4 a[124:127], v0, s[64:67], s98 offen offset:0 sc1\n"
"v_accvgpr_read_b32 v38, a34\n"
"v_mfma_f32_16x16x32_bf16 a[40:43], a[204:207], v[240:243], a[40:43]\n"
"s_xor_b32 s98, s31, 136192\n"
"v_accvgpr_read_b32 v39, a35\n"
"v_mul_f32 v36, v7, v36\n"
"v_mfma_f32_16x16x32_bf16 a[40:43], a[208:211], v[24:27], a[40:43]\n"
"v_mul_f32 v37, v7, v37\n"
"v_mul_f32 v38, v7, v38\n"
"v_mfma_f32_16x16x32_bf16 a[36:39], a[208:211], v[252:255], a[36:39]\n"
"buffer_load_dwordx4 a[120:123], v0, s[64:67], s98 offen offset:0 sc1\n"
"v_mul_f32 v39, v7, v39\n"
"v_mfma_f32_16x16x32_bf16 a[36:39], a[204:207], v[236:239], a[36:39]\n"
"v_cvt_pk_bf16_f32 v28, v36, v37\n"
"v_cvt_pk_bf16_f32 v29, v38, v39\n"
"v_mfma_f32_16x16x32_bf16 a[36:39], a[200:203], v[220:223], a[36:39]\n"
"s_xor_b32 s98, s31, 140288\n"
"v_accvgpr_read_b32 v36, a48\n"
"v_accvgpr_read_b32 v37, a49\n"
"v_mfma_f32_16x16x32_bf16 a[36:39], a[196:199], v[204:207], a[36:39]\n"
"s_waitcnt lgkmcnt(0)\n"
"v_accvgpr_read_b32 v38, a50\n"
"v_accvgpr_read_b32 v39, a51\n"
"v_mfma_f32_16x16x32_bf16 a[52:55], a[176:179], v[76:79], a[52:55]\n"
"buffer_load_dwordx4 a[136:139], v0, s[64:67], s98 offen offset:0 sc1\n"
"v_mul_f32 v36, v7, v36\n"
"v_mfma_f32_16x16x32_bf16 a[52:55], a[172:175], v[92:95], a[52:55]\n"
"s_xor_b32 s98, s31, 144384\n"
"v_mul_f32 v37, v7, v37\n"
"v_mul_f32 v38, v7, v38\n"
"v_mfma_f32_16x16x32_bf16 a[52:55], a[168:171], v[108:111], a[52:55]\n"
"v_mul_f32 v39, v7, v39\n"
"v_cvt_pk_bf16_f32 v30, v36, v37\n"
"v_mfma_f32_16x16x32_bf16 a[52:55], a[164:167], v[124:127], a[52:55]\n"
"buffer_load_dwordx4 v[48:51], v0, s[64:67], s98 offen offset:0 sc1\n"
"v_cvt_pk_bf16_f32 v31, v38, v39\n"
"v_mfma_f32_16x16x32_bf16 a[56:59], a[164:167], v[128:131], a[56:59]\n"
"v_accvgpr_read_b32 v36, a28\n"
"v_accvgpr_read_b32 v37, a29\n"
"v_mfma_f32_16x16x32_bf16 a[56:59], a[168:171], v[112:115], a[56:59]\n"
"s_add_u32 s99, s33, 524288\n"
"v_accvgpr_read_b32 v38, a30\n"
"v_accvgpr_read_b32 v39, a31\n"
"v_mfma_f32_16x16x32_bf16 a[56:59], a[172:175], v[96:99], a[56:59]\n"
"ds_read_b64_tr_b16 a[208:209], v11 offset:16384\n"
"v_mul_f32 v36, v6, v36\n"
"v_mfma_f32_16x16x32_bf16 a[56:59], a[176:179], v[80:83], a[56:59]\n"
"buffer_store_dwordx4 v[28:31], v0, s[64:67], s99 offen offset:3072 sc1\n"
"ds_read_b64_tr_b16 a[210:211], v11 offset:18432\n"
"v_mfma_f32_16x16x32_bf16 a[60:63], a[176:179], v[84:87], a[60:63]\n"
"ds_read_b64_tr_b16 a[204:205], v11 offset:20480\n"
"v_mul_f32 v37, v6, v37\n"
"v_mfma_f32_16x16x32_bf16 a[60:63], a[172:175], v[100:103], a[60:63]\n"
"s_xor_b32 s98, s31, 133120\n"
"ds_read_b64_tr_b16 a[206:207], v11 offset:22528\n"
"v_mul_f32 v38, v6, v38\n"
"v_mfma_f32_16x16x32_bf16 a[60:63], a[168:171], v[116:119], a[60:63]\n"
"ds_read_b64_tr_b16 a[200:201], v11 offset:24576\n"
"v_mul_f32 v39, v6, v39\n"
"v_mfma_f32_16x16x32_bf16 a[60:63], a[164:167], v[132:135], a[60:63]\n"
".Lpg9:\n"
"s_cbranch_execz .Lpgret0\n"
"buffer_load_dwordx4 v[64:67], v0, s[64:67], s98 offen offset:0 sc1\n"
"ds_read_b64_tr_b16 a[202:203], v11 offset:26624\n"
"v_mfma_f32_16x16x32_bf16 a[64:67], a[164:167], v[136:139], a[64:67]\n"
"s_xor_b32 s98, s31, 137216\n"
"ds_read_b64_tr_b16 a[196:197], v11 offset:28672\n"
"v_cvt_pk_bf16_f32 v20, v36, v37\n"
"v_mfma_f32_16x16x32_bf16 a[64:67], a[168:171], v[120:123], a[64:67]\n"
"ds_read_b64_tr_b16 a[198:199], v11 offset:30720\n"
"v_cvt_pk_bf16_f32 v21, v38, v39\n"
"v_mfma_f32_16x16x32_bf16 a[64:67], a[172:175], v[104:107], a[64:67]\n"
"buffer_load_dwordx4 v[60:63], v0, s[64:67], s98 offen offset:0 sc1\n"
"v_accvgpr_read_b32 v36, a44\n"
"v_mfma_f32_16x16x32_bf16 a[64:67], a[176:179], v[88:91], a[64:67]\n"
"s_waitcnt lgkmcnt(6)\n"
"v_accvgpr_read_b32 v37, a45\n"
"v_accvgpr_read_b32 v38, a46\n"
"v_mfma_f32_16x16x32_bf16 a[80:83], a[208:211], v[88:91], a[80:83]\n"
"s_waitcnt lgkmcnt(4)\n"
"s_xor_b32 s98, s31, 141312\n"
"v_accvgpr_read_b32 v39, a47\n"
"v_mul_f32 v36, v6, v36\n"
"v_mfma_f32_16x16x32_bf16 a[80:83], a[204:207], v[104:107], a[80:83]\n"
"s_waitcnt lgkmcnt(2)\n"
"v_mul_f32 v37, v6, v37\n"
"v_mul_f32 v38, v6, v38\n"
"v_mfma_f32_16x16x32_bf16 a[80:83], a[200:203], v[120:123], a[80:83]\n"
"s_waitcnt lgkmcnt(0)\n"
"buffer_load_dwordx4 v[44:47], v0, s[64:67], s98 offen offset:0 sc1\n"
"v_mul_f32 v39, v6, v39\n"
"v_mfma_f32_16x16x32_bf16 a[80:83], a[196:199], v[136:139], a[80:83]\n"
"v_cvt_pk_bf16_f32 v22, v36, v37\n"
"v_cvt_pk_bf16_f32 v23, v38, v39\n"
"v_mfma_f32_16x16x32_bf16 a[76:79], a[196:199], v[132:135], a[76:79]\n"
"s_xor_b32 s98, s31, 145408\n"
"v_accvgpr_read_b32 v36, a24\n"
"v_accvgpr_read_b32 v37, a25\n"
"v_mfma_f32_16x16x32_bf16 a[76:79], a[200:203], v[116:119], a[76:79]\n"
"v_accvgpr_read_b32 v38, a26\n"
"v_accvgpr_read_b32 v39, a27\n"
"v_mfma_f32_16x16x32_bf16 a[76:79], a[204:207], v[100:103], a[76:79]\n"
"buffer_load_dwordx4 a[132:135], v0, s[64:67], s98 offen offset:0 sc1\n"
"ds_read_b64_tr_b16 a[164:165], v10 offset:32768\n"
"v_mfma_f32_16x16x32_bf16 a[76:79], a[208:211], v[84:87], a[76:79]\n"
"ds_read_b64_tr_b16 a[166:167], v10 offset:34816\n"
"v_mul_f32 v36, v5, v36\n"
"v_mfma_f32_16x16x32_bf16 a[72:75], a[208:211], v[80:83], a[72:75]\n"
"ds_read_b64_tr_b16 a[168:169], v10 offset:36864\n"
"v_mul_f32 v37, v5, v37\n"
"v_mfma_f32_16x16x32_bf16 a[72:75], a[204:207], v[96:99], a[72:75]\n"
"buffer_store_dwordx4 v[20:23], v0, s[64:67], s99 offen offset:2048 sc1\n"
"ds_read_b64_tr_b16 a[170:171], v10 offset:38912\n"
"v_mfma_f32_16x16x32_bf16 a[72:75], a[200:203], v[112:115], a[72:75]\n"
"ds_read_b64_tr_b16 a[172:173], v10 offset:40960\n"
"v_mul_f32 v38, v5, v38\n"
"v_mfma_f32_16x16x32_bf16 a[72:75], a[196:199], v[128:131], a[72:75]\n"
"s_xor_b32 s98, s31, 134144\n"
"ds_read_b64_tr_b16 a[174:175], v10 offset:43008\n"
"v_mul_f32 v39, v5, v39\n"
"v_mfma_f32_16x16x32_bf16 a[68:71], a[196:199], v[124:127], a[68:71]\n"
"ds_read_b64_tr_b16 a[176:177], v10 offset:45056\n"
"v_cvt_pk_bf16_f32 v28, v36, v37\n"
"v_mfma_f32_16x16x32_bf16 a[68:71], a[200:203], v[108:111], a[68:71]\n"
"buffer_load_dwordx4 a[116:119], v0, s[64:67], s98 offen offset:0 sc1\n"
"ds_read_b64_tr_b16 a[178:179], v10 offset:47104\n"
"v_mfma_f32_16x16x32_bf16 a[68:71], a[204:207], v[92:95], a[68:71]\n"
"v_cvt_pk_bf16_f32 v29, v38, v39\n"
"v_accvgpr_read_b32 v36, a40\n"
"v_mfma_f32_16x16x32_bf16 a[68:71], a[208:211], v[76:79], a[68:71]\n"
"s_waitcnt lgkmcnt(6)\n"
"s_xor_b32 s98, s31, 138240\n"
"v_accvgpr_read_b32 v37, a41\n"
"v_accvgpr_read_b32 v38, a42\n"
"v_mfma_f32_16x16x32_bf16 a[52:55], a[164:167], v[140:143], a[52:55]\n"
"s_waitcnt lgkmcnt(4)\n"
"v_accvgpr_read_b32 v39, a43\n"
"v_mul_f32 v36, v5, v36\n"
"v_mfma_f32_16x16x32_bf16 a[52:55], a[168:171], v[156:159], a[52:55]\n"
"s_waitcnt lgkmcnt(2)\n"
"buffer_load_dwordx4 v[88:91], v0, s[64:67], s98 offen offset:0 sc1\n"
"v_mul_f32 v37, v5, v37\n"
"v_mfma_f32_16x16x32_bf16 a[52:55], a[172:175], v[172:175], a[52:55]\n"
"s_waitcnt lgkmcnt(0)\n"
"v_mul_f32 v38, v5, v38\n"
"v_mul_f32 v39, v5, v39\n"
"v_mfma_f32_16x16x32_bf16 a[52:55], a[176:179], v[188:191], a[52:55]\n"
"s_xor_b32 s98, s31, 142336\n"
"v_cvt_pk_bf16_f32 v30, v36, v37\n"
"v_cvt_pk_bf16_f32 v31, v38, v39\n"
"v_mfma_f32_16x16x32_bf16 a[56:59], a[176:179], v[192:195], a[56:59]\n"
"v_accvgpr_read_b32 v36, a20\n"
"v_accvgpr_read_b32 v37, a21\n"
"v_mfma_f32_16x16x32_bf16 a[56:59], a[172:175], v[176:179], a[56:59]\n"
"buffer_load_dwordx4 v[104:107], v0, s[64:67], s98 offen offset:0 sc1\n"
"v_accvgpr_read_b32 v38, a22\n"
"v_mfma_f32_16x16x32_bf16 a[56:59], a[168:171], v[160:163], a[56:59]\n"
"ds_read_b64_tr_b16 a[196:197], v11 offset:32768\n"
"v_accvgpr_read_b32 v39, a23\n"
"v_mfma_f32_16x16x32_bf16 a[56:59], a[164:167], v[144:147], a[56:59]\n"
"ds_read_b64_tr_b16 a[198:199], v11 offset:34816\n"
"v_mul_f32 v36, v4, v36\n"
"v_mfma_f32_16x16x32_bf16 a[60:63], a[164:167], v[148:151], a[60:63]\n"
"buffer_store_dwordx4 v[28:31], v0, s[64:67], s99 offen offset:1024 sc1\n"
"ds_read_b64_tr_b16 a[200:201], v11 offset:36864\n"
"v_mfma_f32_16x16x32_bf16 a[60:63], a[168:171], v[164:167], a[60:63]\n"
"ds_read_b64_tr_b16 a[202:203], v11 offset:38912\n"
"v_mul_f32 v37, v4, v37\n"
"v_mfma_f32_16x16x32_bf16 a[60:63], a[172:175], v[180:183], a[60:63]\n"
"s_xor_b32 s98, s31, 146432\n"
"ds_read_b64_tr_b16 a[204:205], v11 offset:40960\n"
"v_mul_f32 v38, v4, v38\n"
"v_mfma_f32_16x16x32_bf16 a[60:63], a[176:179], v[196:199], a[60:63]\n"
"ds_read_b64_tr_b16 a[206:207], v11 offset:43008\n"
"v_mul_f32 v39, v4, v39\n"
"v_mfma_f32_16x16x32_bf16 a[64:67], a[176:179], v[200:203], a[64:67]\n"
"buffer_load_dwordx4 v[136:139], v0, s[64:67], s98 offen offset:0 sc1\n"
"ds_read_b64_tr_b16 a[208:209], v11 offset:45056\n"
"v_mfma_f32_16x16x32_bf16 a[64:67], a[172:175], v[184:187], a[64:67]\n"
"s_andn2_b32 s8, 0x7ff, s13\n"
"ds_read_b64_tr_b16 a[210:211], v11 offset:47104\n"
"v_cvt_pk_bf16_f32 v20, v36, v37\n"
"v_mfma_f32_16x16x32_bf16 a[64:67], a[168:171], v[168:171], a[64:67]\n"
"s_xor_b32 s98, s31, 131072\n"
"v_cvt_pk_bf16_f32 v21, v38, v39\n"
"v_accvgpr_read_b32 v36, a36\n"
"v_mfma_f32_16x16x32_bf16 a[64:67], a[164:167], v[152:155], a[64:67]\n"
"s_waitcnt lgkmcnt(6)\n"
"s_lshr_b32 s8, s8, 4\n"
"v_accvgpr_read_b32 v37, a37\n"
"v_accvgpr_read_b32 v38, a38\n"
"v_mfma_f32_16x16x32_bf16 a[80:83], a[196:199], v[152:155], a[80:83]\n"
"s_waitcnt lgkmcnt(4)\n"
"s_xor_b32 s8, s8, s40\n"
"buffer_load_dwordx4 v[116:119], v0, s[64:67], s98 offen offset:0 sc1\n"
"v_accvgpr_read_b32 v39, a39\n"
"v_mfma_f32_16x16x32_bf16 a[80:83], a[200:203], v[168:171], a[80:83]\n"
"s_waitcnt lgkmcnt(2)\n"
"s_xor_b32 s98, s31, 135168\n"
"v_mul_f32 v36, v4, v36\n"
"v_mul_f32 v37, v4, v37\n"
"v_mfma_f32_16x16x32_bf16 a[80:83], a[204:207], v[184:187], a[80:83]\n"
"s_waitcnt lgkmcnt(0)\n"
"s_andn2_b32 s10, 0x7ff, s14\n"
"ds_read_b64_tr_b16 a[176:177], v10 offset:49152\n"
"v_mul_f32 v38, v4, v38\n"
"v_mfma_f32_16x16x32_bf16 a[80:83], a[208:211], v[200:203], a[80:83]\n"
"buffer_load_dwordx4 v[100:103], v0, s[64:67], s98 offen offset:0 sc1\n"
"ds_read_b64_tr_b16 a[178:179], v10 offset:51200\n"
"v_mfma_f32_16x16x32_bf16 a[76:79], a[208:211], v[196:199], a[76:79]\n"
"s_lshr_b32 s10, s10, 4\n"
"ds_read_b64_tr_b16 a[172:173], v10 offset:53248\n"
"v_mul_f32 v39, v4, v39\n"
"v_mfma_f32_16x16x32_bf16 a[76:79], a[204:207], v[180:183], a[76:79]\n"
"s_xor_b32 s98, s31, 139264\n"
"ds_read_b64_tr_b16 a[174:175], v10 offset:55296\n"
"v_cvt_pk_bf16_f32 v22, v36, v37\n"
"v_mfma_f32_16x16x32_bf16 a[76:79], a[200:203], v[164:167], a[76:79]\n"
"s_xor_b32 s10, s10, s40\n"
"ds_read_b64_tr_b16 a[168:169], v10 offset:57344\n"
"v_cvt_pk_bf16_f32 v23, v38, v39\n"
"v_mfma_f32_16x16x32_bf16 a[76:79], a[196:199], v[148:151], a[76:79]\n"
"buffer_load_dwordx4 v[84:87], v0, s[64:67], s98 offen offset:0 sc1\n"
"ds_read_b64_tr_b16 a[170:171], v10 offset:59392\n"
"v_mfma_f32_16x16x32_bf16 a[72:75], a[196:199], v[144:147], a[72:75]\n"
"s_min_u32 s9, s8, s10\n"
"ds_read_b64_tr_b16 a[164:165], v10 offset:61440\n"
"v_mfma_f32_16x16x32_bf16 a[72:75], a[200:203], v[160:163], a[72:75]\n"
"s_min_u32 s39, s9, 1\n"
"ds_read_b64_tr_b16 a[166:167], v10 offset:63488\n"
"v_mfma_f32_16x16x32_bf16 a[72:75], a[204:207], v[176:179], a[72:75]\n"
"s_lshl_b32 s39, s39, 28\n"
"v_mfma_f32_16x16x32_bf16 a[72:75], a[208:211], v[192:195], a[72:75]\n"
"buffer_store_dwordx4 v[20:23], v0, s[64:67], s99 offen offset:0 sc1\n"
"v_mfma_f32_16x16x32_bf16 a[68:71], a[208:211], v[188:191], a[68:71]\n"
"s_xor_b32 s98, s31, 143360\n"
"ds_read_b64_tr_b16 a[208:209], v11 offset:49152\n"
"v_mfma_f32_16x16x32_bf16 a[68:71], a[204:207], v[172:175], a[68:71]\n"
"ds_read_b64_tr_b16 a[210:211], v11 offset:51200\n"
"v_mfma_f32_16x16x32_bf16 a[68:71], a[200:203], v[156:159], a[68:71]\n"
"buffer_load_dwordx4 v[80:83], v0, s[64:67], s98 offen offset:0 sc1\n"
"ds_read_b64_tr_b16 a[204:205], v11 offset:53248\n"
"v_mfma_f32_16x16x32_bf16 a[68:71], a[196:199], v[140:143], a[68:71]\n"
"s_waitcnt lgkmcnt(7)\n"
"ds_read_b64_tr_b16 a[206:207], v11 offset:55296\n"
"v_mfma_f32_16x16x32_bf16 a[52:55], a[176:179], v[204:207], a[52:55]\n"
"s_xor_b32 s98, s31, 1024\n"
"v_mov_b32 v196, 0\n"
"v_mov_b32 v197, 0\n"
"v_mfma_f32_16x16x32_bf16 a[52:55], a[172:175], v[220:223], a[52:55]\n"
"s_waitcnt lgkmcnt(6)\n"
"v_mov_b32 v198, 0\n"
"v_mov_b32 v199, 0\n"
"v_mfma_f32_16x16x32_bf16 a[52:55], a[168:171], v[236:239], a[52:55]\n"
"s_waitcnt lgkmcnt(4)\n"
"buffer_load_dwordx4 v[96:99], v0, s[64:67], s98 offen offset:0 sc1\n"
"v_mov_b32 v200, 0\n"
"v_mfma_f32_16x16x32_bf16 a[52:55], a[164:167], v[252:255], a[52:55]\n"
"v_mov_b32 v201, 0\n"
"v_mov_b32 v202, 0\n"
"v_mfma_f32_16x16x32_bf16 a[56:59], a[164:167], v[24:27], a[56:59]\n"
"s_xor_b32 s98, s31, 5120\n"
"v_mov_b32 v203, 0\n"
"v_lshlrev_b32 v191, 3, v0\n"
"v_mfma_f32_16x16x32_bf16 a[56:59], a[168:171], v[240:243], a[56:59]\n"
"ds_read_b64_tr_b16 a[200:201], v11 offset:57344\n"
"v_min_u32 v191, 5376, v191\n"
"v_mfma_f32_16x16x32_bf16 a[56:59], a[172:175], v[224:227], a[56:59]\n"
"buffer_load_dwordx4 v[112:115], v0, s[64:67], s98 offen offset:0 sc1\n"
"ds_read_b64_tr_b16 a[202:203], v11 offset:59392\n"
"v_mfma_f32_16x16x32_bf16 a[56:59], a[176:179], v[208:211], a[56:59]\n"
"ds_read_b64_tr_b16 a[196:197], v11 offset:61440\n"
"v_add_u32 v191, s39, v191\n"
"v_mfma_f32_16x16x32_bf16 a[60:63], a[176:179], v[212:215], a[60:63]\n"
"s_xor_b32 s98, s31, 9216\n"
"ds_read_b64_tr_b16 a[198:199], v11 offset:63488\n"
"v_mfma_f32_16x16x32_bf16 a[60:63], a[172:175], v[228:231], a[60:63]\n"
"s_lshl_b32 s39, s91, 13\n"
"v_mfma_f32_16x16x32_bf16 a[60:63], a[168:171], v[244:247], a[60:63]\n"
"s_add_u32 s39, s39, 262144\n"
"buffer_load_dwordx4 v[108:111], v0, s[64:67], s98 offen offset:0 sc1\n"
"v_mfma_f32_16x16x32_bf16 a[60:63], a[164:167], v[32:35], a[60:63]\n"
"s_xor_b32 s98, s31, 13312\n"
"v_mfma_f32_16x16x32_bf16 a[64:67], a[164:167], v[16:19], a[64:67]\n"
"v_mfma_f32_16x16x32_bf16 a[64:67], a[168:171], v[248:251], a[64:67]\n"
"buffer_load_dwordx4 v[92:95], v0, s[64:67], s98 offen offset:0 sc1\n"
"v_mfma_f32_16x16x32_bf16 a[64:67], a[172:175], v[232:235], a[64:67]\n"
"v_mfma_f32_16x16x32_bf16 a[64:67], a[176:179], v[216:219], a[64:67]\n"
"s_waitcnt lgkmcnt(4)\n"
"v_mfma_f32_16x16x32_bf16 a[80:83], a[208:211], v[216:219], a[80:83]\n"
"s_waitcnt lgkmcnt(0)\n"
"v_mfma_f32_16x16x32_bf16 a[80:83], a[204:207], v[232:235], a[80:83]\n"
"s_barrier\n"
"buffer_load_dword v190, v191, s[64:67], s39 offen sc1\n"
"v_mfma_f32_16x16x32_bf16 a[80:83], a[200:203], v[248:251], a[80:83]\n"
"v_mfma_f32_16x16x32_bf16 a[80:83], a[196:199], v[16:19], a[80:83]\n"
"s_xor_b32 s98, s31, 2048\n"
"v_mfma_f32_16x16x32_bf16 a[76:79], a[196:199], v[32:35], a[76:79]\n"
"v_mfma_f32_16x16x32_bf16 a[76:79], a[200:203], v[244:247], a[76:79]\n"
"buffer_load_dwordx4 v[76:79], v0, s[64:67], s98 offen offset:0 sc1\n"
"v_mfma_f32_16x16x32_bf16 a[76:79], a[204:207], v[228:231], a[76:79]\n"
"v_mfma_f32_16x16x32_bf16 a[76:79], a[208:211], v[212:215], a[76:79]\n"
"v_mfma_f32_16x16x32_bf16 a[72:75], a[208:211], v[208:211], a[72:75]\n"
"v_accvgpr_read_b32 v36, a64\n"
"v_accvgpr_read_b32 v37, a65\n"
"v_mfma_f32_16x16x32_bf16 a[72:75], a[204:207], v[224:227], a[72:75]\n"
"v_accvgpr_read_b32 v38, a66\n"
"v_accvgpr_read_b32 v39, a67\n"
"v_mfma_f32_16x16x32_bf16 a[72:75], a[200:203], v[240:243], a[72:75]\n"
"v_mul_f32 v36, v7, v36\n"
"v_mul_f32 v37, v7, v37\n"
"v_mfma_f32_16x16x32_bf16 a[72:75], a[196:199], v[24:27], a[72:75]\n"
"v_mul_f32 v38, v7, v38\n"
"v_mul_f32 v39, v7, v39\n"
"v_mfma_f32_16x16x32_bf16 a[68:71], a[196:199], v[252:255], a[68:71]\n"
"v_cvt_pk_bf16_f32 v28, v36, v37\n"
"v_cvt_pk_bf16_f32 v29, v38, v39\n"
"v_mfma_f32_16x16x32_bf16 a[68:71], a[200:203], v[236:239], a[68:71]\n"
"v_accvgpr_read_b32 v36, a80\n"
"v_accvgpr_read_b32 v37, a81\n"
"v_mfma_f32_16x16x32_bf16 a[68:71], a[204:207], v[220:223], a[68:71]\n"
"v_accvgpr_read_b32 v38, a82\n"
"v_accvgpr_read_b32 v39, a83\n"
"v_mfma_f32_16x16x32_bf16 a[68:71], a[208:211], v[204:207], a[68:71]\n"
"s_waitcnt vmcnt(24)\n"
"v_mul_f32 v36, v7, v36\n"
"v_mul_f32 v37, v7, v37\n"
"v_mfma_f32_16x16x32_bf16 v[212:215], a[224:227], a[124:127], 0\n"
"s_xor_b32 s98, s31, 6144\n"
"v_mul_f32 v38, v7, v38\n"
"v_mul_f32 v39, v7, v39\n"
"v_mfma_f32_16x16x32_bf16 v[212:215], a[240:243], a[120:123], v[212:215]\n"
"s_waitcnt vmcnt(22)\n"
"buffer_load_dwordx4 a[196:199], v0, s[64:67], s98 offen offset:0 sc1\n"
"v_cvt_pk_bf16_f32 v30, v36, v37\n"
"v_mfma_f32_16x16x32_bf16 v[212:215], a[144:147], a[136:139], v[212:215]\n"
"s_add_u32 s25, s35, 4096\n"
"v_cvt_pk_bf16_f32 v31, v38, v39\n"
"v_accvgpr_read_b32 v36, a60\n"
"v_mfma_f32_16x16x32_bf16 v[212:215], a[192:195], v[48:51], v[212:215]\n"
"v_accvgpr_read_b32 v37, a61\n"
"v_accvgpr_read_b32 v38, a62\n"
"v_mfma_f32_16x16x32_bf16 v[228:231], a[188:191], v[48:51], 0\n"
"buffer_load_dword v2, v1, s[64:67], s25 offen sc1\n"
"v_accvgpr_read_b32 v39, a63\n"
"v_mfma_f32_16x16x32_bf16 v[228:231], a[252:255], a[136:139], v[228:231]\n"
"v_mul_f32 v36, v6, v36\n"
"v_mul_f32 v37, v6, v37\n"
"v_mfma_f32_16x16x32_bf16 v[228:231], a[236:239], a[120:123], v[228:231]\n"
"s_add_u32 s99, s33, 786432\n"
"v_mul_f32 v38, v6, v38\n"
"v_mul_f32 v39, v6, v39\n"
"v_mfma_f32_16x16x32_bf16 v[228:231], a[220:223], a[124:127], v[228:231]\n"
"v_cvt_pk_bf16_f32 v20, v36, v37\n"
"v_cvt_pk_bf16_f32 v21, v38, v39\n"
"v_mfma_f32_16x16x32_bf16 v[244:247], a[216:219], a[124:127], 0\n"
"buffer_store_dwordx4 v[28:31], v0, s[64:67], s99 offen offset:3072 sc1\n"
"v_accvgpr_read_b32 v36, a76\n"
"v_mfma_f32_16x16x32_bf16 v[244:247], a[232:235], a[120:123], v[244:247]\n"
"s_xor_b32 s98, s31, 10240\n"
"v_accvgpr_read_b32 v37, a77\n"
"v_accvgpr_read_b32 v38, a78\n"
"v_mfma_f32_16x16x32_bf16 v[244:247], a[248:251], a[136:139], v[244:247]\n"
"v_accvgpr_read_b32 v39, a79\n"
"v_mul_f32 v36, v6, v36\n"
"v_mfma_f32_16x16x32_bf16 v[244:247], a[184:187], v[48:51], v[244:247]\n"
"buffer_load_dwordx4 a[200:203], v0, s[64:67], s98 offen offset:0 sc1\n"
"v_mul_f32 v37, v6, v37\n"
"v_mfma_f32_16x16x32_bf16 v[32:35], a[180:183], v[48:51], 0\n"
"v_mul_f32 v38, v6, v38\n"
"v_mul_f32 v39, v6, v39\n"
"v_mfma_f32_16x16x32_bf16 v[32:35], a[244:247], a[136:139], v[32:35]\n"
"s_xor_b32 s98, s31, 14336\n"
"v_cvt_pk_bf16_f32 v22, v36, v37\n"
"v_cvt_pk_bf16_f32 v23, v38, v39\n"
"v_mfma_f32_16x16x32_bf16 v[32:35], a[228:231], a[120:123], v[32:35]\n"
"v_mfma_f32_16x16x32_bf16 v[32:35], a[212:215], a[124:127], v[32:35]\n"
"s_waitcnt vmcnt(24)\n"
"buffer_load_dwordx4 a[204:207], v0, s[64:67], s98 offen offset:0 sc1\n"
"v_accvgpr_read_b32 v36, a56\n"
"v_mfma_f32_16x16x32_bf16 v[208:211], a[212:215], v[64:67], 0\n"
"s_waitcnt vmcnt(23)\n"
"v_accvgpr_read_b32 v37, a57\n"
"v_accvgpr_read_b32 v38, a58\n"
"v_mfma_f32_16x16x32_bf16 v[208:211], a[228:231], v[60:63], v[208:211]\n"
"s_xor_b32 s98, s31, 3072\n"
"v_accvgpr_read_b32 v39, a59\n"
"v_mul_f32 v36, v5, v36\n"
"v_mfma_f32_16x16x32_bf16 v[208:211], a[244:247], v[44:47], v[208:211]\n"
"s_waitcnt vmcnt(22)\n"
"v_mul_f32 v37, v5, v37\n"
"v_mul_f32 v38, v5, v38\n"
"v_mfma_f32_16x16x32_bf16 v[208:211], a[180:183], a[132:135], v[208:211]\n"
"buffer_load_dwordx4 a[208:211], v0, s[64:67], s98 offen offset:0 sc1\n"
"v_mul_f32 v39, v5, v39\n"
"v_mfma_f32_16x16x32_bf16 v[224:227], a[184:187], a[132:135], 0\n"
"v_cvt_pk_bf16_f32 v28, v36, v37\n"
"v_cvt_pk_bf16_f32 v29, v38, v39\n"
"v_mfma_f32_16x16x32_bf16 v[224:227], a[248:251], v[44:47], v[224:227]\n"
"s_waitcnt vmcnt(13)\n"
"v_accvgpr_read_b32 v36, a72\n"
"v_accvgpr_read_b32 v37, a73\n"
"v_mfma_f32_16x16x32_bf16 v[224:227], a[232:235], v[60:63], v[224:227]\n"
"s_add_u32 s24, s34, 8192\n"
"v_accvgpr_read_b32 v38, a74\n"
"v_accvgpr_read_b32 v39, a75\n"
"v_mfma_f32_16x16x32_bf16 v[224:227], a[216:219], v[64:67], v[224:227]\n"
"buffer_store_dword v12, v14, s[64:67], s24 offen sc1\n"
"v_mul_f32 v36, v5, v36\n"
"v_mfma_f32_16x16x32_bf16 v[240:243], a[220:223], v[64:67], 0\n"
"s_cmp_eq_u32 s9, 0\n"
"s_cbranch_scc1 .Lyq\n.Lyqb:\n"
"v_mfma_f32_16x16x32_bf16 v[240:243], a[236:239], v[60:63], v[240:243]\n"
"v_mul_f32 v37, v5, v37\n"
"v_mul_f32 v38, v5, v38\n"
"v_mfma_f32_16x16x32_bf16 v[240:243], a[252:255], v[44:47], v[240:243]\n"
"buffer_store_dwordx4 v[20:23], v0, s[64:67], s99 offen offset:2048 sc1\n"
"v_mul_f32 v39, v5, v39\n"
"v_mfma_f32_16x16x32_bf16 v[240:243], a[188:191], a[132:135], v[240:243]\n"
"v_cvt_pk_bf16_f32 v30, v36, v37\n"
"v_cvt_pk_bf16_f32 v31, v38, v39\n"
"v_mfma_f32_16x16x32_bf16 v[24:27], a[192:195], a[132:135], 0\n"
"s_xor_b32 s98, s31, 7168\n"
"v_mfma_f32_16x16x32_bf16 v[24:27], a[144:147], v[44:47], v[24:27]\n"
"v_mfma_f32_16x16x32_bf16 v[24:27], a[240:243], v[60:63], v[24:27]\n"
"buffer_load_dwordx4 v[48:51], v0, s[64:67], s98 offen offset:0 sc1\n"
"v_mfma_f32_16x16x32_bf16 v[24:27], a[224:227], v[64:67], v[24:27]\n"
"v_mfma_f32_16x16x32_bf16 v[204:207], a[224:227], a[116:119], 0\n"
"s_xor_b32 s98, s31, 11264\n"
"v_accvgpr_read_b32 v36, a52\n"
"v_accvgpr_read_b32 v37, a53\n"
"v_mfma_f32_16x16x32_bf16 v[204:207], a[240:243], v[88:91], v[204:207]\n"
"v_accvgpr_read_b32 v38, a54\n"
"v_accvgpr_read_b32 v39, a55\n"
"v_mfma_f32_16x16x32_bf16 v[204:207], a[144:147], v[104:107], v[204:207]\n"
"buffer_load_dwordx4 a[136:139], v0, s[64:67], s98 offen offset:0 sc1\n"
"v_mul_f32 v36, v4, v36\n"
"v_mfma_f32_16x16x32_bf16 v[204:207], a[192:195], v[136:139], v[204:207]\n"
"s_xor_b32 s98, s31, 15360\n"
"v_mul_f32 v37, v4, v37\n"
"v_mul_f32 v38, v4, v38\n"
"v_mfma_f32_16x16x32_bf16 v[220:223], a[188:191], v[136:139], 0\n"
"v_mul_f32 v39, v4, v39\n"
"v_cvt_pk_bf16_f32 v20, v36, v37\n"
"v_mfma_f32_16x16x32_bf16 v[220:223], a[252:255], v[104:107], v[220:223]\n"
"buffer_load_dwordx4 a[120:123], v0, s[64:67], s98 offen offset:0 sc1\n"
"v_cvt_pk_bf16_f32 v21, v38, v39\n"
"v_mfma_f32_16x16x32_bf16 v[220:223], a[236:239], v[88:91], v[220:223]\n"
"v_accvgpr_read_b32 v36, a68\n"
"v_accvgpr_read_b32 v37, a69\n"
"v_mfma_f32_16x16x32_bf16 v[220:223], a[220:223], a[116:119], v[220:223]\n"
"v_accvgpr_read_b32 v38, a70\n"
"v_accvgpr_read_b32 v39, a71\n"
"v_mfma_f32_16x16x32_bf16 v[236:239], a[216:219], a[116:119], 0\n"
"v_mul_f32 v36, v4, v36\n"
"v_mul_f32 v37, v4, v37\n"
"v_mfma_f32_16x16x32_bf16 v[236:239], a[232:235], v[88:91], v[236:239]\n"
"buffer_store_dwordx4 v[28:31], v0, s[64:67], s99 offen offset:1024 sc1\n"
"v_mul_f32 v38, v4, v38\n"
"v_mfma_f32_16x16x32_bf16 v[236:239], a[248:251], v[104:107], v[236:239]\n"
"v_mul_f32 v39, v4, v39\n"
"v_cvt_pk_bf16_f32 v22, v36, v37\n"
"v_mfma_f32_16x16x32_bf16 v[236:239], a[184:187], v[136:139], v[236:239]\n"
"s_xor_b32 s98, s31, 0\n"
"v_cvt_pk_bf16_f32 v23, v38, v39\n"
"v_mfma_f32_16x16x32_bf16 v[252:255], a[180:183], v[136:139], 0\n"
"v_mfma_f32_16x16x32_bf16 v[252:255], a[244:247], v[104:107], v[252:255]\n"
"buffer_load_dwordx4 a[124:127], v0, s[64:67], s98 offen offset:0 sc1\n"
"v_mfma_f32_16x16x32_bf16 v[252:255], a[228:231], v[88:91], v[252:255]\n"
"s_xor_b32 s98, s31, 4096\n"
"v_mfma_f32_16x16x32_bf16 v[252:255], a[212:215], a[116:119], v[252:255]\n"
"v_mfma_f32_16x16x32_bf16 v[216:219], a[212:215], v[116:119], 0\n"
"buffer_load_dwordx4 a[132:135], v0, s[64:67], s98 offen offset:0 sc1\n"
"v_mfma_f32_16x16x32_bf16 v[216:219], a[228:231], v[100:103], v[216:219]\n"
"v_mfma_f32_16x16x32_bf16 v[216:219], a[244:247], v[84:87], v[216:219]\n"
"s_waitcnt vmcnt(20)\n"
"s_xor_b32 s98, s31, 8192\n"
"v_mfma_f32_16x16x32_bf16 v[216:219], a[180:183], v[80:83], v[216:219]\n"
"v_mfma_f32_16x16x32_bf16 v[232:235], a[184:187], v[80:83], 0\n"
"buffer_load_dwordx4 v[44:47], v0, s[64:67], s98 offen offset:0 sc1\n"
"v_mfma_f32_16x16x32_bf16 v[232:235], a[248:251], v[84:87], v[232:235]\n"
"v_mfma_f32_16x16x32_bf16 v[232:235], a[232:235], v[100:103], v[232:235]\n"
"s_xor_b32 s98, s31, 12288\n"
"v_mfma_f32_16x16x32_bf16 v[232:235], a[216:219], v[116:119], v[232:235]\n"
"v_mfma_f32_16x16x32_bf16 v[248:251], a[220:223], v[116:119], 0\n"
"buffer_load_dwordx4 v[60:63], v0, s[64:67], s98 offen offset:0 sc1\n"
"v_mfma_f32_16x16x32_bf16 v[248:251], a[236:239], v[100:103], v[248:251]\n"
"v_mfma_f32_16x16x32_bf16 v[248:251], a[252:255], v[84:87], v[248:251]\n"
"v_mfma_f32_16x16x32_bf16 v[248:251], a[188:191], v[80:83], v[248:251]\n"
"v_mfma_f32_16x16x32_bf16 v[16:19], a[192:195], v[80:83], 0\n"
"s_waitcnt vmcnt(14)\n"
"buffer_store_dwordx4 v[20:23], v0, s[64:67], s99 offen offset:0 sc1\n"
"v_cmp_ne_u32 vcc, s92, v2\n"
"v_mfma_f32_16x16x32_bf16 v[16:19], a[144:147], v[84:87], v[16:19]\n"
"v_mfma_f32_16x16x32_bf16 v[16:19], a[240:243], v[100:103], v[16:19]\n"
"s_cbranch_vccnz .Lrt5\n"
".Lbk5:\n"
"v_mfma_f32_16x16x32_bf16 v[16:19], a[224:227], v[116:119], v[16:19]\n"
"s_xor_b32 s98, s31, 394240\n"
"v_mfma_f32_16x16x32_bf16 a[32:35], a[224:227], v[96:99], 0\n"
"buffer_load_dwordx4 v[64:67], v0, s[64:67], s98 offen offset:0 sc1\n"
"v_mfma_f32_16x16x32_bf16 a[32:35], a[240:243], v[112:115], a[32:35]\n"
"s_xor_b32 s98, s31, 398336\n"
"v_mfma_f32_16x16x32_bf16 a[32:35], a[144:147], v[108:111], a[32:35]\n"
"v_mfma_f32_16x16x32_bf16 a[32:35], a[192:195], v[92:95], a[32:35]\n"
"buffer_load_dwordx4 v[136:139], v0, s[64:67], s98 offen offset:0 sc1\n"
"v_mfma_f32_16x16x32_bf16 a[28:31], a[188:191], v[92:95], 0\n"
"v_mfma_f32_16x16x32_bf16 a[28:31], a[252:255], v[108:111], a[28:31]\n"
"s_xor_b32 s98, s31, 402432\n"
"v_mfma_f32_16x16x32_bf16 a[28:31], a[236:239], v[112:115], a[28:31]\n"
"s_add_u32 s11, s43, 16384\n"
"v_and_b32 v10, 24, v8\n"
"v_sub_u32 v9, v8, v10\n"
"v_mfma_f32_16x16x32_bf16 a[28:31], a[220:223], v[96:99], a[28:31]\n"
"buffer_load_dwordx4 v[104:107], v0, s[64:67], s98 offen offset:0 sc1\n"
"v_lshlrev_b32 v10, 2, v10\n"
"v_mfma_f32_16x16x32_bf16 a[24:27], a[216:219], v[96:99], 0\n"
"s_xor_b32 s98, s31, 406528\n"
"v_xor_b32 v9, v9, v10\n"
"v_add_u32 v9, s11, v9\n"
"v_mfma_f32_16x16x32_bf16 a[24:27], a[232:235], v[112:115], a[24:27]\n"
"v_mfma_f32_16x16x32_bf16 a[24:27], a[248:251], v[108:111], a[24:27]\n"
"buffer_load_dwordx4 v[88:91], v0, s[64:67], s98 offen offset:0 sc1\n"
"v_mfma_f32_16x16x32_bf16 a[24:27], a[184:187], v[92:95], a[24:27]\n"
"v_mfma_f32_16x16x32_bf16 a[20:23], a[180:183], v[92:95], 0\n"
"s_xor_b32 s98, s31, 395264\n"
"v_mfma_f32_16x16x32_bf16 a[20:23], a[244:247], v[108:111], a[20:23]\n"
"v_mfma_f32_16x16x32_bf16 a[20:23], a[228:231], v[112:115], a[20:23]\n"
"buffer_load_dwordx4 a[116:119], v0, s[64:67], s98 offen offset:0 sc1\n"
"v_mfma_f32_16x16x32_bf16 a[20:23], a[212:215], v[96:99], a[20:23]\n"
"s_cmp_eq_u32 s8, 0\n"
"s_cbranch_scc1 .Lyg0\n.Lygb0:\n"
"v_mfma_f32_16x16x32_bf16 a[36:39], a[212:215], v[76:79], 0\n"
"s_cmp_eq_u32 s10, 0\n"
"s_cbranch_scc1 .Lyg1\n.Lygb1:\n"
"v_mfma_f32_16x16x32_bf16 a[36:39], a[228:231], a[196:199], a[36:39]\n"
"s_waitcnt vmcnt(18)\n"
"s_xor_b32 s98, s31, 399360\n"
"v_mfma_f32_16x16x32_bf16 a[36:39], a[244:247], a[200:203], a[36:39]\n"
"s_waitcnt vmcnt(17)\n"
"buffer_load_dwordx4 v[80:83], v0, s[64:67], s98 offen offset:0 sc1\n"
"v_mfma_f32_16x16x32_bf16 a[36:39], a[180:183], a[204:207], a[36:39]\n"
"s_xor_b32 s98, s31, 403456\n"
"v_mfma_f32_16x16x32_bf16 a[40:43], a[184:187], a[204:207], 0\n"
"v_mfma_f32_16x16x32_bf16 a[40:43], a[248:251], a[200:203], a[40:43]\n"
"buffer_load_dwordx4 v[84:87], v0, s[64:67], s98 offen offset:0 sc1\n"
"v_mfma_f32_16x16x32_bf16 a[40:43], a[232:235], a[196:199], a[40:43]\n"
"v_mfma_f32_16x16x32_bf16 a[40:43], a[216:219], v[76:79], a[40:43]\n"
"s_xor_b32 s98, s31, 407552\n"
"v_mfma_f32_16x16x32_bf16 a[44:47], a[220:223], v[76:79], 0\n"
"v_mfma_f32_16x16x32_bf16 a[44:47], a[236:239], a[196:199], a[44:47]\n"
"buffer_load_dwordx4 v[100:103], v0, s[64:67], s98 offen offset:0 sc1\n"
"v_mfma_f32_16x16x32_bf16 a[44:47], a[252:255], a[200:203], a[44:47]\n"
"ds_read_b64_tr_b16 a[64:65], v9 offset:0\n"
"v_mfma_f32_16x16x32_bf16 a[44:47], a[188:191], a[204:207], a[44:47]\n"
"s_xor_b32 s98, s31, 396288\n"
"v_mfma_f32_16x16x32_bf16 a[48:51], a[192:195], a[204:207], 0\n"
"v_mfma_f32_16x16x32_bf16 a[48:51], a[144:147], a[200:203], a[48:51]\n"
"buffer_load_dwordx4 v[116:119], v0, s[64:67], s98 offen offset:0 sc1\n"
"ds_read_b64_tr_b16 a[66:67], v9 offset:2048\n"
"v_mfma_f32_16x16x32_bf16 a[48:51], a[240:243], a[196:199], a[48:51]\n"
"v_mfma_f32_16x16x32_bf16 a[48:51], a[224:227], v[76:79], a[48:51]\n"
"s_waitcnt vmcnt(20)\n"
"s_xor_b32 s98, s31, 400384\n"
"v_mfma_f32_16x16x32_bf16 a[176:179], a[224:227], a[208:211], 0\n"
"s_waitcnt vmcnt(17)\n"
"ds_read_b64_tr_b16 a[80:81], v9 offset:8\n"
"v_mfma_f32_16x16x32_bf16 a[176:179], a[240:243], v[48:51], a[176:179]\n"
"s_waitcnt vmcnt(16)\n"
"buffer_load_dwordx4 v[92:95], v0, s[64:67], s98 offen offset:0 sc1\n"
"v_mfma_f32_16x16x32_bf16 a[176:179], a[144:147], a[136:139], a[176:179]\n"
"s_waitcnt vmcnt(16)\n"
"s_xor_b32 s98, s31, 404480\n"
"v_mfma_f32_16x16x32_bf16 a[176:179], a[192:195], a[120:123], a[176:179]\n"
"ds_read_b64_tr_b16 a[82:83], v9 offset:2056\n"
"v_mfma_f32_16x16x32_bf16 a[172:175], a[188:191], a[120:123], 0\n"
"buffer_load_dwordx4 v[108:111], v0, s[64:67], s98 offen offset:0 sc1\n"
"v_mfma_f32_16x16x32_bf16 a[172:175], a[252:255], a[136:139], a[172:175]\n"
"v_mfma_f32_16x16x32_bf16 a[172:175], a[236:239], v[48:51], a[172:175]\n"
"s_xor_b32 s98, s31, 408576\n"
"ds_read_b64_tr_b16 a[60:61], v9 offset:16\n"
"v_mfma_f32_16x16x32_bf16 a[172:175], a[220:223], a[208:211], a[172:175]\n"
"v_mfma_f32_16x16x32_bf16 a[168:171], a[216:219], a[208:211], 0\n"
"buffer_load_dwordx4 v[112:115], v0, s[64:67], s98 offen offset:0 sc1\n"
"v_mfma_f32_16x16x32_bf16 a[168:171], a[232:235], v[48:51], a[168:171]\n"
"ds_read_b64_tr_b16 a[62:63], v9 offset:2064\n"
"v_mfma_f32_16x16x32_bf16 a[168:171], a[248:251], a[136:139], a[168:171]\n"
"s_waitcnt vmcnt(12)\n"
"v_mfma_f32_16x16x32_bf16 a[168:171], a[184:187], a[120:123], a[168:171]\n"
"s_add_u32 s24, s34, 12288\n"
"v_mfma_f32_16x16x32_bf16 a[164:167], a[180:183], a[120:123], 0\n"
"buffer_store_dword v12, v14, s[64:67], s24 offen sc1\n"
"ds_read_b64_tr_b16 a[76:77], v9 offset:24\n"
"v_mfma_f32_16x16x32_bf16 a[164:167], a[244:247], a[136:139], a[164:167]\n"
"s_xor_b32 s98, s31, 393216\n"
"v_mfma_f32_16x16x32_bf16 a[164:167], a[228:231], v[48:51], a[164:167]\n"
"s_lshl_b32 s95, s40, 7\n"
"v_mfma_f32_16x16x32_bf16 a[164:167], a[212:215], a[208:211], a[164:167]\n"
"buffer_load_dwordx4 v[96:99], v0, s[64:67], s98 offen offset:0 sc1\n"
"ds_read_b64_tr_b16 a[78:79], v9 offset:2072\n"
"v_mfma_f32_16x16x32_bf16 v[120:123], a[212:215], a[124:127], 0\n"
"s_add_u32 s94, s95, 4096\n"
"v_mfma_f32_16x16x32_bf16 v[120:123], a[228:231], a[132:135], v[120:123]\n"
"s_xor_b32 s98, s31, 397312\n"
"v_mfma_f32_16x16x32_bf16 v[120:123], a[244:247], v[44:47], v[120:123]\n"
"s_add_u32 s95, s95, s91\n"
"ds_read_b64_tr_b16 a[56:57], v9 offset:16384\n"
"v_mfma_f32_16x16x32_bf16 v[120:123], a[180:183], v[60:63], v[120:123]\n"
"s_lshl_b32 s95, s95, 7\n"
"buffer_load_dwordx4 a[204:207], v0, s[64:67], s98 offen offset:0 sc1\n"
"v_and_b32 v151, 0xf0, v0\n"
"v_mfma_f32_16x16x32_bf16 v[124:127], a[184:187], v[60:63], 0\n"
"v_lshrrev_b32 v150, 3, v0\n"
"v_and_b32 v150, 0x60, v150\n"
"v_mfma_f32_16x16x32_bf16 v[124:127], a[248:251], v[44:47], v[124:127]\n"
"s_add_u32 s25, s35, 8192\n"
"ds_read_b64_tr_b16 a[58:59], v9 offset:18432\n"
"v_lshl_add_u32 v150, v151, 6, v150\n"
"v_mfma_f32_16x16x32_bf16 v[124:127], a[232:235], a[132:135], v[124:127]\n"
"v_add_u32 v150, s95, v150\n"
"v_lshrrev_b32 v151, 1, v151\n"
"v_mfma_f32_16x16x32_bf16 v[124:127], a[216:219], a[124:127], v[124:127]\n"
"buffer_load_dword v2, v1, s[64:67], s25 offen sc1\n"
"v_mfma_f32_16x16x32_bf16 v[128:131], a[220:223], a[124:127], 0\n"
"s_xor_b32 s98, s31, 401408\n"
"ds_read_b64_tr_b16 a[72:73], v9 offset:16392\n"
"v_mfma_f32_16x16x32_bf16 v[128:131], a[236:239], a[132:135], v[128:131]\n"
"buffer_load_dwordx4 v[140:143], v150, s[44:47], 0 offen\n"
"v_mfma_f32_16x16x32_bf16 v[128:131], a[252:255], v[44:47], v[128:131]\n"
"buffer_load_dwordx4 a[200:203], v0, s[64:67], s98 offen offset:0 sc1\n"
"v_mfma_f32_16x16x32_bf16 v[128:131], a[188:191], v[60:63], v[128:131]\n"
"ds_read_b64_tr_b16 a[74:75], v9 offset:18440\n"
"v_mfma_f32_16x16x32_bf16 v[132:135], a[192:195], v[60:63], 0\n"
"s_xor_b32 s98, s31, 405504\n"
"v_mfma_f32_16x16x32_bf16 v[132:135], a[144:147], v[44:47], v[132:135]\n"
".Lpg10:\n"
"s_cbranch_execz .Lpgret0\n"
"buffer_load_dwordx4 v[144:147], v150, s[44:47], 0 offen offset:16\n"
"v_mfma_f32_16x16x32_bf16 v[132:135], a[240:243], a[132:135], v[132:135]\n"
"buffer_load_dwordx4 a[196:199], v0, s[64:67], s98 offen offset:0 sc1\n"
"ds_read_b64_tr_b16 a[52:53], v9 offset:16400\n"
"v_mfma_f32_16x16x32_bf16 v[132:135], a[224:227], a[124:127], v[132:135]\n"
"s_waitcnt vmcnt(19)\n"
"v_mfma_f32_16x16x32_bf16 v[212:215], a[160:163], v[64:67], v[212:215]\n"
"s_waitcnt vmcnt(17)\n"
"s_xor_b32 s98, s31, 263168\n"
"v_mfma_f32_16x16x32_bf16 v[212:215], a[96:99], v[136:139], v[212:215]\n"
"buffer_load_dwordx2 v[148:149], v151, s[64:67], s94 offen sc1\n"
"ds_read_b64_tr_b16 a[54:55], v9 offset:18448\n"
"v_mfma_f32_16x16x32_bf16 v[212:215], a[112:115], v[104:107], v[212:215]\n"
"s_waitcnt vmcnt(17)\n"
"buffer_load_dwordx4 v[76:79], v0, s[64:67], s98 offen offset:0 sc1\n"
"v_mfma_f32_16x16x32_bf16 v[212:215], a[12:15], v[88:91], v[212:215]\n"
"v_mfma_f32_16x16x32_bf16 v[228:231], a[8:11], v[88:91], v[228:231]\n"
"s_xor_b32 s98, s31, 267264\n"
"ds_read_b64_tr_b16 a[68:69], v9 offset:16408\n"
"v_mfma_f32_16x16x32_bf16 v[228:231], a[108:111], v[104:107], v[228:231]\n"
"v_mfma_f32_16x16x32_bf16 v[228:231], a[92:95], v[136:139], v[228:231]\n"
"buffer_load_dwordx4 a[120:123], v0, s[64:67], s98 offen offset:0 sc1\n"
"v_mfma_f32_16x16x32_bf16 v[228:231], a[156:159], v[64:67], v[228:231]\n"
"s_xor_b32 s98, s31, 271360\n"
"ds_read_b64_tr_b16 a[70:71], v9 offset:18456\n"
"v_subrev_u32 v9, 65536, v9\n"
"v_mfma_f32_16x16x32_bf16 v[244:247], a[152:155], v[64:67], v[244:247]\n"
"v_mfma_f32_16x16x32_bf16 v[244:247], a[88:91], v[136:139], v[244:247]\n"
"buffer_load_dwordx4 a[136:139], v0, s[64:67], s98 offen offset:0 sc1\n"
"v_mfma_f32_16x16x32_bf16 v[244:247], a[104:107], v[104:107], v[244:247]\n"
"ds_read_b64_tr_b16 a[212:213], v9 offset:0\n"
"v_mfma_f32_16x16x32_bf16 v[244:247], a[4:7], v[88:91], v[244:247]\n"
"s_xor_b32 s98, s31, 275456\n"
"v_mfma_f32_16x16x32_bf16 v[32:35], a[0:3], v[88:91], v[32:35]\n"
"v_mfma_f32_16x16x32_bf16 v[32:35], a[100:103], v[104:107], v[32:35]\n"
"buffer_load_dwordx4 v[48:51], v0, s[64:67], s98 offen offset:0 sc1\n"
"ds_read_b64_tr_b16 a[214:215], v9 offset:2048\n"
"v_mfma_f32_16x16x32_bf16 v[32:35], a[84:87], v[136:139], v[32:35]\n"
"v_mfma_f32_16x16x32_bf16 v[32:35], a[148:151], v[64:67], v[32:35]\n"
"s_waitcnt vmcnt(20)\n"
"s_xor_b32 s98, s31, 264192\n"
"v_mfma_f32_16x16x32_bf16 v[208:211], a[148:151], a[116:119], v[208:211]\n"
"s_waitcnt vmcnt(19)\n"
"ds_read_b64_tr_b16 a[228:229], v9 offset:8\n"
"v_mfma_f32_16x16x32_bf16 v[208:211], a[84:87], v[80:83], v[208:211]\n"
"s_waitcnt vmcnt(18)\n"
"buffer_load_dwordx4 a[208:211], v0, s[64:67], s98 offen offset:0 sc1\n"
"v_mfma_f32_16x16x32_bf16 v[208:211], a[100:103], v[84:87], v[208:211]\n"
"s_waitcnt vmcnt(18)\n"
"s_xor_b32 s98, s31, 268288\n"
"v_mfma_f32_16x16x32_bf16 v[208:211], a[0:3], v[100:103], v[208:211]\n"
"ds_read_b64_tr_b16 a[230:231], v9 offset:2056\n"
"v_mfma_f32_16x16x32_bf16 v[224:227], a[4:7], v[100:103], v[224:227]\n"
"buffer_load_dwordx4 v[60:63], v0, s[64:67], s98 offen offset:0 sc1\n"
"v_mfma_f32_16x16x32_bf16 v[224:227], a[104:107], v[84:87], v[224:227]\n"
"v_mfma_f32_16x16x32_bf16 v[224:227], a[88:91], v[80:83], v[224:227]\n"
"s_xor_b32 s98, s31, 272384\n"
"ds_read_b64_tr_b16 a[244:245], v9 offset:16\n"
"v_mfma_f32_16x16x32_bf16 v[224:227], a[152:155], a[116:119], v[224:227]\n"
"v_mfma_f32_16x16x32_bf16 v[240:243], a[156:159], a[116:119], v[240:243]\n"
"buffer_load_dwordx4 v[44:47], v0, s[64:67], s98 offen offset:0 sc1\n"
"v_mfma_f32_16x16x32_bf16 v[240:243], a[92:95], v[80:83], v[240:243]\n"
"ds_read_b64_tr_b16 a[246:247], v9 offset:2064\n"
"v_mfma_f32_16x16x32_bf16 v[240:243], a[108:111], v[84:87], v[240:243]\n"
"s_xor_b32 s98, s31, 276480\n"
"v_mfma_f32_16x16x32_bf16 v[240:243], a[8:11], v[100:103], v[240:243]\n"
"v_mfma_f32_16x16x32_bf16 v[24:27], a[12:15], v[100:103], v[24:27]\n"
"s_waitcnt vmcnt(12)\n"
"buffer_load_dwordx4 a[132:135], v0, s[64:67], s98 offen offset:0 sc1\n"
"v_cmp_ne_u32 vcc, s92, v2\n"
"v_mfma_f32_16x16x32_bf16 v[24:27], a[112:115], v[84:87], v[24:27]\n"
"ds_read_b64_tr_b16 a[180:181], v9 offset:24\n"
"v_mfma_f32_16x16x32_bf16 v[24:27], a[96:99], v[80:83], v[24:27]\n"
"s_cbranch_vccnz .Lrt6\n"
".Lbk6:\n"
"v_mfma_f32_16x16x32_bf16 v[24:27], a[160:163], a[116:119], v[24:27]\n"
"s_xor_b32 s98, s31, 656384\n"
"ds_read_b64_tr_b16 a[182:183], v9 offset:2072\n"
"v_mfma_f32_16x16x32_bf16 v[204:207], a[160:163], v[116:119], v[204:207]\n"
"buffer_load_dwordx4 a[124:127], v0, s[64:67], s98 offen offset:0 sc1\n"
"v_mfma_f32_16x16x32_bf16 v[204:207], a[96:99], v[92:95], v[204:207]\n"
"s_xor_b32 s98, s31, 660480\n"
"v_mfma_f32_16x16x32_bf16 v[204:207], a[112:115], v[108:111], v[204:207]\n"
"ds_read_b64_tr_b16 a[216:217], v9 offset:16384\n"
"v_mfma_f32_16x16x32_bf16 v[204:207], a[12:15], v[112:115], v[204:207]\n"
"buffer_load_dwordx4 v[88:91], v0, s[64:67], s98 offen offset:0 sc1\n"
"v_mfma_f32_16x16x32_bf16 v[220:223], a[8:11], v[112:115], v[220:223]\n"
"v_mfma_f32_16x16x32_bf16 v[220:223], a[108:111], v[108:111], v[220:223]\n"
"s_xor_b32 s98, s31, 664576\n"
"ds_read_b64_tr_b16 a[218:219], v9 offset:18432\n"
"v_mfma_f32_16x16x32_bf16 v[220:223], a[92:95], v[92:95], v[220:223]\n"
"v_mfma_f32_16x16x32_bf16 v[220:223], a[156:159], v[116:119], v[220:223]\n"
"buffer_load_dwordx4 v[104:107], v0, s[64:67], s98 offen offset:0 sc1\n"
"v_mfma_f32_16x16x32_bf16 v[236:239], a[152:155], v[116:119], v[236:239]\n"
"ds_read_b64_tr_b16 a[232:233], v9 offset:16392\n"
"v_mfma_f32_16x16x32_bf16 v[236:239], a[88:91], v[92:95], v[236:239]\n"
"s_xor_b32 s98, s31, 668672\n"
"v_mfma_f32_16x16x32_bf16 v[236:239], a[104:107], v[108:111], v[236:239]\n"
"v_mfma_f32_16x16x32_bf16 v[236:239], a[4:7], v[112:115], v[236:239]\n"
"buffer_load_dwordx4 v[136:139], v0, s[64:67], s98 offen offset:0 sc1\n"
"ds_read_b64_tr_b16 a[234:235], v9 offset:18440\n"
"v_mfma_f32_16x16x32_bf16 v[252:255], a[0:3], v[112:115], v[252:255]\n"
"s_xor_b32 s98, s31, 657408\n"
"v_mfma_f32_16x16x32_bf16 v[252:255], a[100:103], v[108:111], v[252:255]\n"
"v_mfma_f32_16x16x32_bf16 v[252:255], a[84:87], v[92:95], v[252:255]\n"
"buffer_load_dwordx4 v[64:67], v0, s[64:67], s98 offen offset:0 sc1\n"
"ds_read_b64_tr_b16 a[248:249], v9 offset:16400\n"
"v_mfma_f32_16x16x32_bf16 v[252:255], a[148:151], v[116:119], v[252:255]\n"
"v_mfma_f32_16x16x32_bf16 v[216:219], a[148:151], v[96:99], v[216:219]\n"
"s_xor_b32 s98, s31, 661504\n"
"v_mfma_f32_16x16x32_bf16 v[216:219], a[84:87], a[204:207], v[216:219]\n"
"s_waitcnt vmcnt(16)\n"
"ds_read_b64_tr_b16 a[250:251], v9 offset:18448\n"
"v_mfma_f32_16x16x32_bf16 v[216:219], a[100:103], a[200:203], v[216:219]\n"
"s_waitcnt vmcnt(14)\n"
"buffer_load_dwordx4 v[100:103], v0, s[64:67], s98 offen offset:0 sc1\n"
"v_mfma_f32_16x16x32_bf16 v[216:219], a[0:3], a[196:199], v[216:219]\n"
"v_mfma_f32_16x16x32_bf16 v[232:235], a[4:7], a[196:199], v[232:235]\n"
"s_add_u32 s25, s35, 12288\n"
"ds_read_b64_tr_b16 a[184:185], v9 offset:16408\n"
"v_mfma_f32_16x16x32_bf16 v[232:235], a[104:107], a[200:203], v[232:235]\n"
"v_mfma_f32_16x16x32_bf16 v[232:235], a[88:91], a[204:207], v[232:235]\n"
"buffer_load_dword v2, v1, s[64:67], s25 offen sc1\n"
"v_mfma_f32_16x16x32_bf16 v[232:235], a[152:155], v[96:99], v[232:235]\n"
"s_xor_b32 s98, s31, 665600\n"
"ds_read_b64_tr_b16 a[186:187], v9 offset:18456\n"
"v_mfma_f32_16x16x32_bf16 v[248:251], a[156:159], v[96:99], v[248:251]\n"
"v_mfma_f32_16x16x32_bf16 v[248:251], a[92:95], a[204:207], v[248:251]\n"
"buffer_load_dwordx4 v[84:87], v0, s[64:67], s98 offen offset:0 sc1\n"
"v_mfma_f32_16x16x32_bf16 v[248:251], a[108:111], a[200:203], v[248:251]\n"
"ds_read_b64_tr_b16 a[220:221], v9 offset:32768\n"
"v_mfma_f32_16x16x32_bf16 v[248:251], a[8:11], a[196:199], v[248:251]\n"
"s_xor_b32 s98, s31, 669696\n"
"v_mfma_f32_16x16x32_bf16 v[16:19], a[12:15], a[196:199], v[16:19]\n"
"v_mfma_f32_16x16x32_bf16 v[16:19], a[112:115], a[200:203], v[16:19]\n"
"buffer_load_dwordx4 v[80:83], v0, s[64:67], s98 offen offset:0 sc1\n"
"ds_read_b64_tr_b16 a[222:223], v9 offset:34816\n"
"v_mfma_f32_16x16x32_bf16 v[16:19], a[96:99], a[204:207], v[16:19]\n"
"s_waitcnt vmcnt(17)\n"
"v_cmp_ne_u32 s[28:29], s92, v148\n"
"v_mfma_f32_16x16x32_bf16 v[16:19], a[160:163], v[96:99], v[16:19]\n"
"s_waitcnt vmcnt(16)\n"
"s_xor_b32 s98, s31, 658432\n"
"v_mfma_f32_16x16x32_bf16 a[32:35], a[160:163], v[76:79], a[32:35]\n"
"s_waitcnt vmcnt(15)\n"
"ds_read_b64_tr_b16 a[236:237], v9 offset:32776\n"
"v_mfma_f32_16x16x32_bf16 a[32:35], a[96:99], a[120:123], a[32:35]\n"
"s_waitcnt vmcnt(14)\n"
"buffer_load_dwordx4 a[116:119], v0, s[64:67], s98 offen offset:0 sc1\n"
"v_mfma_f32_16x16x32_bf16 a[32:35], a[112:115], a[136:139], a[32:35]\n"
"s_waitcnt vmcnt(14)\n"
"s_xor_b32 s98, s31, 662528\n"
"v_mfma_f32_16x16x32_bf16 a[32:35], a[12:15], v[48:51], a[32:35]\n"
"s_cmp_lg_u64 s[28:29], 0\n"
"s_cbranch_scc1 .Lrt8\n.Lbk8:\n"
"v_mfma_f32_16x16x32_bf16 a[28:31], a[8:11], v[48:51], a[28:31]\n"
"buffer_load_dwordx4 v[112:115], v0, s[64:67], s98 offen offset:0 sc1\n"
"ds_read_b64_tr_b16 a[238:239], v9 offset:34824\n"
"v_mfma_f32_16x16x32_bf16 a[28:31], a[108:111], a[136:139], a[28:31]\n"
"s_lshl_b32 s94, s17, 12\n"
"v_mfma_f32_16x16x32_bf16 a[28:31], a[92:95], a[120:123], a[28:31]\n"
"s_xor_b32 s98, s31, 666624\n"
"ds_read_b64_tr_b16 a[252:253], v9 offset:32784\n"
"v_add_u32 v4, s94, v0\n"
"v_mfma_f32_16x16x32_bf16 a[28:31], a[156:159], v[76:79], a[28:31]\n"
"v_xor_b32 v5, 4096, v4\n"
"v_xor_b32 v6, 8192, v4\n"
"v_mfma_f32_16x16x32_bf16 a[24:27], a[152:155], v[76:79], a[24:27]\n"
"buffer_load_dwordx4 v[108:111], v0, s[64:67], s98 offen offset:0 sc1\n"
"v_xor_b32 v7, 12288, v4\n"
"v_mfma_f32_16x16x32_bf16 a[24:27], a[88:91], a[120:123], a[24:27]\n"
"s_mov_b32 s8, 117835008\n"
"ds_read_b64_tr_b16 a[254:255], v9 offset:34832\n"
"v_mfma_f32_16x16x32_bf16 a[24:27], a[104:107], a[136:139], a[24:27]\n"
"s_xor_b32 s98, s31, 670720\n"
"v_mfma_f32_16x16x32_bf16 a[24:27], a[4:7], v[48:51], a[24:27]\n"
"s_mov_b32 s9, 117835009\n"
"v_mfma_f32_16x16x32_bf16 a[20:23], a[0:3], v[48:51], a[20:23]\n"
"buffer_load_dwordx4 v[92:95], v0, s[64:67], s98 offen offset:0 sc1\n"
"ds_read_b64_tr_b16 a[188:189], v9 offset:32792\n"
"v_mfma_f32_16x16x32_bf16 a[20:23], a[100:103], a[136:139], a[20:23]\n"
"s_mov_b32 s10, 117835010\n"
"v_mfma_f32_16x16x32_bf16 a[20:23], a[84:87], a[120:123], a[20:23]\n"
"s_xor_b32 s98, s31, 655360\n"
"v_mfma_f32_16x16x32_bf16 a[20:23], a[148:151], v[76:79], a[20:23]\n"
"s_waitcnt vmcnt(16)\n"
"s_mov_b32 s11, 117835011\n"
"ds_read_b64_tr_b16 a[190:191], v9 offset:34840\n"
"v_mfma_f32_16x16x32_bf16 a[36:39], a[148:151], a[208:211], a[36:39]\n"
"s_waitcnt vmcnt(15)\n"
"s_andn2_b32 s99, 1, s90\n"
"buffer_load_dwordx4 v[116:119], v0, s[64:67], s98 offen offset:0 sc1\n"
"v_mfma_f32_16x16x32_bf16 a[36:39], a[84:87], v[60:63], a[36:39]\n"
"s_waitcnt vmcnt(15)\n"
"s_xor_b32 s98, s31, 659456\n"
"v_mfma_f32_16x16x32_bf16 a[36:39], a[100:103], v[44:47], a[36:39]\n"
"s_waitcnt vmcnt(14)\n"
"s_lshl_b32 s99, s99, 3\n"
"ds_read_b64_tr_b16 a[224:225], v9 offset:49152\n"
"v_mfma_f32_16x16x32_bf16 a[36:39], a[0:3], a[132:135], a[36:39]\n"
"s_add_u32 s99, s99, s91\n"
"buffer_load_dwordx4 a[196:199], v0, s[64:67], s98 offen offset:0 sc1\n"
"v_mfma_f32_16x16x32_bf16 a[40:43], a[4:7], a[132:135], a[40:43]\n"
"s_lshl_b32 s99, s99, 2\n"
"v_mfma_f32_16x16x32_bf16 a[40:43], a[104:107], v[44:47], a[40:43]\n"
"s_xor_b32 s98, s31, 663552\n"
"ds_read_b64_tr_b16 a[226:227], v9 offset:51200\n"
"v_mfma_f32_16x16x32_bf16 a[40:43], a[88:91], v[60:63], a[40:43]\n"
"s_add_u32 s99, s99, s17\n"
"v_mfma_f32_16x16x32_bf16 a[40:43], a[152:155], a[208:211], a[40:43]\n"
"s_lshl_b32 s99, s99, 12\n"
"buffer_load_dwordx4 a[200:203], v0, s[64:67], s98 offen offset:0 sc1\n"
"v_mfma_f32_16x16x32_bf16 a[44:47], a[156:159], a[208:211], a[44:47]\n"
"s_add_u32 s99, s99, s6\n"
"ds_read_b64_tr_b16 a[240:241], v9 offset:49160\n"
"v_mfma_f32_16x16x32_bf16 a[44:47], a[92:95], v[60:63], a[44:47]\n"
"s_xor_b32 s98, s31, 667648\n"
"v_mfma_f32_16x16x32_bf16 a[44:47], a[108:111], v[44:47], a[44:47]\n"
"s_add_u32 s99, s99, 1310720\n"
"v_mfma_f32_16x16x32_bf16 a[44:47], a[8:11], a[132:135], a[44:47]\n"
"s_waitcnt vmcnt(9)\n"
"buffer_load_dwordx4 a[204:207], v0, s[64:67], s98 offen offset:0 sc1\n"
"v_cmp_ne_u32 vcc, s92, v2\n"
"v_mfma_f32_16x16x32_bf16 a[48:51], a[12:15], a[132:135], a[48:51]\n"
"ds_read_b64_tr_b16 a[242:243], v9 offset:51208\n"
"v_lshlrev_b32 v148, 16, v149\n"
"v_mfma_f32_16x16x32_bf16 a[48:51], a[112:115], v[44:47], a[48:51]\n"
"s_cbranch_vccnz .Lrt7\n"
".Lbk7:\n"
"v_mfma_f32_16x16x32_bf16 a[48:51], a[96:99], v[60:63], a[48:51]\n"
"s_xor_b32 s98, s31, 918528\n"
"ds_read_b64_tr_b16 a[144:145], v9 offset:49168\n"
"v_and_b32 v149, 0xffff0000, v149\n"
"v_mfma_f32_16x16x32_bf16 a[48:51], a[160:163], a[208:211], a[48:51]\n"
"buffer_load_dwordx4 v[96:99], v0, s[64:67], s98 offen offset:0 sc1\n"
"v_mfma_f32_16x16x32_bf16 v[212:215], v[56:59], a[124:127], v[212:215]\n"
"s_xor_b32 s98, s31, 922624\n"
"v_rcp_f32 v149, v149\n"
"v_mfma_f32_16x16x32_bf16 v[212:215], a[140:143], v[88:91], v[212:215]\n"
"s_waitcnt lgkmcnt(3)\n"
"v_lshlrev_b32 v156, 16, v140\n"
"v_and_b32 v157, 0xffff0000, v140\n"
"v_mfma_f32_16x16x32_bf16 v[212:215], a[76:79], v[104:107], v[212:215]\n"
"buffer_load_dwordx4 v[48:51], v0, s[64:67], s98 offen offset:0 sc1\n"
"v_lshlrev_b32 v158, 16, v141\n"
"v_mfma_f32_16x16x32_bf16 v[212:215], a[68:71], v[136:139], v[212:215]\n"
"ds_read_b64_tr_b16 a[146:147], v9 offset:51216\n"
"v_and_b32 v159, 0xffff0000, v141\n"
"v_mfma_f32_16x16x32_bf16 v[228:231], a[52:55], v[136:139], v[228:231]\n"
"s_xor_b32 s98, s31, 926720\n"
"v_lshlrev_b32 v160, 16, v142\n"
"v_and_b32 v161, 0xffff0000, v142\n"
"v_mfma_f32_16x16x32_bf16 v[228:231], a[60:63], v[104:107], v[228:231]\n"
"ds_read_b64_tr_b16 a[192:193], v9 offset:49176\n"
"v_lshlrev_b32 v162, 16, v143\n"
"v_mfma_f32_16x16x32_bf16 v[228:231], v[52:55], v[88:91], v[228:231]\n"
"buffer_load_dwordx4 a[136:139], v0, s[64:67], s98 offen offset:0 sc1\n"
"v_and_b32 v163, 0xffff0000, v143\n"
"v_mfma_f32_16x16x32_bf16 v[228:231], v[40:43], a[124:127], v[228:231]\n"
"v_sub_f32 v156, v156, v148\n"
"v_sub_f32 v157, v157, v148\n"
"v_mfma_f32_16x16x32_bf16 v[244:247], a[128:131], a[124:127], v[244:247]\n"
"s_xor_b32 s98, s31, 930816\n"
"ds_read_b64_tr_b16 a[194:195], v9 offset:51224\n"
"v_sub_f32 v158, v158, v148\n"
"v_mfma_f32_16x16x32_bf16 v[244:247], v[68:71], v[88:91], v[244:247]\n"
"v_sub_f32 v159, v159, v148\n"
"v_sub_f32 v160, v160, v148\n"
"v_mfma_f32_16x16x32_bf16 v[244:247], a[80:83], v[104:107], v[244:247]\n"
"buffer_load_dwordx4 a[120:123], v0, s[64:67], s98 offen offset:0 sc1\n"
"v_sub_f32 v161, v161, v148\n"
"v_mfma_f32_16x16x32_bf16 v[244:247], a[72:75], v[136:139], v[244:247]\n"
"s_xor_b32 s98, s31, 919552\n"
"v_sub_f32 v162, v162, v148\n"
"v_sub_f32 v163, v163, v148\n"
"v_mfma_f32_16x16x32_bf16 v[32:35], a[56:59], v[136:139], v[32:35]\n"
"v_cvt_pk_bf16_f32 v157, v156, v157\n"
"v_cvt_pk_bf16_f32 v159, v158, v159\n"
"v_mfma_f32_16x16x32_bf16 v[32:35], a[64:67], v[104:107], v[32:35]\n"
"buffer_load_dwordx4 v[76:79], v0, s[64:67], s98 offen offset:0 sc1\n"
"v_cvt_pk_bf16_f32 v161, v160, v161\n"
"v_mfma_f32_16x16x32_bf16 v[32:35], v[72:75], v[88:91], v[32:35]\n"
"v_cvt_pk_bf16_f32 v163, v162, v163\n"
"v_lshlrev_b32 v156, 16, v157\n"
"v_mfma_f32_16x16x32_bf16 v[32:35], a[16:19], a[124:127], v[32:35]\n"
"s_xor_b32 s98, s31, 923648\n"
"v_and_b32 v157, 0xffff0000, v157\n"
"v_lshlrev_b32 v158, 16, v159\n"
"v_mfma_f32_16x16x32_bf16 v[208:211], a[16:19], v[64:67], v[208:211]\n"
"v_and_b32 v159, 0xffff0000, v159\n"
"v_lshlrev_b32 v160, 16, v161\n"
"v_mfma_f32_16x16x32_bf16 v[208:211], v[72:75], v[100:103], v[208:211]\n"
"s_waitcnt vmcnt(14)\n"
"buffer_load_dwordx4 a[132:135], v0, s[64:67], s98 offen offset:0 sc1\n"
"v_and_b32 v161, 0xffff0000, v161\n"
"v_mfma_f32_16x16x32_bf16 v[208:211], a[64:67], v[84:87], v[208:211]\n"
"s_waitcnt vmcnt(14)\n"
"v_lshlrev_b32 v162, 16, v163\n"
"v_and_b32 v163, 0xffff0000, v163\n"
"v_mfma_f32_16x16x32_bf16 v[208:211], a[56:59], v[80:83], v[208:211]\n"
"s_xor_b32 s98, s31, 927744\n"
"v_mul_f32 v156, v156, v149\n"
"v_mul_f32 v157, v157, v149\n"
"v_mfma_f32_16x16x32_bf16 v[224:227], a[72:75], v[80:83], v[224:227]\n"
"v_mul_f32 v158, v158, v149\n"
"v_mul_f32 v159, v159, v149\n"
"v_mfma_f32_16x16x32_bf16 v[224:227], a[80:83], v[84:87], v[224:227]\n"
"buffer_load_dwordx4 v[44:47], v0, s[64:67], s98 offen offset:0 sc1\n"
"v_mul_f32 v160, v160, v149\n"
"v_mfma_f32_16x16x32_bf16 v[224:227], v[68:71], v[100:103], v[224:227]\n"
"s_xor_b32 s98, s31, 931840\n"
"v_mul_f32 v161, v161, v149\n"
"v_mul_f32 v162, v162, v149\n"
"v_mfma_f32_16x16x32_bf16 v[224:227], a[128:131], v[64:67], v[224:227]\n"
"v_mul_f32 v163, v163, v149\n"
"v_cvt_pk_bf16_f32 v157, v156, v157\n"
"v_mfma_f32_16x16x32_bf16 v[240:243], v[40:43], v[64:67], v[240:243]\n"
"buffer_load_dwordx4 v[60:63], v0, s[64:67], s98 offen offset:0 sc1\n"
"v_cvt_pk_bf16_f32 v159, v158, v159\n"
"v_mfma_f32_16x16x32_bf16 v[240:243], v[52:55], v[100:103], v[240:243]\n"
"v_cvt_pk_bf16_f32 v161, v160, v161\n"
"v_cvt_pk_bf16_f32 v163, v162, v163\n"
"v_mfma_f32_16x16x32_bf16 v[240:243], a[60:63], v[84:87], v[240:243]\n"
"s_xor_b32 s98, s31, 920576\n"
"v_lshlrev_b32 v156, 16, v157\n"
"v_and_b32 v157, 0xffff0000, v157\n"
"v_mfma_f32_16x16x32_bf16 v[240:243], a[52:55], v[80:83], v[240:243]\n"
"v_lshlrev_b32 v158, 16, v159\n"
"v_and_b32 v159, 0xffff0000, v159\n"
"v_mfma_f32_16x16x32_bf16 v[24:27], a[68:71], v[80:83], v[24:27]\n"
"buffer_load_dwordx4 a[208:211], v0, s[64:67], s98 offen offset:0 sc1\n"
"v_lshlrev_b32 v160, 16, v161\n"
"v_mfma_f32_16x16x32_bf16 v[24:27], a[76:79], v[84:87], v[24:27]\n"
"v_and_b32 v161, 0xffff0000, v161\n"
"v_lshlrev_b32 v162, 16, v163\n"
"v_mfma_f32_16x16x32_bf16 v[24:27], a[140:143], v[100:103], v[24:27]\n"
"s_xor_b32 s98, s31, 924672\n"
"v_and_b32 v163, 0xffff0000, v163\n"
"v_lshlrev_b32 v172, 16, v196\n"
"v_mfma_f32_16x16x32_bf16 v[24:27], v[56:59], v[64:67], v[24:27]\n"
"s_waitcnt vmcnt(16)\n"
"v_and_b32 v173, 0xffff0000, v196\n"
"v_lshlrev_b32 v174, 16, v197\n"
"v_mfma_f32_16x16x32_bf16 v[204:207], v[56:59], a[116:119], v[204:207]\n"
"s_waitcnt vmcnt(14)\n"
"buffer_load_dwordx4 v[136:139], v0, s[64:67], s98 offen offset:0 sc1\n"
"v_and_b32 v175, 0xffff0000, v197\n"
"v_mfma_f32_16x16x32_bf16 v[204:207], a[140:143], v[112:115], v[204:207]\n"
"s_xor_b32 s98, s31, 928768\n"
"v_lshlrev_b32 v176, 16, v198\n"
"v_and_b32 v177, 0xffff0000, v198\n"
"v_mfma_f32_16x16x32_bf16 v[204:207], a[76:79], v[108:111], v[204:207]\n"
"s_waitcnt vmcnt(14)\n"
"v_lshlrev_b32 v178, 16, v199\n"
"v_and_b32 v179, 0xffff0000, v199\n"
"v_mfma_f32_16x16x32_bf16 v[204:207], a[68:71], v[92:95], v[204:207]\n"
"buffer_load_dwordx4 v[104:107], v0, s[64:67], s98 offen offset:0 sc1\n"
"v_add_f32 v156, v156, v172\n"
"v_mfma_f32_16x16x32_bf16 v[220:223], a[52:55], v[92:95], v[220:223]\n"
"v_add_f32 v157, v157, v173\n"
"v_add_f32 v158, v158, v174\n"
"v_mfma_f32_16x16x32_bf16 v[220:223], a[60:63], v[108:111], v[220:223]\n"
"s_xor_b32 s98, s31, 932864\n"
"v_add_f32 v159, v159, v175\n"
"v_add_f32 v160, v160, v176\n"
"v_mfma_f32_16x16x32_bf16 v[220:223], v[52:55], v[112:115], v[220:223]\n"
"v_add_f32 v161, v161, v177\n"
"v_add_f32 v162, v162, v178\n"
"v_mfma_f32_16x16x32_bf16 v[220:223], v[40:43], a[116:119], v[220:223]\n"
"buffer_load_dwordx4 v[88:91], v0, s[64:67], s98 offen offset:0 sc1\n"
"v_add_f32 v163, v163, v179\n"
"v_mfma_f32_16x16x32_bf16 v[236:239], a[128:131], a[116:119], v[236:239]\n"
"v_cvt_pk_bf16_f32 v157, v156, v157\n"
"v_cvt_pk_bf16_f32 v159, v158, v159\n"
"v_mfma_f32_16x16x32_bf16 v[236:239], v[68:71], v[112:115], v[236:239]\n"
"s_xor_b32 s98, s31, 917504\n"
"v_cvt_pk_bf16_f32 v161, v160, v161\n"
"v_cvt_pk_bf16_f32 v163, v162, v163\n"
"v_mfma_f32_16x16x32_bf16 v[236:239], a[80:83], v[108:111], v[236:239]\n"
"v_lshlrev_b32 v156, 16, v157\n"
"v_and_b32 v157, 0xffff0000, v157\n"
"v_mfma_f32_16x16x32_bf16 v[236:239], a[72:75], v[92:95], v[236:239]\n"
"buffer_load_dwordx4 a[124:127], v0, s[64:67], s98 offen offset:0 sc1\n"
"v_lshlrev_b32 v158, 16, v159\n"
"v_mfma_f32_16x16x32_bf16 v[252:255], a[56:59], v[92:95], v[252:255]\n"
"v_and_b32 v159, 0xffff0000, v159\n"
"v_lshlrev_b32 v160, 16, v161\n"
"v_mfma_f32_16x16x32_bf16 v[252:255], a[64:67], v[108:111], v[252:255]\n"
"s_xor_b32 s98, s31, 921600\n"
"v_and_b32 v161, 0xffff0000, v161\n"
"v_lshlrev_b32 v162, 16, v163\n"
"v_mfma_f32_16x16x32_bf16 v[252:255], v[72:75], v[112:115], v[252:255]\n"
"v_and_b32 v163, 0xffff0000, v163\n"
"v_mul_f32 v172, 0xbfb8aa3b, v156\n"
"v_mfma_f32_16x16x32_bf16 v[252:255], a[16:19], a[116:119], v[252:255]\n"
"s_waitcnt vmcnt(15)\n"
"buffer_load_dwordx4 v[80:83], v0, s[64:67], s98 offen offset:0 sc1\n"
"v_mul_f32 v173, 0xbfb8aa3b, v157\n"
"v_mfma_f32_16x16x32_bf16 v[216:219], a[16:19], v[116:119], v[216:219]\n"
"s_xor_b32 s98, s31, 925696\n"
"v_mul_f32 v174, 0xbfb8aa3b, v158\n"
"v_mul_f32 v175, 0xbfb8aa3b, v159\n"
"v_mfma_f32_16x16x32_bf16 v[216:219], v[72:75], a[196:199], v[216:219]\n"
"s_waitcnt vmcnt(15)\n"
"v_mul_f32 v176, 0xbfb8aa3b, v160\n"
"v_mul_f32 v177, 0xbfb8aa3b, v161\n"
"v_mfma_f32_16x16x32_bf16 v[216:219], a[64:67], a[200:203], v[216:219]\n"
"s_waitcnt vmcnt(14)\n"
"buffer_load_dwordx4 v[84:87], v0, s[64:67], s98 offen offset:0 sc1\n"
"v_mul_f32 v178, 0xbfb8aa3b, v162\n"
"v_mfma_f32_16x16x32_bf16 v[216:219], a[56:59], a[204:207], v[216:219]\n"
"v_mul_f32 v179, 0xbfb8aa3b, v163\n"
"v_mfma_f32_16x16x32_bf16 v[232:235], a[72:75], a[204:207], v[232:235]\n"
"s_xor_b32 s98, s31, 929792\n"
"v_exp_f32 v172, v172\n"
"v_mfma_f32_16x16x32_bf16 v[232:235], a[80:83], a[200:203], v[232:235]\n"
"v_exp_f32 v173, v173\n"
"v_mfma_f32_16x16x32_bf16 v[232:235], v[68:71], a[196:199], v[232:235]\n"
"buffer_load_dwordx4 v[100:103], v0, s[64:67], s98 offen offset:0 sc1\n"
"v_mfma_f32_16x16x32_bf16 v[232:235], a[128:131], v[116:119], v[232:235]\n"
"v_exp_f32 v174, v174\n"
"v_mfma_f32_16x16x32_bf16 v[248:251], v[40:43], v[116:119], v[248:251]\n"
"s_xor_b32 s98, s31, 265216\n"
"v_exp_f32 v175, v175\n"
"v_mfma_f32_16x16x32_bf16 v[248:251], v[52:55], a[196:199], v[248:251]\n"
"v_exp_f32 v176, v176\n"
"v_mfma_f32_16x16x32_bf16 v[248:251], a[60:63], a[200:203], v[248:251]\n"
"buffer_load_dwordx4 v[64:67], v0, s[64:67], s98 offen offset:0 sc1\n"
"v_mfma_f32_16x16x32_bf16 v[248:251], a[52:55], a[204:207], v[248:251]\n"
"s_xor_b32 s98, s31, 269312\n"
"v_exp_f32 v177, v177\n"
"v_mfma_f32_16x16x32_bf16 v[16:19], a[68:71], a[204:207], v[16:19]\n"
"v_exp_f32 v178, v178\n"
"v_mfma_f32_16x16x32_bf16 v[16:19], a[76:79], a[200:203], v[16:19]\n"
"buffer_load_dwordx4 v[92:95], v0, s[64:67], s98 offen offset:0 sc1\n"
"v_mfma_f32_16x16x32_bf16 v[16:19], a[140:143], a[196:199], v[16:19]\n"
"s_waitcnt vmcnt(17)\n"
"v_exp_f32 v179, v179\n"
"v_mfma_f32_16x16x32_bf16 v[16:19], v[56:59], v[116:119], v[16:19]\n"
"s_xor_b32 s98, s31, 273408\n"
"v_add_f32 v172, 1.0, v172\n"
"v_add_f32 v173, 1.0, v173\n"
"v_mfma_f32_16x16x32_bf16 v[212:215], a[180:183], v[96:99], v[212:215]\n"
"s_waitcnt vmcnt(16)\n"
"v_add_f32 v174, 1.0, v174\n"
"v_add_f32 v175, 1.0, v175\n"
"v_mfma_f32_16x16x32_bf16 v[212:215], a[184:187], v[48:51], v[212:215]\n"
"s_waitcnt vmcnt(15)\n"
"buffer_load_dwordx4 v[108:111], v0, s[64:67], s98 offen offset:0 sc1\n"
"v_add_f32 v176, 1.0, v176\n"
"v_mfma_f32_16x16x32_bf16 v[212:215], a[188:191], a[136:139], v[212:215]\n"
"s_waitcnt vmcnt(15) lgkmcnt(0)\n"
"v_add_f32 v177, 1.0, v177\n"
"v_add_f32 v178, 1.0, v178\n"
"v_mfma_f32_16x16x32_bf16 v[212:215], a[192:195], a[120:123], v[212:215]\n"
"s_xor_b32 s98, s31, 277504\n"
"v_add_f32 v179, 1.0, v179\n"
"v_mfma_f32_16x16x32_bf16 v[228:231], a[144:147], a[120:123], v[228:231]\n"
"v_rcp_f32 v172, v172\n"
"v_mfma_f32_16x16x32_bf16 v[228:231], a[252:255], a[136:139], v[228:231]\n"
"buffer_load_dwordx4 v[112:115], v0, s[64:67], s98 offen offset:0 sc1\n"
"v_mfma_f32_16x16x32_bf16 v[228:231], a[248:251], v[48:51], v[228:231]\n"
"s_xor_b32 s98, s31, 262144\n"
"v_rcp_f32 v173, v173\n"
"v_mfma_f32_16x16x32_bf16 v[228:231], a[244:247], v[96:99], v[228:231]\n"
"ds_write_b128 v5, v[212:215] offset:19456\n"
"v_mfma_f32_16x16x32_bf16 v[244:247], a[228:231], v[96:99], v[244:247]\n"
"buffer_load_dwordx4 a[116:119], v0, s[64:67], s98 offen offset:0 sc1\n"
"v_mfma_f32_16x16x32_bf16 v[244:247], a[232:235], v[48:51], v[244:247]\n"
"v_rcp_f32 v174, v174\n"
"v_mfma_f32_16x16x32_bf16 v[244:247], a[236:239], a[136:139], v[244:247]\n"
"s_xor_b32 s98, s31, 266240\n"
"v_rcp_f32 v175, v175\n"
"v_mfma_f32_16x16x32_bf16 v[244:247], a[240:243], a[120:123], v[244:247]\n"
"ds_write_b128 v5, v[228:231] offset:18432\n"
"v_mfma_f32_16x16x32_bf16 v[32:35], a[224:227], a[120:123], v[32:35]\n"
"buffer_load_dwordx4 a[204:207], v0, s[64:67], s98 offen offset:0 sc1\n"
"v_mfma_f32_16x16x32_bf16 v[32:35], a[220:223], a[136:139], v[32:35]\n"
"v_rcp_f32 v176, v176\n"
"v_mfma_f32_16x16x32_bf16 v[32:35], a[216:219], v[48:51], v[32:35]\n"
"s_xor_b32 s98, s31, 270336\n"
"v_rcp_f32 v177, v177\n"
"v_mfma_f32_16x16x32_bf16 v[32:35], a[212:215], v[96:99], v[32:35]\n"
"s_waitcnt vmcnt(17)\n"
"ds_write_b128 v5, v[244:247] offset:17408\n"
"v_mfma_f32_16x16x32_bf16 v[208:211], a[212:215], v[76:79], v[208:211]\n"
"s_waitcnt vmcnt(16)\n"
"buffer_load_dwordx4 a[200:203], v0, s[64:67], s98 offen offset:0 sc1\n"
"v_mfma_f32_16x16x32_bf16 v[208:211], a[216:219], a[132:135], v[208:211]\n"
"s_waitcnt vmcnt(15)\n"
"v_rcp_f32 v178, v178\n"
"v_mfma_f32_16x16x32_bf16 v[208:211], a[220:223], v[44:47], v[208:211]\n"
"s_xor_b32 s98, s31, 274432\n"
"v_rcp_f32 v179, v179\n"
"v_mfma_f32_16x16x32_bf16 v[208:211], a[224:227], v[60:63], v[208:211]\n"
"ds_write_b128 v5, v[32:35] offset:16384\n"
"v_mul_f32 v156, v156, v172\n"
"v_mfma_f32_16x16x32_bf16 v[224:227], a[240:243], v[60:63], v[224:227]\n"
"buffer_load_dwordx4 a[196:199], v0, s[64:67], s98 offen offset:0 sc1\n"
"v_mul_f32 v157, v157, v173\n"
"v_mfma_f32_16x16x32_bf16 v[224:227], a[236:239], v[44:47], v[224:227]\n"
"s_xor_b32 s98, s31, 525312\n"
"v_mul_f32 v158, v158, v174\n"
"v_mul_f32 v159, v159, v175\n"
"v_mfma_f32_16x16x32_bf16 v[224:227], a[232:235], a[132:135], v[224:227]\n"
"v_mul_f32 v160, v160, v176\n"
"v_mul_f32 v161, v161, v177\n"
"v_mfma_f32_16x16x32_bf16 v[224:227], a[228:231], v[76:79], v[224:227]\n"
"buffer_load_dwordx4 v[116:119], v0, s[64:67], s98 offen offset:0 sc1\n"
"ds_write_b128 v6, v[208:211] offset:32768\n"
"v_mfma_f32_16x16x32_bf16 v[240:243], a[244:247], v[76:79], v[240:243]\n"
"v_mul_f32 v162, v162, v178\n"
"v_mul_f32 v163, v163, v179\n"
"v_mfma_f32_16x16x32_bf16 v[240:243], a[248:251], a[132:135], v[240:243]\n"
"s_xor_b32 s98, s31, 529408\n"
"v_cvt_pk_bf16_f32 v157, v156, v157\n"
"v_cvt_pk_bf16_f32 v159, v158, v159\n"
"v_mfma_f32_16x16x32_bf16 v[240:243], a[252:255], v[44:47], v[240:243]\n"
"v_cvt_pk_bf16_f32 v161, v160, v161\n"
"v_cvt_pk_bf16_f32 v163, v162, v163\n"
"v_mfma_f32_16x16x32_bf16 v[240:243], a[144:147], v[60:63], v[240:243]\n"
"buffer_load_dwordx4 a[120:123], v0, s[64:67], s98 offen offset:0 sc1\n"
"ds_write_b128 v6, v[224:227] offset:33792\n"
"v_mfma_f32_16x16x32_bf16 v[24:27], a[192:195], v[60:63], v[24:27]\n"
"v_lshlrev_b32 v156, 16, v157\n"
"v_and_b32 v157, 0xffff0000, v157\n"
"v_mfma_f32_16x16x32_bf16 v[24:27], a[188:191], v[44:47], v[24:27]\n"
"s_xor_b32 s98, s31, 533504\n"
"v_lshlrev_b32 v158, 16, v159\n"
"v_and_b32 v159, 0xffff0000, v159\n"
"v_mfma_f32_16x16x32_bf16 v[24:27], a[184:187], a[132:135], v[24:27]\n"
"s_waitcnt vmcnt(16)\n"
"v_lshlrev_b32 v160, 16, v161\n"
"v_and_b32 v161, 0xffff0000, v161\n"
"v_mfma_f32_16x16x32_bf16 v[24:27], a[180:183], v[76:79], v[24:27]\n"
"buffer_load_dwordx4 a[136:139], v0, s[64:67], s98 offen offset:0 sc1\n"
"ds_write_b128 v6, v[240:243] offset:34816\n"
"v_mfma_f32_16x16x32_bf16 v[204:207], a[180:183], a[208:211], v[204:207]\n"
"s_xor_b32 s98, s31, 537600\n"
"v_lshlrev_b32 v162, 16, v163\n"
"v_and_b32 v163, 0xffff0000, v163\n"
"v_mfma_f32_16x16x32_bf16 v[204:207], a[184:187], v[136:139], v[204:207]\n"
"s_waitcnt vmcnt(16)\n"
"v_lshlrev_b32 v164, 16, v144\n"
"v_and_b32 v165, 0xffff0000, v144\n"
"v_mfma_f32_16x16x32_bf16 v[204:207], a[188:191], v[104:107], v[204:207]\n"
"s_waitcnt vmcnt(15)\n"
"buffer_load_dwordx4 v[48:51], v0, s[64:67], s98 offen offset:0 sc1\n"
"v_lshlrev_b32 v166, 16, v145\n"
"v_mfma_f32_16x16x32_bf16 v[204:207], a[192:195], v[88:91], v[204:207]\n"
"ds_write_b128 v6, v[24:27] offset:35840\n"
"v_and_b32 v167, 0xffff0000, v145\n"
"v_mfma_f32_16x16x32_bf16 v[220:223], a[144:147], v[88:91], v[220:223]\n"
"s_xor_b32 s98, s31, 526336\n"
"v_lshlrev_b32 v168, 16, v146\n"
"v_and_b32 v169, 0xffff0000, v146\n"
"v_mfma_f32_16x16x32_bf16 v[220:223], a[252:255], v[104:107], v[220:223]\n"
"v_lshlrev_b32 v170, 16, v147\n"
"v_and_b32 v171, 0xffff0000, v147\n"
"v_mfma_f32_16x16x32_bf16 v[220:223], a[248:251], v[136:139], v[220:223]\n"
"buffer_load_dwordx4 v[96:99], v0, s[64:67], s98 offen offset:0 sc1\n"
"v_sub_f32 v164, v164, v148\n"
"v_mfma_f32_16x16x32_bf16 v[220:223], a[244:247], a[208:211], v[220:223]\n"
"ds_write_b128 v7, v[204:207] offset:52224\n"
"v_sub_f32 v165, v165, v148\n"
"v_mfma_f32_16x16x32_bf16 v[236:239], a[228:231], a[208:211], v[236:239]\n"
"s_xor_b32 s98, s31, 530432\n"
"v_sub_f32 v166, v166, v148\n"
"v_sub_f32 v167, v167, v148\n"
"v_mfma_f32_16x16x32_bf16 v[236:239], a[232:235], v[136:139], v[236:239]\n"
"v_sub_f32 v168, v168, v148\n"
"v_sub_f32 v169, v169, v148\n"
"v_mfma_f32_16x16x32_bf16 v[236:239], a[236:239], v[104:107], v[236:239]\n"
"buffer_load_dwordx4 v[60:63], v0, s[64:67], s98 offen offset:0 sc1\n"
"v_sub_f32 v170, v170, v148\n"
"v_mfma_f32_16x16x32_bf16 v[236:239], a[240:243], v[88:91], v[236:239]\n"
"s_xor_b32 s98, s31, 534528\n"
"ds_write_b128 v7, v[220:223] offset:51200\n"
"v_sub_f32 v171, v171, v148\n"
"v_mfma_f32_16x16x32_bf16 v[252:255], a[224:227], v[88:91], v[252:255]\n"
"v_cvt_pk_bf16_f32 v165, v164, v165\n"
"v_cvt_pk_bf16_f32 v167, v166, v167\n"
"v_mfma_f32_16x16x32_bf16 v[252:255], a[220:223], v[104:107], v[252:255]\n"
"buffer_load_dwordx4 v[44:47], v0, s[64:67], s98 offen offset:0 sc1\n"
"v_cvt_pk_bf16_f32 v169, v168, v169\n"
"v_mfma_f32_16x16x32_bf16 v[252:255], a[216:219], v[136:139], v[252:255]\n"
"s_waitcnt vmcnt(18)\n"
"v_cvt_pk_bf16_f32 v171, v170, v171\n"
"v_lshlrev_b32 v164, 16, v165\n"
"v_mfma_f32_16x16x32_bf16 v[252:255], a[212:215], a[208:211], v[252:255]\n"
"s_xor_b32 s98, s31, 538624\n"
".Lpg11:\n"
"s_cbranch_execz .Lpgret0\n"
"s_nop 0\n"
"ds_write_b128 v7, v[236:239] offset:50176\n"
"v_and_b32 v165, 0xffff0000, v165\n"
"v_mfma_f32_16x16x32_bf16 v[216:219], a[212:215], a[124:127], v[216:219]\n"
"s_waitcnt vmcnt(17)\n"
"v_lshlrev_b32 v166, 16, v167\n"
"v_and_b32 v167, 0xffff0000, v167\n"
"v_mfma_f32_16x16x32_bf16 v[216:219], a[216:219], v[80:83], v[216:219]\n"
"s_waitcnt vmcnt(16)\n"
"buffer_load_dwordx4 a[132:135], v0, s[64:67], s98 offen offset:0 sc1\n"
"v_lshlrev_b32 v168, 16, v169\n"
"v_mfma_f32_16x16x32_bf16 v[216:219], a[220:223], v[84:87], v[216:219]\n"
"s_waitcnt vmcnt(16)\n"
"v_and_b32 v169, 0xffff0000, v169\n"
"v_lshlrev_b32 v170, 16, v171\n"
"v_mfma_f32_16x16x32_bf16 v[216:219], a[224:227], v[100:103], v[216:219]\n"
"s_xor_b32 s98, s31, 527360\n"
"ds_write_b128 v7, v[252:255] offset:49152\n"
"v_and_b32 v171, 0xffff0000, v171\n"
"v_mfma_f32_16x16x32_bf16 v[232:235], a[240:243], v[100:103], v[232:235]\n"
"v_mul_f32 v164, v164, v149\n"
"v_mul_f32 v165, v165, v149\n"
"v_mfma_f32_16x16x32_bf16 v[232:235], a[236:239], v[84:87], v[232:235]\n"
"buffer_load_dwordx4 v[76:79], v0, s[64:67], s98 offen offset:0 sc1\n"
"v_mul_f32 v166, v166, v149\n"
"v_mfma_f32_16x16x32_bf16 v[232:235], a[232:235], v[80:83], v[232:235]\n"
"s_waitcnt lgkmcnt(0)\n"
"v_mul_f32 v167, v167, v149\n"
"v_mul_f32 v168, v168, v149\n"
"v_mfma_f32_16x16x32_bf16 v[232:235], a[228:231], a[124:127], v[232:235]\n"
"s_xor_b32 s98, s31, 531456\n"
"v_mul_f32 v169, v169, v149\n"
"v_mul_f32 v170, v170, v149\n"
"v_mfma_f32_16x16x32_bf16 v[248:251], a[244:247], a[124:127], v[248:251]\n"
"s_barrier\n"
"v_mul_f32 v171, v171, v149\n"
"v_cvt_pk_bf16_f32 v165, v164, v165\n"
"v_mfma_f32_16x16x32_bf16 v[248:251], a[248:251], v[80:83], v[248:251]\n"
"buffer_load_dwordx4 v[88:91], v0, s[64:67], s98 offen offset:0 sc1\n"
"ds_read_b128 v[32:35], v4 offset:16384\n"
"v_mfma_f32_16x16x32_bf16 v[248:251], a[252:255], v[84:87], v[248:251]\n"
"s_xor_b32 s98, s31, 535552\n"
"ds_read_b128 v[208:211], v4 offset:32768\n"
"v_cvt_pk_bf16_f32 v167, v166, v167\n"
"v_mfma_f32_16x16x32_bf16 v[248:251], a[144:147], v[100:103], v[248:251]\n"
"ds_read_b128 v[252:255], v4 offset:49152\n"
"v_cvt_pk_bf16_f32 v169, v168, v169\n"
"v_mfma_f32_16x16x32_bf16 v[16:19], a[192:195], v[100:103], v[16:19]\n"
"buffer_load_dwordx4 v[104:107], v0, s[64:67], s98 offen offset:0 sc1\n"
"ds_read_b128 v[244:247], v4 offset:17408\n"
"v_mfma_f32_16x16x32_bf16 v[16:19], a[188:191], v[84:87], v[16:19]\n"
"ds_read_b128 v[224:227], v4 offset:33792\n"
"v_cvt_pk_bf16_f32 v171, v170, v171\n"
"v_mfma_f32_16x16x32_bf16 v[16:19], a[184:187], v[80:83], v[16:19]\n"
"s_xor_b32 s98, s31, 539648\n"
"ds_read_b128 v[236:239], v4 offset:50176\n"
"v_lshlrev_b32 v164, 16, v165\n"
"v_mfma_f32_16x16x32_bf16 v[16:19], a[180:183], a[124:127], v[16:19]\n"
"s_waitcnt vmcnt(17)\n"
"ds_read_b128 v[228:231], v4 offset:18432\n"
"v_and_b32 v165, 0xffff0000, v165\n"
"v_mfma_f32_16x16x32_bf16 a[176:179], a[160:163], v[64:67], a[176:179]\n"
"buffer_load_dwordx4 v[136:139], v0, s[64:67], s98 offen offset:0 sc1\n"
"ds_read_b128 v[240:243], v4 offset:34816\n"
"v_mfma_f32_16x16x32_bf16 a[176:179], a[96:99], v[92:95], a[176:179]\n"
"s_waitcnt vmcnt(16)\n"
"ds_read_b128 v[220:223], v4 offset:51200\n"
"v_lshlrev_b32 v166, 16, v167\n"
"v_mfma_f32_16x16x32_bf16 a[176:179], a[112:115], v[108:111], a[176:179]\n"
"s_xor_b32 s98, s31, 524288\n"
"ds_read_b128 v[212:215], v4 offset:19456\n"
"v_and_b32 v167, 0xffff0000, v167\n"
"v_mfma_f32_16x16x32_bf16 a[176:179], a[12:15], v[112:115], a[176:179]\n"
"s_waitcnt lgkmcnt(8)\n"
"ds_read_b128 v[24:27], v4 offset:35840\n"
"v_lshlrev_b32 v168, 16, v169\n"
"v_mfma_f32_16x16x32_bf16 a[172:175], a[8:11], v[112:115], a[172:175]\n"
"buffer_load_dwordx4 a[208:211], v0, s[64:67], s98 offen offset:0 sc1\n"
"ds_read_b128 v[204:207], v4 offset:52224\n"
"v_mfma_f32_16x16x32_bf16 a[172:175], a[108:111], v[108:111], a[172:175]\n"
"s_xor_b32 s98, s31, 528384\n"
"v_add_f32 v32, v32, v208\n"
"v_add_f32 v33, v33, v209\n"
"v_mfma_f32_16x16x32_bf16 a[172:175], a[92:95], v[92:95], a[172:175]\n"
"v_add_f32 v34, v34, v210\n"
"v_add_f32 v35, v35, v211\n"
"v_mfma_f32_16x16x32_bf16 a[172:175], a[156:159], v[64:67], a[172:175]\n"
"s_waitcnt lgkmcnt(9)\n"
"buffer_load_dwordx4 v[100:103], v0, s[64:67], s98 offen offset:0 sc1\n"
"v_add_f32 v216, v216, v252\n"
"v_mfma_f32_16x16x32_bf16 a[168:171], a[152:155], v[64:67], a[168:171]\n"
"v_add_f32 v217, v217, v253\n"
"v_add_f32 v218, v218, v254\n"
"v_mfma_f32_16x16x32_bf16 a[168:171], a[88:91], v[92:95], a[168:171]\n"
"s_xor_b32 s98, s31, 532480\n"
"v_add_f32 v219, v219, v255\n"
"v_add_f32 v216, v216, v32\n"
"v_mfma_f32_16x16x32_bf16 a[168:171], a[104:107], v[108:111], a[168:171]\n"
"v_add_f32 v217, v217, v33\n"
"v_add_f32 v218, v218, v34\n"
"v_mfma_f32_16x16x32_bf16 a[168:171], a[4:7], v[112:115], a[168:171]\n"
"buffer_load_dwordx4 v[84:87], v0, s[64:67], s98 offen offset:0 sc1\n"
"v_add_f32 v219, v219, v35\n"
"v_mfma_f32_16x16x32_bf16 a[164:167], a[0:3], v[112:115], a[164:167]\n"
"v_perm_b32 v216, v216, v12, s8\n"
"v_perm_b32 v217, v217, v12, s9\n"
"v_mfma_f32_16x16x32_bf16 a[164:167], a[100:103], v[108:111], a[164:167]\n"
"s_xor_b32 s98, s31, 536576\n"
"v_perm_b32 v218, v218, v12, s10\n"
"v_perm_b32 v219, v219, v12, s11\n"
"v_mfma_f32_16x16x32_bf16 a[164:167], a[84:87], v[92:95], a[164:167]\n"
"s_waitcnt lgkmcnt(3)\n"
"buffer_store_dwordx4 v[216:219], v0, s[64:67], s99 offen offset:0 sc1\n"
"v_add_f32 v244, v244, v224\n"
"v_mfma_f32_16x16x32_bf16 a[164:167], a[148:151], v[64:67], a[164:167]\n"
"s_waitcnt vmcnt(18)\n"
"buffer_load_dwordx4 v[80:83], v0, s[64:67], s98 offen offset:0 sc1\n"
"v_add_f32 v245, v245, v225\n"
"v_mfma_f32_16x16x32_bf16 v[120:123], a[148:151], a[116:119], v[120:123]\n"
"s_xor_b32 s98, s31, 787456\n"
"v_add_f32 v246, v246, v226\n"
"v_add_f32 v247, v247, v227\n"
"v_mfma_f32_16x16x32_bf16 v[120:123], a[84:87], a[204:207], v[120:123]\n"
"s_waitcnt vmcnt(18)\n"
"v_add_f32 v232, v232, v236\n"
"v_add_f32 v233, v233, v237\n"
"v_mfma_f32_16x16x32_bf16 v[120:123], a[100:103], a[200:203], v[120:123]\n"
"s_waitcnt vmcnt(17)\n"
"buffer_load_dwordx4 a[124:127], v0, s[64:67], s98 offen offset:0 sc1\n"
"v_add_f32 v234, v234, v238\n"
"v_mfma_f32_16x16x32_bf16 v[120:123], a[0:3], a[196:199], v[120:123]\n"
"v_add_f32 v235, v235, v239\n"
"v_add_f32 v232, v232, v244\n"
"v_mfma_f32_16x16x32_bf16 v[124:127], a[4:7], a[196:199], v[124:127]\n"
"s_xor_b32 s98, s31, 791552\n"
"v_add_f32 v233, v233, v245\n"
"v_add_f32 v234, v234, v246\n"
"v_mfma_f32_16x16x32_bf16 v[124:127], a[104:107], a[200:203], v[124:127]\n"
"v_add_f32 v235, v235, v247\n"
"v_perm_b32 v232, v232, v12, s8\n"
"v_mfma_f32_16x16x32_bf16 v[124:127], a[88:91], a[204:207], v[124:127]\n"
"buffer_load_dwordx4 v[112:115], v0, s[64:67], s98 offen offset:0 sc1\n"
"v_perm_b32 v233, v233, v12, s9\n"
"v_mfma_f32_16x16x32_bf16 v[124:127], a[152:155], a[116:119], v[124:127]\n"
"v_perm_b32 v234, v234, v12, s10\n"
"v_perm_b32 v235, v235, v12, s11\n"
"v_mfma_f32_16x16x32_bf16 v[128:131], a[156:159], a[116:119], v[128:131]\n"
"s_xor_b32 s98, s31, 795648\n"
"buffer_store_dwordx4 v[232:235], v0, s[64:67], s99 offen offset:1024 sc1\n"
"v_add_f32 v228, v228, v240\n"
"v_mfma_f32_16x16x32_bf16 v[128:131], a[92:95], a[204:207], v[128:131]\n"
"v_add_f32 v229, v229, v241\n"
"v_add_f32 v230, v230, v242\n"
"v_mfma_f32_16x16x32_bf16 v[128:131], a[108:111], a[200:203], v[128:131]\n"
"buffer_load_dwordx4 v[108:111], v0, s[64:67], s98 offen offset:0 sc1\n"
"v_add_f32 v231, v231, v243\n"
"v_mfma_f32_16x16x32_bf16 v[128:131], a[8:11], a[196:199], v[128:131]\n"
"v_add_f32 v248, v248, v220\n"
"v_add_f32 v249, v249, v221\n"
"v_mfma_f32_16x16x32_bf16 v[132:135], a[12:15], a[196:199], v[132:135]\n"
"s_xor_b32 s98, s31, 799744\n"
"v_add_f32 v250, v250, v222\n"
"v_add_f32 v251, v251, v223\n"
"v_mfma_f32_16x16x32_bf16 v[132:135], a[112:115], a[200:203], v[132:135]\n"
"v_add_f32 v248, v248, v228\n"
"v_add_f32 v249, v249, v229\n"
"v_mfma_f32_16x16x32_bf16 v[132:135], a[96:99], a[204:207], v[132:135]\n"
"s_waitcnt vmcnt(20)\n"
"buffer_load_dwordx4 v[92:95], v0, s[64:67], s98 offen offset:0 sc1\n"
"v_add_f32 v250, v250, v230\n"
"v_mfma_f32_16x16x32_bf16 v[132:135], a[160:163], a[116:119], v[132:135]\n"
"s_xor_b32 s98, s31, 788480\n"
"v_add_f32 v251, v251, v231\n"
"v_perm_b32 v248, v248, v12, s8\n"
"v_mfma_f32_16x16x32_bf16 a[32:35], v[56:59], v[116:119], a[32:35]\n"
"s_waitcnt vmcnt(20)\n"
"v_perm_b32 v249, v249, v12, s9\n"
"v_perm_b32 v250, v250, v12, s10\n"
"v_mfma_f32_16x16x32_bf16 a[32:35], a[140:143], a[120:123], a[32:35]\n"
"s_waitcnt vmcnt(19)\n"
"buffer_load_dwordx4 v[64:67], v0, s[64:67], s98 offen offset:0 sc1\n"
"v_perm_b32 v251, v251, v12, s11\n"
"v_mfma_f32_16x16x32_bf16 a[32:35], a[76:79], a[136:139], a[32:35]\n"
"s_waitcnt vmcnt(19) lgkmcnt(0)\n"
"buffer_store_dwordx4 v[248:251], v0, s[64:67], s99 offen offset:2048 sc1\n"
"v_add_f32 v212, v212, v24\n"
"v_mfma_f32_16x16x32_bf16 a[32:35], a[68:71], v[48:51], a[32:35]\n"
"s_xor_b32 s98, s31, 792576\n"
"v_add_f32 v213, v213, v25\n"
"v_add_f32 v214, v214, v26\n"
"v_mfma_f32_16x16x32_bf16 a[28:31], a[52:55], v[48:51], a[28:31]\n"
"v_add_f32 v215, v215, v27\n"
"v_add_f32 v16, v16, v204\n"
"v_mfma_f32_16x16x32_bf16 a[28:31], a[60:63], a[136:139], a[28:31]\n"
"buffer_load_dwordx4 a[196:199], v0, s[64:67], s98 offen offset:0 sc1\n"
"v_add_f32 v17, v17, v205\n"
"v_mfma_f32_16x16x32_bf16 a[28:31], v[52:55], a[120:123], a[28:31]\n"
"v_add_f32 v18, v18, v206\n"
"v_add_f32 v19, v19, v207\n"
"v_mfma_f32_16x16x32_bf16 a[28:31], v[40:43], v[116:119], a[28:31]\n"
"s_xor_b32 s98, s31, 796672\n"
"v_add_f32 v16, v16, v212\n"
"v_add_f32 v17, v17, v213\n"
"v_mfma_f32_16x16x32_bf16 a[24:27], a[128:131], v[116:119], a[24:27]\n"
"v_add_f32 v18, v18, v214\n"
"v_add_f32 v19, v19, v215\n"
"v_mfma_f32_16x16x32_bf16 a[24:27], v[68:71], a[120:123], a[24:27]\n"
"buffer_load_dwordx4 a[200:203], v0, s[64:67], s98 offen offset:0 sc1\n"
"v_and_b32 v169, 0xffff0000, v169\n"
"v_mfma_f32_16x16x32_bf16 a[24:27], a[80:83], a[136:139], a[24:27]\n"
"v_perm_b32 v16, v16, v12, s8\n"
"v_lshlrev_b32 v170, 16, v171\n"
"v_mfma_f32_16x16x32_bf16 a[24:27], a[72:75], v[48:51], a[24:27]\n"
"v_and_b32 v171, 0xffff0000, v171\n"
"v_lshlrev_b32 v172, 16, v200\n"
"v_mfma_f32_16x16x32_bf16 a[20:23], a[56:59], v[48:51], a[20:23]\n"
"s_xor_b32 s98, s31, 800768\n"
"v_perm_b32 v17, v17, v12, s9\n"
"v_and_b32 v173, 0xffff0000, v200\n"
"v_mfma_f32_16x16x32_bf16 a[20:23], a[64:67], a[136:139], a[20:23]\n"
"v_lshlrev_b32 v174, 16, v201\n"
"v_perm_b32 v18, v18, v12, s10\n"
"v_mfma_f32_16x16x32_bf16 a[20:23], v[72:75], a[120:123], a[20:23]\n"
"buffer_load_dwordx4 a[204:207], v0, s[64:67], s98 offen offset:0 sc1\n"
"v_and_b32 v175, 0xffff0000, v201\n"
"v_mfma_f32_16x16x32_bf16 a[20:23], a[16:19], v[116:119], a[20:23]\n"
"s_waitcnt vmcnt(22)\n"
"v_lshlrev_b32 v176, 16, v202\n"
"v_perm_b32 v19, v19, v12, s11\n"
"v_mfma_f32_16x16x32_bf16 a[36:39], a[16:19], v[96:99], a[36:39]\n"
"s_waitcnt vmcnt(21)\n"
"v_and_b32 v177, 0xffff0000, v202\n"
"v_lshlrev_b32 v178, 16, v203\n"
"v_mfma_f32_16x16x32_bf16 a[36:39], v[72:75], v[60:63], a[36:39]\n"
"s_waitcnt vmcnt(20)\n"
"buffer_store_dwordx4 v[16:19], v0, s[64:67], s99 offen offset:3072 sc1\n"
"v_and_b32 v179, 0xffff0000, v203\n"
"v_mfma_f32_16x16x32_bf16 a[36:39], a[64:67], v[44:47], a[36:39]\n"
"s_waitcnt vmcnt(20)\n"
"v_add_f32 v164, v164, v172\n"
"v_add_f32 v165, v165, v173\n"
"v_mfma_f32_16x16x32_bf16 a[36:39], a[56:59], a[132:135], a[36:39]\n"
"v_add_f32 v166, v166, v174\n"
"v_add_f32 v167, v167, v175\n"
"v_mfma_f32_16x16x32_bf16 a[40:43], a[72:75], a[132:135], a[40:43]\n"
"s_xor_b32 s98, s31, 789504\n"
"v_add_f32 v168, v168, v176\n"
"v_add_f32 v169, v169, v177\n"
"v_mfma_f32_16x16x32_bf16 a[40:43], a[80:83], v[44:47], a[40:43]\n"
"v_add_f32 v170, v170, v178\n"
"v_add_f32 v171, v171, v179\n"
"v_mfma_f32_16x16x32_bf16 a[40:43], v[68:71], v[60:63], a[40:43]\n"
"buffer_load_dwordx4 a[116:119], v0, s[64:67], s98 offen offset:0 sc1\n"
"v_cvt_pk_bf16_f32 v165, v164, v165\n"
"v_mfma_f32_16x16x32_bf16 a[40:43], a[128:131], v[96:99], a[40:43]\n"
"v_cvt_pk_bf16_f32 v167, v166, v167\n"
"v_cvt_pk_bf16_f32 v169, v168, v169\n"
"v_mfma_f32_16x16x32_bf16 a[44:47], v[40:43], v[96:99], a[44:47]\n"
"v_add_u32 v4, 0x10000, v4\n"
"v_cvt_pk_bf16_f32 v171, v170, v171\n"
"v_mfma_f32_16x16x32_bf16 a[44:47], v[52:55], v[60:63], a[44:47]\n"
"v_lshlrev_b32 v164, 16, v165\n"
"v_add_u32 v5, 0x10000, v5\n"
"v_mfma_f32_16x16x32_bf16 a[44:47], a[60:63], v[44:47], a[44:47]\n"
"v_and_b32 v165, 0xffff0000, v165\n"
"v_add_u32 v6, 0x10000, v6\n"
"v_mfma_f32_16x16x32_bf16 a[44:47], a[52:55], a[132:135], a[44:47]\n"
"v_lshlrev_b32 v166, 16, v167\n"
"v_and_b32 v167, 0xffff0000, v167\n"
"v_mfma_f32_16x16x32_bf16 a[48:51], a[68:71], a[132:135], a[48:51]\n"
"s_waitcnt vmcnt(20)\n"
"v_lshlrev_b32 v168, 16, v169\n"
"v_add_u32 v7, 0x10000, v7\n"
"v_mfma_f32_16x16x32_bf16 a[48:51], a[76:79], v[44:47], a[48:51]\n"
"s_xor_b32 s98, s31, 793600\n"
"v_and_b32 v169, 0xffff0000, v169\n"
"v_lshlrev_b32 v170, 16, v171\n"
"v_mfma_f32_16x16x32_bf16 a[48:51], a[140:143], v[60:63], a[48:51]\n"
"s_xor_b32 s99, s99, 131072\n"
"v_and_b32 v171, 0xffff0000, v171\n"
"v_mul_f32 v172, 0xbfb8aa3b, v164\n"
"v_mfma_f32_16x16x32_bf16 a[48:51], v[56:59], v[96:99], a[48:51]\n"
"s_bfe_u32 s8, s92, 524288\n"
"buffer_load_dwordx4 v[48:51], v0, s[64:67], s98 offen offset:0 sc1\n"
"v_mul_f32 v173, 0xbfb8aa3b, v165\n"
"v_mfma_f32_16x16x32_bf16 a[176:179], v[56:59], v[76:79], a[176:179]\n"
"s_waitcnt vmcnt(18)\n"
"s_bfe_u32 s9, s92, 524296\n"
"v_mul_f32 v174, 0xbfb8aa3b, v166\n"
"v_mul_f32 v175, 0xbfb8aa3b, v167\n"
"v_mfma_f32_16x16x32_bf16 a[176:179], a[140:143], v[88:91], a[176:179]\n"
"s_bfe_u32 s10, s92, 524304\n"
"v_mul_f32 v176, 0xbfb8aa3b, v168\n"
"v_mul_f32 v177, 0xbfb8aa3b, v169\n"
"v_mfma_f32_16x16x32_bf16 a[176:179], a[76:79], v[104:107], a[176:179]\n"
"s_bfe_u32 s11, s92, 524312\n"
"v_mul_f32 v178, 0xbfb8aa3b, v170\n"
"v_mul_f32 v179, 0xbfb8aa3b, v171\n"
"v_mfma_f32_16x16x32_bf16 a[176:179], a[68:71], v[136:139], a[176:179]\n"
"v_exp_f32 v172, v172\n"
"v_mfma_f32_16x16x32_bf16 a[172:175], a[52:55], v[136:139], a[172:175]\n"
"v_exp_f32 v173, v173\n"
"v_mfma_f32_16x16x32_bf16 a[172:175], a[60:63], v[104:107], a[172:175]\n"
"s_xor_b32 s98, s31, 797696\n"
"v_exp_f32 v174, v174\n"
"v_mfma_f32_16x16x32_bf16 a[172:175], v[52:55], v[88:91], a[172:175]\n"
"v_exp_f32 v175, v175\n"
"v_mfma_f32_16x16x32_bf16 a[172:175], v[40:43], v[76:79], a[172:175]\n"
"buffer_load_dwordx4 a[136:139], v0, s[64:67], s98 offen offset:0 sc1\n"
"v_mfma_f32_16x16x32_bf16 a[168:171], a[128:131], v[76:79], a[168:171]\n"
"v_exp_f32 v176, v176\n"
"v_mfma_f32_16x16x32_bf16 a[168:171], v[68:71], v[88:91], a[168:171]\n"
"v_exp_f32 v177, v177\n"
"v_mfma_f32_16x16x32_bf16 a[168:171], a[80:83], v[104:107], a[168:171]\n"
"v_exp_f32 v178, v178\n"
"v_mfma_f32_16x16x32_bf16 a[168:171], a[72:75], v[136:139], a[168:171]\n"
"v_exp_f32 v179, v179\n"
"v_mfma_f32_16x16x32_bf16 a[164:167], a[56:59], v[136:139], a[164:167]\n"
"v_add_f32 v172, 1.0, v172\n"
"v_add_f32 v173, 1.0, v173\n"
"v_mfma_f32_16x16x32_bf16 a[164:167], a[64:67], v[104:107], a[164:167]\n"
"s_xor_b32 s98, s31, 801792\n"
"v_add_f32 v174, 1.0, v174\n"
"v_add_f32 v175, 1.0, v175\n"
"v_mfma_f32_16x16x32_bf16 a[164:167], v[72:75], v[88:91], a[164:167]\n"
"v_add_f32 v176, 1.0, v176\n"
"v_add_f32 v177, 1.0, v177\n"
"v_mfma_f32_16x16x32_bf16 a[164:167], a[16:19], v[76:79], a[164:167]\n"
"s_waitcnt vmcnt(18)\n"
"buffer_load_dwordx4 a[120:123], v0, s[64:67], s98 offen offset:0 sc1\n"
"v_add_f32 v178, 1.0, v178\n"
"v_mfma_f32_16x16x32_bf16 v[120:123], a[16:19], a[208:211], v[120:123]\n"
"s_waitcnt vmcnt(18)\n"
"v_add_f32 v179, 1.0, v179\n"
"v_mfma_f32_16x16x32_bf16 v[120:123], v[72:75], v[100:103], v[120:123]\n"
"s_waitcnt vmcnt(17)\n"
"v_rcp_f32 v172, v172\n"
"v_mfma_f32_16x16x32_bf16 v[120:123], a[64:67], v[84:87], v[120:123]\n"
"s_waitcnt vmcnt(15)\n"
"v_rcp_f32 v173, v173\n"
"v_mfma_f32_16x16x32_bf16 v[120:123], a[56:59], v[80:83], v[120:123]\n"
"v_rcp_f32 v174, v174\n"
"v_mfma_f32_16x16x32_bf16 v[124:127], a[72:75], v[80:83], v[124:127]\n"
"v_rcp_f32 v175, v175\n"
"v_mfma_f32_16x16x32_bf16 v[124:127], a[80:83], v[84:87], v[124:127]\n"
"s_xor_b32 s98, s31, 786432\n"
"v_rcp_f32 v176, v176\n"
"v_mfma_f32_16x16x32_bf16 v[124:127], v[68:71], v[100:103], v[124:127]\n"
"v_rcp_f32 v177, v177\n"
"v_mfma_f32_16x16x32_bf16 v[124:127], a[128:131], a[208:211], v[124:127]\n"
"buffer_load_dwordx4 v[116:119], v0, s[64:67], s98 offen offset:0 sc1\n"
"v_mfma_f32_16x16x32_bf16 v[128:131], v[40:43], a[208:211], v[128:131]\n"
"v_rcp_f32 v178, v178\n"
"v_mfma_f32_16x16x32_bf16 v[128:131], v[52:55], v[100:103], v[128:131]\n"
"v_rcp_f32 v179, v179\n"
"v_mfma_f32_16x16x32_bf16 v[128:131], a[60:63], v[84:87], v[128:131]\n"
"v_mul_f32 v164, v164, v172\n"
"v_mul_f32 v165, v165, v173\n"
"v_mfma_f32_16x16x32_bf16 v[128:131], a[52:55], v[80:83], v[128:131]\n"
"v_mul_f32 v166, v166, v174\n"
"v_mul_f32 v167, v167, v175\n"
"v_mfma_f32_16x16x32_bf16 v[132:135], a[68:71], v[80:83], v[132:135]\n"
"v_mul_f32 v168, v168, v176\n"
"v_mul_f32 v169, v169, v177\n"
"v_mfma_f32_16x16x32_bf16 v[132:135], a[76:79], v[84:87], v[132:135]\n"
"v_mul_f32 v170, v170, v178\n"
"v_mul_f32 v171, v171, v179\n"
"v_mfma_f32_16x16x32_bf16 v[132:135], a[140:143], v[100:103], v[132:135]\n"
"s_xor_b32 s98, s31, 790528\n"
"v_cvt_pk_bf16_f32 v165, v164, v165\n"
"v_cvt_pk_bf16_f32 v167, v166, v167\n"
"v_mfma_f32_16x16x32_bf16 v[132:135], v[56:59], a[208:211], v[132:135]\n"
"s_waitcnt vmcnt(15)\n"
"v_cvt_pk_bf16_f32 v169, v168, v169\n"
"v_cvt_pk_bf16_f32 v171, v170, v171\n"
"v_mfma_f32_16x16x32_bf16 a[32:35], a[180:183], a[124:127], a[32:35]\n"
"s_waitcnt vmcnt(14)\n"
"buffer_load_dwordx4 a[132:135], v0, s[64:67], s98 offen offset:0 sc1\n"
"v_lshlrev_b32 v164, 16, v165\n"
"v_mfma_f32_16x16x32_bf16 a[32:35], a[184:187], v[112:115], a[32:35]\n"
"s_waitcnt vmcnt(13)\n"
"v_and_b32 v165, 0xffff0000, v165\n"
"v_lshlrev_b32 v166, 16, v167\n"
"v_mfma_f32_16x16x32_bf16 a[32:35], a[188:191], v[108:111], a[32:35]\n"
"s_waitcnt vmcnt(12)\n"
"v_and_b32 v167, 0xffff0000, v167\n"
"v_lshlrev_b32 v168, 16, v169\n"
"v_mfma_f32_16x16x32_bf16 a[32:35], a[192:195], v[92:95], a[32:35]\n"
"v_and_b32 v169, 0xffff0000, v169\n"
"v_lshlrev_b32 v170, 16, v171\n"
"v_mfma_f32_16x16x32_bf16 a[28:31], a[144:147], v[92:95], a[28:31]\n"
"v_and_b32 v171, 0xffff0000, v171\n"
"v_lshlrev_b32 v172, 16, v140\n"
"v_mfma_f32_16x16x32_bf16 a[28:31], a[252:255], v[108:111], a[28:31]\n"
"v_and_b32 v173, 0xffff0000, v140\n"
"v_lshlrev_b32 v174, 16, v141\n"
"v_mfma_f32_16x16x32_bf16 a[28:31], a[248:251], v[112:115], a[28:31]\n"
"s_xor_b32 s98, s31, 794624\n"
"v_and_b32 v175, 0xffff0000, v141\n"
"v_lshlrev_b32 v176, 16, v142\n"
"v_mfma_f32_16x16x32_bf16 a[28:31], a[244:247], a[124:127], a[28:31]\n"
"ds_write_b128 v5, a[32:35] offset:19456\n"
"v_and_b32 v177, 0xffff0000, v142\n"
"v_mfma_f32_16x16x32_bf16 a[24:27], a[228:231], a[124:127], a[24:27]\n"
"buffer_load_dwordx4 v[44:47], v0, s[64:67], s98 offen offset:0 sc1\n"
"v_lshlrev_b32 v178, 16, v143\n"
"v_mfma_f32_16x16x32_bf16 a[24:27], a[232:235], v[112:115], a[24:27]\n"
"v_and_b32 v179, 0xffff0000, v143\n"
"v_lshlrev_b32 v196, 16, v144\n"
"v_mfma_f32_16x16x32_bf16 a[24:27], a[236:239], v[108:111], a[24:27]\n"
"v_and_b32 v197, 0xffff0000, v144\n"
"v_lshlrev_b32 v198, 16, v145\n"
"v_mfma_f32_16x16x32_bf16 a[24:27], a[240:243], v[92:95], a[24:27]\n"
"ds_write_b128 v5, a[28:31] offset:18432\n"
"v_and_b32 v199, 0xffff0000, v145\n"
"v_mfma_f32_16x16x32_bf16 a[20:23], a[224:227], v[92:95], a[20:23]\n"
"v_lshlrev_b32 v200, 16, v146\n"
"v_and_b32 v201, 0xffff0000, v146\n"
"v_mfma_f32_16x16x32_bf16 a[20:23], a[220:223], v[108:111], a[20:23]\n"
"v_lshlrev_b32 v202, 16, v147\n"
"v_and_b32 v203, 0xffff0000, v147\n"
"v_mfma_f32_16x16x32_bf16 a[20:23], a[216:219], v[112:115], a[20:23]\n"
"s_xor_b32 s98, s31, 798720\n"
"v_mfma_f32_16x16x32_bf16 a[20:23], a[212:215], a[124:127], a[20:23]\n"
"s_waitcnt vmcnt(12)\n"
"ds_write_b128 v5, a[24:27] offset:17408\n"
"v_mfma_f32_16x16x32_bf16 a[36:39], a[212:215], v[64:67], a[36:39]\n"
"s_waitcnt vmcnt(10)\n"
"buffer_load_dwordx4 v[60:63], v0, s[64:67], s98 offen offset:0 sc1\n"
"v_mfma_f32_16x16x32_bf16 a[36:39], a[216:219], a[196:199], a[36:39]\n"
"s_waitcnt vmcnt(10)\n"
"v_mfma_f32_16x16x32_bf16 a[36:39], a[220:223], a[200:203], a[36:39]\n"
"s_waitcnt vmcnt(9)\n"
"v_mfma_f32_16x16x32_bf16 a[36:39], a[224:227], a[204:207], a[36:39]\n"
"v_mfma_f32_16x16x32_bf16 a[40:43], a[240:243], a[204:207], a[40:43]\n"
"v_mfma_f32_16x16x32_bf16 a[40:43], a[236:239], a[200:203], a[40:43]\n"
"ds_write_b128 v5, a[20:23] offset:16384\n"
"v_mfma_f32_16x16x32_bf16 a[40:43], a[232:235], a[196:199], a[40:43]\n"
"v_mfma_f32_16x16x32_bf16 a[40:43], a[228:231], v[64:67], a[40:43]\n"
"v_mfma_f32_16x16x32_bf16 a[44:47], a[244:247], v[64:67], a[44:47]\n"
"v_mfma_f32_16x16x32_bf16 a[44:47], a[248:251], a[196:199], a[44:47]\n"
"v_mfma_f32_16x16x32_bf16 a[44:47], a[252:255], a[200:203], a[44:47]\n"
"ds_write_b128 v6, a[36:39] offset:32768\n"
"v_mfma_f32_16x16x32_bf16 a[44:47], a[144:147], a[204:207], a[44:47]\n"
"v_mfma_f32_16x16x32_bf16 a[48:51], a[192:195], a[204:207], a[48:51]\n"
"v_mfma_f32_16x16x32_bf16 a[48:51], a[188:191], a[200:203], a[48:51]\n"
"v_mfma_f32_16x16x32_bf16 a[48:51], a[184:187], a[196:199], a[48:51]\n"
"ds_write_b128 v6, a[40:43] offset:33792\n"
"v_mfma_f32_16x16x32_bf16 a[48:51], a[180:183], v[64:67], a[48:51]\n"
"s_waitcnt vmcnt(7)\n"
"v_mfma_f32_16x16x32_bf16 a[176:179], a[180:183], a[116:119], a[176:179]\n"
"s_waitcnt vmcnt(6)\n"
"v_mfma_f32_16x16x32_bf16 a[176:179], a[184:187], v[48:51], a[176:179]\n"
"s_waitcnt vmcnt(5)\n"
"v_mfma_f32_16x16x32_bf16 a[176:179], a[188:191], a[136:139], a[176:179]\n"
"s_waitcnt vmcnt(4)\n"
"ds_write_b128 v6, a[44:47] offset:34816\n"
"v_mfma_f32_16x16x32_bf16 a[176:179], a[192:195], a[120:123], a[176:179]\n"
"v_mfma_f32_16x16x32_bf16 a[172:175], a[144:147], a[120:123], a[172:175]\n"
"v_mfma_f32_16x16x32_bf16 a[172:175], a[252:255], a[136:139], a[172:175]\n"
"v_mfma_f32_16x16x32_bf16 a[172:175], a[248:251], v[48:51], a[172:175]\n"
"ds_write_b128 v6, a[48:51] offset:35840\n"
"v_mfma_f32_16x16x32_bf16 a[172:175], a[244:247], a[116:119], a[172:175]\n"
"v_mfma_f32_16x16x32_bf16 a[168:171], a[228:231], a[116:119], a[168:171]\n"
"v_mfma_f32_16x16x32_bf16 a[168:171], a[232:235], v[48:51], a[168:171]\n"
"v_mfma_f32_16x16x32_bf16 a[168:171], a[236:239], a[136:139], a[168:171]\n"
"ds_write_b128 v7, a[176:179] offset:52224\n"
"v_mfma_f32_16x16x32_bf16 a[168:171], a[240:243], a[120:123], a[168:171]\n"
"v_mfma_f32_16x16x32_bf16 a[164:167], a[224:227], a[120:123], a[164:167]\n"
"v_mfma_f32_16x16x32_bf16 a[164:167], a[220:223], a[136:139], a[164:167]\n"
"v_mfma_f32_16x16x32_bf16 a[164:167], a[216:219], v[48:51], a[164:167]\n"
"ds_write_b128 v7, a[172:175] offset:51200\n"
"v_mfma_f32_16x16x32_bf16 a[164:167], a[212:215], a[116:119], a[164:167]\n"
"s_waitcnt vmcnt(3)\n"
"v_mfma_f32_16x16x32_bf16 v[120:123], a[212:215], v[116:119], v[120:123]\n"
"s_waitcnt vmcnt(2)\n"
"v_mfma_f32_16x16x32_bf16 v[120:123], a[216:219], a[132:135], v[120:123]\n"
"s_waitcnt vmcnt(1)\n"
"v_mfma_f32_16x16x32_bf16 v[120:123], a[220:223], v[44:47], v[120:123]\n"
"s_waitcnt vmcnt(0)\n"
"ds_write_b128 v7, a[168:171] offset:50176\n"
"v_mfma_f32_16x16x32_bf16 v[120:123], a[224:227], v[60:63], v[120:123]\n"
"v_mfma_f32_16x16x32_bf16 v[124:127], a[240:243], v[60:63], v[124:127]\n"
"buffer_load_dwordx4 v[180:183], v0, s[64:67], s99 offen offset:0 sc1\n"
"v_mfma_f32_16x16x32_bf16 v[124:127], a[236:239], v[44:47], v[124:127]\n"
"ds_write_b128 v7, a[164:167] offset:49152\n"
"v_mfma_f32_16x16x32_bf16 v[124:127], a[232:235], a[132:135], v[124:127]\n"
"v_mfma_f32_16x16x32_bf16 v[124:127], a[228:231], v[116:119], v[124:127]\n"
"buffer_load_dwordx4 v[184:187], v0, s[64:67], s99 offen offset:1024 sc1\n"
"v_mfma_f32_16x16x32_bf16 v[128:131], a[244:247], v[116:119], v[128:131]\n"
"s_waitcnt lgkmcnt(0)\n"
"v_mfma_f32_16x16x32_bf16 v[128:131], a[248:251], a[132:135], v[128:131]\n"
"s_barrier\n"
"v_mfma_f32_16x16x32_bf16 v[128:131], a[252:255], v[44:47], v[128:131]\n"
"buffer_load_dwordx4 v[188:191], v0, s[64:67], s99 offen offset:2048 sc1\n"
"ds_read_b128 v[32:35], v4 offset:16384\n"
"v_mfma_f32_16x16x32_bf16 v[128:131], a[144:147], v[60:63], v[128:131]\n"
"ds_read_b128 v[208:211], v4 offset:32768\n"
"v_mfma_f32_16x16x32_bf16 v[132:135], a[192:195], v[60:63], v[132:135]\n"
"ds_read_b128 v[252:255], v4 offset:49152\n"
"v_mfma_f32_16x16x32_bf16 v[132:135], a[188:191], v[44:47], v[132:135]\n"
"buffer_load_dwordx4 v[192:195], v0, s[64:67], s99 offen offset:3072 sc1\n"
"ds_read_b128 v[244:247], v4 offset:17408\n"
"v_mfma_f32_16x16x32_bf16 v[132:135], a[184:187], a[132:135], v[132:135]\n"
"ds_read_b128 v[224:227], v4 offset:33792\n"
"v_mfma_f32_16x16x32_bf16 v[132:135], a[180:183], v[116:119], v[132:135]\n"
"ds_read_b128 v[236:239], v4 offset:50176\n"
"ds_read_b128 v[228:231], v4 offset:18432\n"
"ds_read_b128 v[240:243], v4 offset:34816\n"
"ds_read_b128 v[220:223], v4 offset:51200\n"
"ds_read_b128 v[212:215], v4 offset:19456\n"
"ds_read_b128 v[24:27], v4 offset:35840\n"
"ds_read_b128 v[204:207], v4 offset:52224\n"
"s_waitcnt lgkmcnt(10)\n"
"v_pk_add_f32 v[32:33], v[32:33], v[208:209]\n"
"s_waitcnt lgkmcnt(9)\n"
"v_pk_add_f32 v[120:121], v[120:121], v[252:253]\n"
"v_pk_add_f32 v[34:35], v[34:35], v[210:211]\n"
"v_pk_add_f32 v[122:123], v[122:123], v[254:255]\n"
"v_pk_add_f32 v[120:121], v[120:121], v[32:33]\n"
"v_pk_add_f32 v[122:123], v[122:123], v[34:35]\n"
"s_waitcnt lgkmcnt(7)\n"
"v_pk_add_f32 v[244:245], v[244:245], v[224:225]\n"
"s_waitcnt lgkmcnt(6)\n"
"v_pk_add_f32 v[124:125], v[124:125], v[236:237]\n"
"v_pk_add_f32 v[246:247], v[246:247], v[226:227]\n"
"v_pk_add_f32 v[126:127], v[126:127], v[238:239]\n"
"v_pk_add_f32 v[124:125], v[124:125], v[244:245]\n"
"v_pk_add_f32 v[126:127], v[126:127], v[246:247]\n"
"s_waitcnt lgkmcnt(4)\n"
"v_pk_add_f32 v[228:229], v[228:229], v[240:241]\n"
"s_waitcnt lgkmcnt(3)\n"
"v_pk_add_f32 v[128:129], v[128:129], v[220:221]\n"
"v_pk_add_f32 v[230:231], v[230:231], v[242:243]\n"
"v_pk_add_f32 v[130:131], v[130:131], v[222:223]\n"
"v_pk_add_f32 v[128:129], v[128:129], v[228:229]\n"
"v_pk_add_f32 v[130:131], v[130:131], v[230:231]\n"
"s_waitcnt lgkmcnt(1)\n"
"v_pk_add_f32 v[212:213], v[212:213], v[24:25]\n"
"s_waitcnt lgkmcnt(0)\n"
"v_pk_add_f32 v[132:133], v[132:133], v[204:205]\n"
"v_pk_add_f32 v[214:215], v[214:215], v[26:27]\n"
"v_pk_add_f32 v[134:135], v[134:135], v[206:207]\n"
"v_pk_add_f32 v[132:133], v[132:133], v[212:213]\n"
"v_pk_add_f32 v[134:135], v[134:135], v[214:215]\n"
"s_mov_b32 s26, 0\n"
"s_waitcnt vmcnt(0)\n"
".Lxpc:\n"
"v_xor_b32 v180, s8, v180\n"
"v_xor_b32 v181, s9, v181\n"
"v_xor_b32 v182, s10, v182\n"
"v_xor_b32 v183, s11, v183\n"
"v_xor_b32 v184, s8, v184\n"
"v_xor_b32 v185, s9, v185\n"
"v_xor_b32 v186, s10, v186\n"
"v_xor_b32 v187, s11, v187\n"
"v_xor_b32 v188, s8, v188\n"
"v_xor_b32 v189, s9, v189\n"
"v_xor_b32 v190, s10, v190\n"
"v_xor_b32 v191, s11, v191\n"
"v_xor_b32 v192, s8, v192\n"
"v_xor_b32 v193, s9, v193\n"
"v_xor_b32 v194, s10, v194\n"
"v_xor_b32 v195, s11, v195\n"
"v_or_b32 v3, v180, v181\n"
"v_or3_b32 v3, v3, v182, v183\n"
"v_or3_b32 v3, v3, v184, v185\n"
"v_or3_b32 v3, v3, v186, v187\n"
"v_or3_b32 v3, v3, v188, v189\n"
"v_or3_b32 v3, v3, v190, v191\n"
"v_or3_b32 v3, v3, v192, v193\n"
"v_or3_b32 v3, v3, v194, v195\n"
"v_and_b32 v3, 0xff, v3\n"
"v_cmp_ne_u32 vcc, 0, v3\n"
"s_nop 1\n"
"s_cbranch_vccnz .Lxpr\n"
".Lxpk:\n"
"v_pk_add_f32 v[120:121], v[120:121], v[180:181]\n"
"v_pk_add_f32 v[122:123], v[122:123], v[182:183]\n"
"v_pk_add_f32 v[124:125], v[124:125], v[184:185]\n"
"v_pk_add_f32 v[126:127], v[126:127], v[186:187]\n"
"v_pk_fma_f32 v[120:121], v[120:121], v[156:157], v[172:173]\n"
"v_pk_fma_f32 v[122:123], v[122:123], v[158:159], v[174:175]\n"
"v_pk_fma_f32 v[124:125], v[124:125], v[160:161], v[176:177]\n"
"v_pk_fma_f32 v[126:127], v[126:127], v[162:163], v[178:179]\n"
"v_cvt_pk_bf16_f32 v140, v120, v121\n"
"v_cvt_pk_bf16_f32 v141, v122, v123\n"
"v_cvt_pk_bf16_f32 v142, v124, v125\n"
"v_cvt_pk_bf16_f32 v143, v126, v127\n"
"buffer_store_dwordx4 v[140:143], v150, s[68:71], 0 offen offset:0 nt\n"
"v_pk_add_f32 v[128:129], v[128:129], v[188:189]\n"
"v_pk_add_f32 v[130:131], v[130:131], v[190:191]\n"
"v_pk_add_f32 v[132:133], v[132:133], v[192:193]\n"
"v_pk_add_f32 v[134:135], v[134:135], v[194:195]\n"
"v_pk_fma_f32 v[128:129], v[128:129], v[164:165], v[196:197]\n"
"v_pk_fma_f32 v[130:131], v[130:131], v[166:167], v[198:199]\n"
"v_pk_fma_f32 v[132:133], v[132:133], v[168:169], v[200:201]\n"
"v_pk_fma_f32 v[134:135], v[134:135], v[170:171], v[202:203]\n"
"v_cvt_pk_bf16_f32 v144, v128, v129\n"
"v_cvt_pk_bf16_f32 v145, v130, v131\n"
"v_cvt_pk_bf16_f32 v146, v132, v133\n"
"v_cvt_pk_bf16_f32 v147, v134, v135\n"
"buffer_store_dwordx4 v[144:147], v150, s[68:71], 0 offen offset:16 nt\n"
"s_endpgm\n"
".Lpg12:\n"
"s_branch .Lpgret0\n"
".Lxpr:\n"
"buffer_load_dwordx4 v[180:183], v0, s[64:67], s99 offen offset:0 sc1\n"
"buffer_load_dwordx4 v[184:187], v0, s[64:67], s99 offen offset:1024 sc1\n"
"buffer_load_dwordx4 v[188:191], v0, s[64:67], s99 offen offset:2048 sc1\n"
"buffer_load_dwordx4 v[192:195], v0, s[64:67], s99 offen offset:3072 sc1\n"
"s_add_u32 s26, s26, 1\n"
"s_cmp_lt_u32 s26, 0x8000\n"
"s_waitcnt vmcnt(0)\n"
"s_cbranch_scc1 .Lxpc\n"
"s_branch .Lxpk\n"
".Lrt0:\n"
"s_nop 7\n"
"s_nop 7\n"
"s_mov_b32 s26, 0\n"
".Lrl0:\n"
"s_mov_b32 s24, 4096\n"
"buffer_load_dwordx2 v[44:45], v91, s[64:67], s24 offen offset:0 sc1\n"
"buffer_load_dwordx2 v[46:47], v91, s[64:67], s24 offen offset:128 sc1\n"
"buffer_load_dwordx2 v[48:49], v91, s[64:67], s24 offen offset:256 sc1\n"
"buffer_load_dwordx2 v[50:51], v91, s[64:67], s24 offen offset:384 sc1\n"
"s_lshl_b32 s24, s17, 12\n"
"s_add_u32 s24, s24, 32768\n"
"buffer_load_dwordx4 v[60:63], v0, s[64:67], s24 offen offset:0 sc1\n"
"buffer_load_dwordx4 v[64:67], v0, s[64:67], s24 offen offset:1024 sc1\n"
"buffer_load_dwordx4 v[68:71], v0, s[64:67], s24 offen offset:2048 sc1\n"
"buffer_load_dwordx4 v[72:75], v0, s[64:67], s24 offen offset:3072 sc1\n"
"s_waitcnt vmcnt(0) lgkmcnt(0)\n"
"v_xor_b32 v52, s92, v44\n"
"v_bitop3_b32 v52, v52, v46, s92 bitop3:0xf6\n"
"v_bitop3_b32 v52, v52, v48, s92 bitop3:0xf6\n"
"v_bitop3_b32 v52, v52, v50, s92 bitop3:0xf6\n"
"v_bitop3_b32 v52, v52, v60, s92 bitop3:0xf6\n"
"v_bitop3_b32 v52, v52, v62, s92 bitop3:0xf6\n"
"v_bitop3_b32 v52, v52, v64, s92 bitop3:0xf6\n"
"v_bitop3_b32 v52, v52, v66, s92 bitop3:0xf6\n"
"v_bitop3_b32 v52, v52, v68, s92 bitop3:0xf6\n"
"v_bitop3_b32 v52, v52, v70, s92 bitop3:0xf6\n"
"v_bitop3_b32 v52, v52, v72, s92 bitop3:0xf6\n"
"v_bitop3_b32 v52, v52, v74, s92 bitop3:0xf6\n"
"v_cmp_ne_u32 vcc, 0, v52\n"
"s_nop 4\n"
"s_cbranch_vccz .Lbk0\n"
"s_sleep 2\n"
"s_add_u32 s26, s26, 1\n"
"s_cmp_lt_u32 s26, 0x4000\n"
"s_cbranch_scc1 .Lrl0\n"
"s_branch .Lbk0\n"
".Lrt1:\n"
"s_nop 7\n"
"s_nop 7\n"
"s_mov_b32 s26, 0\n"
".Lrl1:\n"
"s_lshl_b32 s24, s91, 5\n"
"s_add_u32 s24, s24, s38\n"
"s_lshl_b32 s24, s24, 9\n"
"s_add_u32 s24, s24, 131072\n"
"buffer_load_dword v192, v193, s[64:67], s24 offen sc1\n"
"s_waitcnt vmcnt(0) lgkmcnt(0)\n"
"v_cmp_ne_u32 vcc, s92, v192\n"
"s_nop 4\n"
"s_cbranch_vccz .Lbk1\n"
"s_sleep 2\n"
"s_add_u32 s26, s26, 1\n"
"s_cmp_lt_u32 s26, 0x4000\n"
"s_cbranch_scc1 .Lrl1\n"
"s_branch .Lbk1\n"
".Lrt2:\n"
"s_nop 7\n"
"s_nop 7\n"
"s_mov_b32 s26, 0\n"
".Lrl2:\n"
"s_mov_b32 s8, 4096\n"
"buffer_load_dwordx2 v[88:89], v84, s[64:67], s8 offen sc1\n"
"s_waitcnt vmcnt(0) lgkmcnt(0)\n"
"v_cmp_ne_u32 s[28:29], s92, v88\n"
"s_nop 4\n"
"s_cmp_lg_u64 s[28:29], 0\n"
"s_cbranch_scc0 .Lbk2\n"
"s_sleep 2\n"
"s_add_u32 s26, s26, 1\n"
"s_cmp_lt_u32 s26, 0x4000\n"
"s_cbranch_scc1 .Lrl2\n"
"s_branch .Lbk2\n"
".Lrt3:\n"
"s_nop 7\n"
"s_nop 7\n"
"s_mov_b32 s26, 0\n"
".Lrl3:\n"
"buffer_load_dwordx4 v[236:239], v235, s[64:67], s37 offen offset:0 sc1\n"
"buffer_load_dwordx4 v[240:243], v235, s[64:67], s37 offen offset:1024 sc1\n"
"buffer_load_dwordx4 v[244:247], v235, s[64:67], s37 offen offset:2048 sc1\n"
"buffer_load_dwordx4 v[248:251], v235, s[64:67], s37 offen offset:3072 sc1\n"
"s_waitcnt vmcnt(0) lgkmcnt(0)\n"
"v_xor_b32 v228, s92, v236\n"
"v_bitop3_b32 v228, v228, v238, s92 bitop3:0xf6\n"
"v_bitop3_b32 v228, v228, v240, s92 bitop3:0xf6\n"
"v_bitop3_b32 v228, v228, v242, s92 bitop3:0xf6\n"
"v_bitop3_b32 v228, v228, v244, s92 bitop3:0xf6\n"
"v_bitop3_b32 v228, v228, v246, s92 bitop3:0xf6\n"
"v_bitop3_b32 v228, v228, v248, s92 bitop3:0xf6\n"
"v_bitop3_b32 v228, v228, v250, s92 bitop3:0xf6\n"
"v_cmp_ne_u32 s[28:29], 0, v228\n"
"s_nop 1\n"
"s_and_b64 s[28:29], s[28:29], s[82:83]\n"
"s_nop 4\n"
"s_cmp_lg_u64 s[28:29], 0\n"
"s_cbranch_scc0 .Lbk3\n"
"s_sleep 2\n"
"s_add_u32 s26, s26, 1\n"
"s_cmp_lt_u32 s26, 0x4000\n"
"s_cbranch_scc1 .Lrl3\n"
"s_branch .Lbk3\n"
".Lrt4:\n"
"s_nop 7\n"
"s_nop 7\n"
"s_mov_b32 s26, 0\n"
".Lrl4:\n"
"s_add_u32 s25, s35, 0\n"
"buffer_load_dword v2, v1, s[64:67], s25 offen sc1\n"
"s_waitcnt vmcnt(0) lgkmcnt(0)\n"
"v_cmp_ne_u32 vcc, s92, v2\n"
"s_nop 4\n"
"s_cbranch_vccz .Lbk4\n"
"s_sleep 2\n"
"s_add_u32 s26, s26, 1\n"
"s_cmp_lt_u32 s26, 0x4000\n"
"s_cbranch_scc1 .Lrl4\n"
"s_branch .Lbk4\n"
".Lyq:\n"
"s_nop 7\n"
"s_nop 7\n"
"s_mov_b32 s26, 0\n"
"s_waitcnt vmcnt(6)\n"
"s_branch .Lyqc\n"
".Lyql:\n"
"s_sleep 2\n"
"buffer_load_dword v190, v191, s[64:67], s39 offen sc1\n"
"s_waitcnt vmcnt(0)\n"
".Lyqc:\n"
"v_cmp_ne_u32 vcc, s92, v190\n"
"s_nop 4\n"
"s_cbranch_vccz .Lyqk\n"
"s_add_u32 s26, s26, 1\n"
"s_cmp_lt_u32 s26, 0x4000\n"
"s_cbranch_scc1 .Lyql\n"
".Lyqk:\n"
"s_cmp_eq_u32 s8, 0\n"
"s_cbranch_scc0 .Lyq1\n"
"v_bfe_u32 v189, v0, 4, 4\n"
"v_lshrrev_b32 v188, 1, v0\n"
"v_and_b32 v188, 0x180, v188\n"
"v_lshl_add_u32 v188, v189, 12, v188\n"
"s_lshl_b32 s96, s91, 9\n"
"s_add_u32 s96, s96, 1572864\n"
"buffer_load_dwordx4 v[140:143], v188, s[64:67], s96 offen offset:0 sc1\n"
"buffer_load_dwordx4 v[144:147], v188, s[64:67], s96 offen offset:16 sc1\n"
"buffer_load_dwordx4 v[148:151], v188, s[64:67], s96 offen offset:32 sc1\n"
"buffer_load_dwordx4 v[152:155], v188, s[64:67], s96 offen offset:48 sc1\n"
"s_add_u32 s96, s96, 65536\n"
"buffer_load_dwordx4 v[156:159], v188, s[64:67], s96 offen offset:0 sc1\n"
"buffer_load_dwordx4 v[160:163], v188, s[64:67], s96 offen offset:16 sc1\n"
"buffer_load_dwordx4 v[164:167], v188, s[64:67], s96 offen offset:32 sc1\n"
"buffer_load_dwordx4 v[168:171], v188, s[64:67], s96 offen offset:48 sc1\n"
"s_add_u32 s96, s96, 65536\n"
"v_mov_b32 v190, 2130706432\n"
"v_cmp_gt_u32 vcc, 11, v189\n"
"s_nop 1\n"
"v_cndmask_b32 v188, v190, v188, vcc\n"
"buffer_load_dwordx4 v[172:175], v188, s[64:67], s96 offen offset:0 sc1\n"
"buffer_load_dwordx4 v[176:179], v188, s[64:67], s96 offen offset:16 sc1\n"
"buffer_load_dwordx4 v[180:183], v188, s[64:67], s96 offen offset:32 sc1\n"
"buffer_load_dwordx4 v[184:187], v188, s[64:67], s96 offen offset:48 sc1\n"
"s_add_u32 s96, s96, 65536\n"
"s_branch .Lyqb\n"
".Lyq1:\n"
"v_bfe_u32 v189, v0, 4, 4\n"
"v_lshrrev_b32 v188, 1, v0\n"
"v_and_b32 v188, 0x180, v188\n"
"v_lshl_add_u32 v188, v189, 12, v188\n"
"s_lshl_b32 s96, s91, 9\n"
"s_add_u32 s96, s96, 1572928\n"
"buffer_load_dwordx4 v[140:143], v188, s[64:67], s96 offen offset:0 sc1\n"
"buffer_load_dwordx4 v[144:147], v188, s[64:67], s96 offen offset:16 sc1\n"
"buffer_load_dwordx4 v[148:151], v188, s[64:67], s96 offen offset:32 sc1\n"
"buffer_load_dwordx4 v[152:155], v188, s[64:67], s96 offen offset:48 sc1\n"
"s_add_u32 s96, s96, 65536\n"
"buffer_load_dwordx4 v[156:159], v188, s[64:67], s96 offen offset:0 sc1\n"
"buffer_load_dwordx4 v[160:163], v188, s[64:67], s96 offen offset:16 sc1\n"
"buffer_load_dwordx4 v[164:167], v188, s[64:67], s96 offen offset:32 sc1\n"
"buffer_load_dwordx4 v[168:171], v188, s[64:67], s96 offen offset:48 sc1\n"
"s_add_u32 s96, s96, 65536\n"
"v_mov_b32 v190, 2130706432\n"
"v_cmp_gt_u32 vcc, 11, v189\n"
"s_nop 1\n"
"v_cndmask_b32 v188, v190, v188, vcc\n"
"buffer_load_dwordx4 v[172:175], v188, s[64:67], s96 offen offset:0 sc1\n"
"buffer_load_dwordx4 v[176:179], v188, s[64:67], s96 offen offset:16 sc1\n"
"buffer_load_dwordx4 v[180:183], v188, s[64:67], s96 offen offset:32 sc1\n"
"buffer_load_dwordx4 v[184:187], v188, s[64:67], s96 offen offset:48 sc1\n"
"s_add_u32 s96, s96, 65536\n"
"s_branch .Lyqb\n"
".Lyg0:\n"
"s_nop 7\n"
"s_nop 7\n"
"s_andn2_b32 s95, 15, s13\n"
"s_waitcnt vmcnt(8)\n"
"v_pk_add_f32 v[140:141], v[140:141], v[156:157]\n"
"v_pk_add_f32 v[142:143], v[142:143], v[158:159]\n"
"v_pk_add_f32 v[144:145], v[144:145], v[160:161]\n"
"v_pk_add_f32 v[146:147], v[146:147], v[162:163]\n"
"v_pk_add_f32 v[148:149], v[148:149], v[164:165]\n"
"v_pk_add_f32 v[150:151], v[150:151], v[166:167]\n"
"v_pk_add_f32 v[152:153], v[152:153], v[168:169]\n"
"v_pk_add_f32 v[154:155], v[154:155], v[170:171]\n"
"v_pk_add_f32 v[140:141], v[140:141], v[172:173]\n"
"v_pk_add_f32 v[142:143], v[142:143], v[174:175]\n"
"v_pk_add_f32 v[144:145], v[144:145], v[176:177]\n"
"v_pk_add_f32 v[146:147], v[146:147], v[178:179]\n"
"v_pk_add_f32 v[148:149], v[148:149], v[180:181]\n"
"v_pk_add_f32 v[150:151], v[150:151], v[182:183]\n"
"v_pk_add_f32 v[152:153], v[152:153], v[184:185]\n"
"v_pk_add_f32 v[154:155], v[154:155], v[186:187]\n"
"v_add_f32_dpp v140, v140, v140 quad_perm:[1,0,3,2] row_mask:0xf bank_mask:0xf\n"
"v_add_f32_dpp v141, v141, v141 quad_perm:[1,0,3,2] row_mask:0xf bank_mask:0xf\n"
"v_add_f32_dpp v142, v142, v142 quad_perm:[1,0,3,2] row_mask:0xf bank_mask:0xf\n"
"v_add_f32_dpp v143, v143, v143 quad_perm:[1,0,3,2] row_mask:0xf bank_mask:0xf\n"
"v_add_f32_dpp v144, v144, v144 quad_perm:[1,0,3,2] row_mask:0xf bank_mask:0xf\n"
"v_add_f32_dpp v145, v145, v145 quad_perm:[1,0,3,2] row_mask:0xf bank_mask:0xf\n"
"v_add_f32_dpp v146, v146, v146 quad_perm:[1,0,3,2] row_mask:0xf bank_mask:0xf\n"
"v_add_f32_dpp v147, v147, v147 quad_perm:[1,0,3,2] row_mask:0xf bank_mask:0xf\n"
"v_add_f32_dpp v148, v148, v148 quad_perm:[1,0,3,2] row_mask:0xf bank_mask:0xf\n"
"v_add_f32_dpp v149, v149, v149 quad_perm:[1,0,3,2] row_mask:0xf bank_mask:0xf\n"
"v_add_f32_dpp v150, v150, v150 quad_perm:[1,0,3,2] row_mask:0xf bank_mask:0xf\n"
"v_add_f32_dpp v151, v151, v151 quad_perm:[1,0,3,2] row_mask:0xf bank_mask:0xf\n"
"v_add_f32_dpp v152, v152, v152 quad_perm:[1,0,3,2] row_mask:0xf bank_mask:0xf\n"
"v_add_f32_dpp v153, v153, v153 quad_perm:[1,0,3,2] row_mask:0xf bank_mask:0xf\n"
"v_add_f32_dpp v154, v154, v154 quad_perm:[1,0,3,2] row_mask:0xf bank_mask:0xf\n"
"v_add_f32_dpp v155, v155, v155 quad_perm:[1,0,3,2] row_mask:0xf bank_mask:0xf\n"
"v_add_f32_dpp v140, v140, v140 quad_perm:[2,3,0,1] row_mask:0xf bank_mask:0xf\n"
"v_add_f32_dpp v141, v141, v141 quad_perm:[2,3,0,1] row_mask:0xf bank_mask:0xf\n"
"v_add_f32_dpp v142, v142, v142 quad_perm:[2,3,0,1] row_mask:0xf bank_mask:0xf\n"
"v_add_f32_dpp v143, v143, v143 quad_perm:[2,3,0,1] row_mask:0xf bank_mask:0xf\n"
"v_add_f32_dpp v144, v144, v144 quad_perm:[2,3,0,1] row_mask:0xf bank_mask:0xf\n"
"v_add_f32_dpp v145, v145, v145 quad_perm:[2,3,0,1] row_mask:0xf bank_mask:0xf\n"
"v_add_f32_dpp v146, v146, v146 quad_perm:[2,3,0,1] row_mask:0xf bank_mask:0xf\n"
"v_add_f32_dpp v147, v147, v147 quad_perm:[2,3,0,1] row_mask:0xf bank_mask:0xf\n"
"v_add_f32_dpp v148, v148, v148 quad_perm:[2,3,0,1] row_mask:0xf bank_mask:0xf\n"
"v_add_f32_dpp v149, v149, v149 quad_perm:[2,3,0,1] row_mask:0xf bank_mask:0xf\n"
"v_add_f32_dpp v150, v150, v150 quad_perm:[2,3,0,1] row_mask:0xf bank_mask:0xf\n"
"v_add_f32_dpp v151, v151, v151 quad_perm:[2,3,0,1] row_mask:0xf bank_mask:0xf\n"
"v_add_f32_dpp v152, v152, v152 quad_perm:[2,3,0,1] row_mask:0xf bank_mask:0xf\n"
"v_add_f32_dpp v153, v153, v153 quad_perm:[2,3,0,1] row_mask:0xf bank_mask:0xf\n"
"v_add_f32_dpp v154, v154, v154 quad_perm:[2,3,0,1] row_mask:0xf bank_mask:0xf\n"
"v_add_f32_dpp v155, v155, v155 quad_perm:[2,3,0,1] row_mask:0xf bank_mask:0xf\n"
"v_add_f32_dpp v140, v140, v140 row_half_mirror row_mask:0xf bank_mask:0xf\n"
"v_add_f32_dpp v141, v141, v141 row_half_mirror row_mask:0xf bank_mask:0xf\n"
"v_add_f32_dpp v142, v142, v142 row_half_mirror row_mask:0xf bank_mask:0xf\n"
"v_add_f32_dpp v143, v143, v143 row_half_mirror row_mask:0xf bank_mask:0xf\n"
"v_add_f32_dpp v144, v144, v144 row_half_mirror row_mask:0xf bank_mask:0xf\n"
"v_add_f32_dpp v145, v145, v145 row_half_mirror row_mask:0xf bank_mask:0xf\n"
"v_add_f32_dpp v146, v146, v146 row_half_mirror row_mask:0xf bank_mask:0xf\n"
"v_add_f32_dpp v147, v147, v147 row_half_mirror row_mask:0xf bank_mask:0xf\n"
"v_add_f32_dpp v148, v148, v148 row_half_mirror row_mask:0xf bank_mask:0xf\n"
"v_add_f32_dpp v149, v149, v149 row_half_mirror row_mask:0xf bank_mask:0xf\n"
"v_add_f32_dpp v150, v150, v150 row_half_mirror row_mask:0xf bank_mask:0xf\n"
"v_add_f32_dpp v151, v151, v151 row_half_mirror row_mask:0xf bank_mask:0xf\n"
"v_add_f32_dpp v152, v152, v152 row_half_mirror row_mask:0xf bank_mask:0xf\n"
"v_add_f32_dpp v153, v153, v153 row_half_mirror row_mask:0xf bank_mask:0xf\n"
"v_add_f32_dpp v154, v154, v154 row_half_mirror row_mask:0xf bank_mask:0xf\n"
"v_add_f32_dpp v155, v155, v155 row_half_mirror row_mask:0xf bank_mask:0xf\n"
"v_add_f32_dpp v140, v140, v140 row_mirror row_mask:0xf bank_mask:0xf\n"
"v_add_f32_dpp v141, v141, v141 row_mirror row_mask:0xf bank_mask:0xf\n"
"v_add_f32_dpp v142, v142, v142 row_mirror row_mask:0xf bank_mask:0xf\n"
"v_add_f32_dpp v143, v143, v143 row_mirror row_mask:0xf bank_mask:0xf\n"
"v_add_f32_dpp v144, v144, v144 row_mirror row_mask:0xf bank_mask:0xf\n"
"v_add_f32_dpp v145, v145, v145 row_mirror row_mask:0xf bank_mask:0xf\n"
"v_add_f32_dpp v146, v146, v146 row_mirror row_mask:0xf bank_mask:0xf\n"
"v_add_f32_dpp v147, v147, v147 row_mirror row_mask:0xf bank_mask:0xf\n"
"v_add_f32_dpp v148, v148, v148 row_mirror row_mask:0xf bank_mask:0xf\n"
"v_add_f32_dpp v149, v149, v149 row_mirror row_mask:0xf bank_mask:0xf\n"
"v_add_f32_dpp v150, v150, v150 row_mirror row_mask:0xf bank_mask:0xf\n"
"v_add_f32_dpp v151, v151, v151 row_mirror row_mask:0xf bank_mask:0xf\n"
"v_add_f32_dpp v152, v152, v152 row_mirror row_mask:0xf bank_mask:0xf\n"
"v_add_f32_dpp v153, v153, v153 row_mirror row_mask:0xf bank_mask:0xf\n"
"v_add_f32_dpp v154, v154, v154 row_mirror row_mask:0xf bank_mask:0xf\n"
"v_add_f32_dpp v155, v155, v155 row_mirror row_mask:0xf bank_mask:0xf\n"
"v_cvt_pk_bf16_f32 v141, v140, v141\n"
"v_cvt_pk_bf16_f32 v143, v142, v143\n"
"v_cvt_pk_bf16_f32 v145, v144, v145\n"
"v_cvt_pk_bf16_f32 v147, v146, v147\n"
"v_cvt_pk_bf16_f32 v149, v148, v149\n"
"v_cvt_pk_bf16_f32 v151, v150, v151\n"
"v_cvt_pk_bf16_f32 v153, v152, v153\n"
"v_cvt_pk_bf16_f32 v155, v154, v155\n"
"v_lshlrev_b32 v140, 16, v141\n"
"v_and_b32 v141, 0xffff0000, v141\n"
"v_lshlrev_b32 v142, 16, v143\n"
"v_and_b32 v143, 0xffff0000, v143\n"
"v_lshlrev_b32 v144, 16, v145\n"
"v_and_b32 v145, 0xffff0000, v145\n"
"v_lshlrev_b32 v146, 16, v147\n"
"v_and_b32 v147, 0xffff0000, v147\n"
"v_lshlrev_b32 v148, 16, v149\n"
"v_and_b32 v149, 0xffff0000, v149\n"
"v_lshlrev_b32 v150, 16, v151\n"
"v_and_b32 v151, 0xffff0000, v151\n"
"v_lshlrev_b32 v152, 16, v153\n"
"v_and_b32 v153, 0xffff0000, v153\n"
"v_lshlrev_b32 v154, 16, v155\n"
"v_and_b32 v155, 0xffff0000, v155\n"
"v_mov_b32 v188, s13\n"
"v_ashrrev_i32 v190, 31, v188\n"
"v_not_b32 v190, v190\n"
"v_or_b32 v190, 0x80000000, v190\n"
"v_xor_b32 v188, v188, v190\n"
"v_and_b32 v188, 0xffff0000, v188\n"
"v_mul_f32 v188, 0xbfb8aa3b, v188\n"
"v_exp_f32 v188, v188\n"
"s_nop 0\n"
"v_add_f32 v188, 1.0, v188\n"
"v_rcp_f32 v188, v188\n"
"s_nop 0\n"
"v_cvt_pk_bf16_f32 v188, v188, v188\n"
"v_and_b32 v188, 0xffff0000, v188\n"
"v_mul_f32 v140, v140, v188\n"
"v_mul_f32 v141, v141, v188\n"
"v_mul_f32 v142, v142, v188\n"
"v_mul_f32 v143, v143, v188\n"
"v_mul_f32 v144, v144, v188\n"
"v_mul_f32 v145, v145, v188\n"
"v_mul_f32 v146, v146, v188\n"
"v_mul_f32 v147, v147, v188\n"
"v_mul_f32 v148, v148, v188\n"
"v_mul_f32 v149, v149, v188\n"
"v_mul_f32 v150, v150, v188\n"
"v_mul_f32 v151, v151, v188\n"
"v_mul_f32 v152, v152, v188\n"
"v_mul_f32 v153, v153, v188\n"
"v_mul_f32 v154, v154, v188\n"
"v_mul_f32 v155, v155, v188\n"
"v_cvt_pk_bf16_f32 v140, v140, v141\n"
"v_cvt_pk_bf16_f32 v142, v142, v143\n"
"v_cvt_pk_bf16_f32 v144, v144, v145\n"
"v_cvt_pk_bf16_f32 v146, v146, v147\n"
"v_cvt_pk_bf16_f32 v148, v148, v149\n"
"v_cvt_pk_bf16_f32 v150, v150, v151\n"
"v_cvt_pk_bf16_f32 v152, v152, v153\n"
"v_cvt_pk_bf16_f32 v154, v154, v155\n"
"v_cmp_eq_u32 vcc, s95, v189\n"
"s_nop 1\n"
"v_cndmask_b32 v196, v196, v140, vcc\n"
"v_cndmask_b32 v197, v197, v142, vcc\n"
"v_cndmask_b32 v198, v198, v144, vcc\n"
"v_cndmask_b32 v199, v199, v146, vcc\n"
"v_cndmask_b32 v200, v200, v148, vcc\n"
"v_cndmask_b32 v201, v201, v150, vcc\n"
"v_cndmask_b32 v202, v202, v152, vcc\n"
"v_cndmask_b32 v203, v203, v154, vcc\n"
"s_nop 3\n"
"s_branch .Lygb0\n"
".Lyg1:\n"
"s_nop 7\n"
"s_nop 7\n"
"s_andn2_b32 s95, 15, s14\n"
"s_cmp_eq_u32 s8, 0\n"
"s_cbranch_scc0 .Lyg1w\n"
"v_bfe_u32 v189, v0, 4, 4\n"
"v_lshrrev_b32 v188, 1, v0\n"
"v_and_b32 v188, 0x180, v188\n"
"v_lshl_add_u32 v188, v189, 12, v188\n"
"s_lshl_b32 s96, s91, 9\n"
"s_add_u32 s96, s96, 1572928\n"
"buffer_load_dwordx4 v[140:143], v188, s[64:67], s96 offen offset:0 sc1\n"
"buffer_load_dwordx4 v[144:147], v188, s[64:67], s96 offen offset:16 sc1\n"
"buffer_load_dwordx4 v[148:151], v188, s[64:67], s96 offen offset:32 sc1\n"
"buffer_load_dwordx4 v[152:155], v188, s[64:67], s96 offen offset:48 sc1\n"
"s_add_u32 s96, s96, 65536\n"
"buffer_load_dwordx4 v[156:159], v188, s[64:67], s96 offen offset:0 sc1\n"
"buffer_load_dwordx4 v[160:163], v188, s[64:67], s96 offen offset:16 sc1\n"
"buffer_load_dwordx4 v[164:167], v188, s[64:67], s96 offen offset:32 sc1\n"
"buffer_load_dwordx4 v[168:171], v188, s[64:67], s96 offen offset:48 sc1\n"
"s_add_u32 s96, s96, 65536\n"
"v_mov_b32 v190, 2130706432\n"
"v_cmp_gt_u32 vcc, 11, v189\n"
"s_nop 1\n"
"v_cndmask_b32 v188, v190, v188, vcc\n"
"buffer_load_dwordx4 v[172:175], v188, s[64:67], s96 offen offset:0 sc1\n"
"buffer_load_dwordx4 v[176:179], v188, s[64:67], s96 offen offset:16 sc1\n"
"buffer_load_dwordx4 v[180:183], v188, s[64:67], s96 offen offset:32 sc1\n"
"buffer_load_dwordx4 v[184:187], v188, s[64:67], s96 offen offset:48 sc1\n"
"s_add_u32 s96, s96, 65536\n"
"s_waitcnt vmcnt(0)\n"
".Lyg1w:\n"
"s_waitcnt vmcnt(8)\n"
"v_pk_add_f32 v[140:141], v[140:141], v[156:157]\n"
"v_pk_add_f32 v[142:143], v[142:143], v[158:159]\n"
"v_pk_add_f32 v[144:145], v[144:145], v[160:161]\n"
"v_pk_add_f32 v[146:147], v[146:147], v[162:163]\n"
"v_pk_add_f32 v[148:149], v[148:149], v[164:165]\n"
"v_pk_add_f32 v[150:151], v[150:151], v[166:167]\n"
"v_pk_add_f32 v[152:153], v[152:153], v[168:169]\n"
"v_pk_add_f32 v[154:155], v[154:155], v[170:171]\n"
"v_pk_add_f32 v[140:141], v[140:141], v[172:173]\n"
"v_pk_add_f32 v[142:143], v[142:143], v[174:175]\n"
"v_pk_add_f32 v[144:145], v[144:145], v[176:177]\n"
"v_pk_add_f32 v[146:147], v[146:147], v[178:179]\n"
"v_pk_add_f32 v[148:149], v[148:149], v[180:181]\n"
"v_pk_add_f32 v[150:151], v[150:151], v[182:183]\n"
"v_pk_add_f32 v[152:153], v[152:153], v[184:185]\n"
"v_pk_add_f32 v[154:155], v[154:155], v[186:187]\n"
"v_add_f32_dpp v140, v140, v140 quad_perm:[1,0,3,2] row_mask:0xf bank_mask:0xf\n"
"v_add_f32_dpp v141, v141, v141 quad_perm:[1,0,3,2] row_mask:0xf bank_mask:0xf\n"
"v_add_f32_dpp v142, v142, v142 quad_perm:[1,0,3,2] row_mask:0xf bank_mask:0xf\n"
"v_add_f32_dpp v143, v143, v143 quad_perm:[1,0,3,2] row_mask:0xf bank_mask:0xf\n"
"v_add_f32_dpp v144, v144, v144 quad_perm:[1,0,3,2] row_mask:0xf bank_mask:0xf\n"
"v_add_f32_dpp v145, v145, v145 quad_perm:[1,0,3,2] row_mask:0xf bank_mask:0xf\n"
"v_add_f32_dpp v146, v146, v146 quad_perm:[1,0,3,2] row_mask:0xf bank_mask:0xf\n"
"v_add_f32_dpp v147, v147, v147 quad_perm:[1,0,3,2] row_mask:0xf bank_mask:0xf\n"
"v_add_f32_dpp v148, v148, v148 quad_perm:[1,0,3,2] row_mask:0xf bank_mask:0xf\n"
"v_add_f32_dpp v149, v149, v149 quad_perm:[1,0,3,2] row_mask:0xf bank_mask:0xf\n"
"v_add_f32_dpp v150, v150, v150 quad_perm:[1,0,3,2] row_mask:0xf bank_mask:0xf\n"
"v_add_f32_dpp v151, v151, v151 quad_perm:[1,0,3,2] row_mask:0xf bank_mask:0xf\n"
"v_add_f32_dpp v152, v152, v152 quad_perm:[1,0,3,2] row_mask:0xf bank_mask:0xf\n"
"v_add_f32_dpp v153, v153, v153 quad_perm:[1,0,3,2] row_mask:0xf bank_mask:0xf\n"
"v_add_f32_dpp v154, v154, v154 quad_perm:[1,0,3,2] row_mask:0xf bank_mask:0xf\n"
"v_add_f32_dpp v155, v155, v155 quad_perm:[1,0,3,2] row_mask:0xf bank_mask:0xf\n"
"v_add_f32_dpp v140, v140, v140 quad_perm:[2,3,0,1] row_mask:0xf bank_mask:0xf\n"
"v_add_f32_dpp v141, v141, v141 quad_perm:[2,3,0,1] row_mask:0xf bank_mask:0xf\n"
"v_add_f32_dpp v142, v142, v142 quad_perm:[2,3,0,1] row_mask:0xf bank_mask:0xf\n"
"v_add_f32_dpp v143, v143, v143 quad_perm:[2,3,0,1] row_mask:0xf bank_mask:0xf\n"
"v_add_f32_dpp v144, v144, v144 quad_perm:[2,3,0,1] row_mask:0xf bank_mask:0xf\n"
"v_add_f32_dpp v145, v145, v145 quad_perm:[2,3,0,1] row_mask:0xf bank_mask:0xf\n"
"v_add_f32_dpp v146, v146, v146 quad_perm:[2,3,0,1] row_mask:0xf bank_mask:0xf\n"
"v_add_f32_dpp v147, v147, v147 quad_perm:[2,3,0,1] row_mask:0xf bank_mask:0xf\n"
"v_add_f32_dpp v148, v148, v148 quad_perm:[2,3,0,1] row_mask:0xf bank_mask:0xf\n"
"v_add_f32_dpp v149, v149, v149 quad_perm:[2,3,0,1] row_mask:0xf bank_mask:0xf\n"
"v_add_f32_dpp v150, v150, v150 quad_perm:[2,3,0,1] row_mask:0xf bank_mask:0xf\n"
"v_add_f32_dpp v151, v151, v151 quad_perm:[2,3,0,1] row_mask:0xf bank_mask:0xf\n"
"v_add_f32_dpp v152, v152, v152 quad_perm:[2,3,0,1] row_mask:0xf bank_mask:0xf\n"
"v_add_f32_dpp v153, v153, v153 quad_perm:[2,3,0,1] row_mask:0xf bank_mask:0xf\n"
"v_add_f32_dpp v154, v154, v154 quad_perm:[2,3,0,1] row_mask:0xf bank_mask:0xf\n"
"v_add_f32_dpp v155, v155, v155 quad_perm:[2,3,0,1] row_mask:0xf bank_mask:0xf\n"
"v_add_f32_dpp v140, v140, v140 row_half_mirror row_mask:0xf bank_mask:0xf\n"
"v_add_f32_dpp v141, v141, v141 row_half_mirror row_mask:0xf bank_mask:0xf\n"
"v_add_f32_dpp v142, v142, v142 row_half_mirror row_mask:0xf bank_mask:0xf\n"
"v_add_f32_dpp v143, v143, v143 row_half_mirror row_mask:0xf bank_mask:0xf\n"
"v_add_f32_dpp v144, v144, v144 row_half_mirror row_mask:0xf bank_mask:0xf\n"
"v_add_f32_dpp v145, v145, v145 row_half_mirror row_mask:0xf bank_mask:0xf\n"
"v_add_f32_dpp v146, v146, v146 row_half_mirror row_mask:0xf bank_mask:0xf\n"
"v_add_f32_dpp v147, v147, v147 row_half_mirror row_mask:0xf bank_mask:0xf\n"
"v_add_f32_dpp v148, v148, v148 row_half_mirror row_mask:0xf bank_mask:0xf\n"
"v_add_f32_dpp v149, v149, v149 row_half_mirror row_mask:0xf bank_mask:0xf\n"
"v_add_f32_dpp v150, v150, v150 row_half_mirror row_mask:0xf bank_mask:0xf\n"
"v_add_f32_dpp v151, v151, v151 row_half_mirror row_mask:0xf bank_mask:0xf\n"
"v_add_f32_dpp v152, v152, v152 row_half_mirror row_mask:0xf bank_mask:0xf\n"
"v_add_f32_dpp v153, v153, v153 row_half_mirror row_mask:0xf bank_mask:0xf\n"
"v_add_f32_dpp v154, v154, v154 row_half_mirror row_mask:0xf bank_mask:0xf\n"
"v_add_f32_dpp v155, v155, v155 row_half_mirror row_mask:0xf bank_mask:0xf\n"
"v_add_f32_dpp v140, v140, v140 row_mirror row_mask:0xf bank_mask:0xf\n"
"v_add_f32_dpp v141, v141, v141 row_mirror row_mask:0xf bank_mask:0xf\n"
"v_add_f32_dpp v142, v142, v142 row_mirror row_mask:0xf bank_mask:0xf\n"
"v_add_f32_dpp v143, v143, v143 row_mirror row_mask:0xf bank_mask:0xf\n"
"v_add_f32_dpp v144, v144, v144 row_mirror row_mask:0xf bank_mask:0xf\n"
"v_add_f32_dpp v145, v145, v145 row_mirror row_mask:0xf bank_mask:0xf\n"
"v_add_f32_dpp v146, v146, v146 row_mirror row_mask:0xf bank_mask:0xf\n"
"v_add_f32_dpp v147, v147, v147 row_mirror row_mask:0xf bank_mask:0xf\n"
"v_add_f32_dpp v148, v148, v148 row_mirror row_mask:0xf bank_mask:0xf\n"
"v_add_f32_dpp v149, v149, v149 row_mirror row_mask:0xf bank_mask:0xf\n"
"v_add_f32_dpp v150, v150, v150 row_mirror row_mask:0xf bank_mask:0xf\n"
"v_add_f32_dpp v151, v151, v151 row_mirror row_mask:0xf bank_mask:0xf\n"
"v_add_f32_dpp v152, v152, v152 row_mirror row_mask:0xf bank_mask:0xf\n"
"v_add_f32_dpp v153, v153, v153 row_mirror row_mask:0xf bank_mask:0xf\n"
"v_add_f32_dpp v154, v154, v154 row_mirror row_mask:0xf bank_mask:0xf\n"
"v_add_f32_dpp v155, v155, v155 row_mirror row_mask:0xf bank_mask:0xf\n"
"v_cvt_pk_bf16_f32 v141, v140, v141\n"
"v_cvt_pk_bf16_f32 v143, v142, v143\n"
"v_cvt_pk_bf16_f32 v145, v144, v145\n"
"v_cvt_pk_bf16_f32 v147, v146, v147\n"
"v_cvt_pk_bf16_f32 v149, v148, v149\n"
"v_cvt_pk_bf16_f32 v151, v150, v151\n"
"v_cvt_pk_bf16_f32 v153, v152, v153\n"
"v_cvt_pk_bf16_f32 v155, v154, v155\n"
"v_lshlrev_b32 v140, 16, v141\n"
"v_and_b32 v141, 0xffff0000, v141\n"
"v_lshlrev_b32 v142, 16, v143\n"
"v_and_b32 v143, 0xffff0000, v143\n"
"v_lshlrev_b32 v144, 16, v145\n"
"v_and_b32 v145, 0xffff0000, v145\n"
"v_lshlrev_b32 v146, 16, v147\n"
"v_and_b32 v147, 0xffff0000, v147\n"
"v_lshlrev_b32 v148, 16, v149\n"
"v_and_b32 v149, 0xffff0000, v149\n"
"v_lshlrev_b32 v150, 16, v151\n"
"v_and_b32 v151, 0xffff0000, v151\n"
"v_lshlrev_b32 v152, 16, v153\n"
"v_and_b32 v153, 0xffff0000, v153\n"
"v_lshlrev_b32 v154, 16, v155\n"
"v_and_b32 v155, 0xffff0000, v155\n"
"v_mov_b32 v188, s14\n"
"v_ashrrev_i32 v190, 31, v188\n"
"v_not_b32 v190, v190\n"
"v_or_b32 v190, 0x80000000, v190\n"
"v_xor_b32 v188, v188, v190\n"
"v_and_b32 v188, 0xffff0000, v188\n"
"v_mul_f32 v188, 0xbfb8aa3b, v188\n"
"v_exp_f32 v188, v188\n"
"s_nop 0\n"
"v_add_f32 v188, 1.0, v188\n"
"v_rcp_f32 v188, v188\n"
"s_nop 0\n"
"v_cvt_pk_bf16_f32 v188, v188, v188\n"
"v_and_b32 v188, 0xffff0000, v188\n"
"v_mul_f32 v140, v140, v188\n"
"v_mul_f32 v141, v141, v188\n"
"v_mul_f32 v142, v142, v188\n"
"v_mul_f32 v143, v143, v188\n"
"v_mul_f32 v144, v144, v188\n"
"v_mul_f32 v145, v145, v188\n"
"v_mul_f32 v146, v146, v188\n"
"v_mul_f32 v147, v147, v188\n"
"v_mul_f32 v148, v148, v188\n"
"v_mul_f32 v149, v149, v188\n"
"v_mul_f32 v150, v150, v188\n"
"v_mul_f32 v151, v151, v188\n"
"v_mul_f32 v152, v152, v188\n"
"v_mul_f32 v153, v153, v188\n"
"v_mul_f32 v154, v154, v188\n"
"v_mul_f32 v155, v155, v188\n"
"v_cvt_pk_bf16_f32 v140, v140, v141\n"
"v_cvt_pk_bf16_f32 v142, v142, v143\n"
"v_cvt_pk_bf16_f32 v144, v144, v145\n"
"v_cvt_pk_bf16_f32 v146, v146, v147\n"
"v_cvt_pk_bf16_f32 v148, v148, v149\n"
"v_cvt_pk_bf16_f32 v150, v150, v151\n"
"v_cvt_pk_bf16_f32 v152, v152, v153\n"
"v_cvt_pk_bf16_f32 v154, v154, v155\n"
"v_cmp_eq_u32 vcc, s95, v189\n"
"s_nop 1\n"
"v_cndmask_b32 v196, v196, v140, vcc\n"
"v_cndmask_b32 v197, v197, v142, vcc\n"
"v_cndmask_b32 v198, v198, v144, vcc\n"
"v_cndmask_b32 v199, v199, v146, vcc\n"
"v_cndmask_b32 v200, v200, v148, vcc\n"
"v_cndmask_b32 v201, v201, v150, vcc\n"
"v_cndmask_b32 v202, v202, v152, vcc\n"
"v_cndmask_b32 v203, v203, v154, vcc\n"
"s_nop 3\n"
"s_branch .Lygb1\n"
".Lrt5:\n"
"s_nop 7\n"
"s_nop 7\n"
"s_mov_b32 s26, 0\n"
".Lrl5:\n"
"s_add_u32 s25, s35, 4096\n"
"buffer_load_dword v2, v1, s[64:67], s25 offen sc1\n"
"s_waitcnt vmcnt(0) lgkmcnt(0)\n"
"v_cmp_ne_u32 vcc, s92, v2\n"
"s_nop 4\n"
"s_cbranch_vccz .Lbk5\n"
"s_sleep 2\n"
"s_add_u32 s26, s26, 1\n"
"s_cmp_lt_u32 s26, 0x4000\n"
"s_cbranch_scc1 .Lrl5\n"
"s_branch .Lbk5\n"
".Lrt6:\n"
"s_nop 7\n"
"s_nop 7\n"
"s_mov_b32 s26, 0\n"
".Lrl6:\n"
"s_add_u32 s25, s35, 8192\n"
"buffer_load_dword v2, v1, s[64:67], s25 offen sc1\n"
"s_waitcnt vmcnt(0) lgkmcnt(0)\n"
"v_cmp_ne_u32 vcc, s92, v2\n"
"s_nop 4\n"
"s_cbranch_vccz .Lbk6\n"
"s_sleep 2\n"
"s_add_u32 s26, s26, 1\n"
"s_cmp_lt_u32 s26, 0x4000\n"
"s_cbranch_scc1 .Lrl6\n"
"s_branch .Lbk6\n"
".Lrt7:\n"
"s_nop 7\n"
"s_nop 7\n"
"s_mov_b32 s26, 0\n"
".Lrl7:\n"
"s_add_u32 s25, s35, 12288\n"
"buffer_load_dword v2, v1, s[64:67], s25 offen sc1\n"
"s_waitcnt vmcnt(0) lgkmcnt(0)\n"
"v_cmp_ne_u32 vcc, s92, v2\n"
"s_nop 4\n"
"s_cbranch_vccz .Lbk7\n"
"s_sleep 2\n"
"s_add_u32 s26, s26, 1\n"
"s_cmp_lt_u32 s26, 0x4000\n"
"s_cbranch_scc1 .Lrl7\n"
"s_branch .Lbk7\n"
".Lrt8:\n"
"s_nop 7\n"
"s_nop 7\n"
"s_mov_b32 s26, 0\n"
".Lrl8:\n"
"buffer_load_dwordx2 v[148:149], v151, s[64:67], s94 offen sc1\n"
"s_waitcnt vmcnt(0) lgkmcnt(0)\n"
"v_cmp_ne_u32 s[28:29], s92, v148\n"
"s_nop 4\n"
"s_cmp_lg_u64 s[28:29], 0\n"
"s_cbranch_scc0 .Lbk8\n"
"s_sleep 2\n"
"s_add_u32 s26, s26, 1\n"
"s_cmp_lt_u32 s26, 0x4000\n"
"s_cbranch_scc1 .Lrl8\n"
"s_branch .Lbk8\n"
".Lhotend:\n"
".Lfallback:\n"
"s_nop 7\n"
"s_nop 7\n"
"s_waitcnt vmcnt(0) lgkmcnt(0)\n"
"v_and_b32 v1, 0xf0, v0\n"
"v_lshlrev_b32 v1, 4, v1\n"
"v_lshrrev_b32 v2, 4, v0\n"
"v_and_b32 v2, 0x30, v2\n"
"v_add_u32 v1, v1, v2\n"
"v_mov_b32 v4, 0xff800000\n"
"v_mov_b32 v5, 0xff800000\n"
"v_mov_b32 v6, 0xff800000\n"
"v_mov_b32 v7, 0xff800000\n"
"s_mov_b32 s94, 0\n"
".Lfbmax:\n"
"s_xor_b32 s95, s17, 0\n"
"s_lshl_b32 s95, s95, 6\n"
"s_add_u32 s95, s95, s94\n"
"buffer_load_dwordx4 a[164:167], v1, s[52:55], s95 offen\n"
"s_xor_b32 s95, s17, 1\n"
"s_lshl_b32 s95, s95, 6\n"
"s_add_u32 s95, s95, s94\n"
"buffer_load_dwordx4 a[168:171], v1, s[52:55], s95 offen\n"
"s_xor_b32 s95, s17, 2\n"
"s_lshl_b32 s95, s95, 6\n"
"s_add_u32 s95, s95, s94\n"
"buffer_load_dwordx4 a[172:175], v1, s[52:55], s95 offen\n"
"s_xor_b32 s95, s17, 3\n"
"s_lshl_b32 s95, s95, 6\n"
"s_add_u32 s95, s95, s94\n"
"buffer_load_dwordx4 a[176:179], v1, s[52:55], s95 offen\n"
"s_waitcnt vmcnt(3)\n"
"v_mfma_f32_16x16x32_bf16 v[20:23], a[164:167], a[52:55], 0\n"
"s_waitcnt vmcnt(2)\n"
"v_mfma_f32_16x16x32_bf16 v[20:23], a[168:171], a[56:59], v[20:23]\n"
"s_waitcnt vmcnt(1)\n"
"v_mfma_f32_16x16x32_bf16 v[20:23], a[172:175], a[60:63], v[20:23]\n"
"s_waitcnt vmcnt(0)\n"
"v_mfma_f32_16x16x32_bf16 v[20:23], a[176:179], a[64:67], v[20:23]\n"
"s_nop 7\n"
"v_max3_f32 v4, v4, v20, v21\n"
"v_max3_f32 v4, v4, v22, v23\n"
"v_mfma_f32_16x16x32_bf16 v[28:31], a[164:167], a[68:71], 0\n"
"v_mfma_f32_16x16x32_bf16 v[28:31], a[168:171], a[72:75], v[28:31]\n"
"v_mfma_f32_16x16x32_bf16 v[28:31], a[172:175], a[76:79], v[28:31]\n"
"v_mfma_f32_16x16x32_bf16 v[28:31], a[176:179], a[80:83], v[28:31]\n"
"s_nop 7\n"
"v_max3_f32 v5, v5, v28, v29\n"
"v_max3_f32 v5, v5, v30, v31\n"
"v_mfma_f32_16x16x32_bf16 v[36:39], a[164:167], a[84:87], 0\n"
"v_mfma_f32_16x16x32_bf16 v[36:39], a[168:171], a[88:91], v[36:39]\n"
"v_mfma_f32_16x16x32_bf16 v[36:39], a[172:175], a[92:95], v[36:39]\n"
"v_mfma_f32_16x16x32_bf16 v[36:39], a[176:179], a[96:99], v[36:39]\n"
"s_nop 7\n"
"v_max3_f32 v6, v6, v36, v37\n"
"v_max3_f32 v6, v6, v38, v39\n"
"v_mfma_f32_16x16x32_bf16 v[20:23], a[164:167], a[100:103], 0\n"
"v_mfma_f32_16x16x32_bf16 v[20:23], a[168:171], a[104:107], v[20:23]\n"
"v_mfma_f32_16x16x32_bf16 v[20:23], a[172:175], a[108:111], v[20:23]\n"
"v_mfma_f32_16x16x32_bf16 v[20:23], a[176:179], a[112:115], v[20:23]\n"
"s_nop 7\n"
"v_max3_f32 v7, v7, v20, v21\n"
"v_max3_f32 v7, v7, v22, v23\n"
"s_add_u32 s94, s94, 4096\n"
"s_cmp_lt_u32 s94, 131072\n"
"s_cbranch_scc1 .Lfbmax\n"
"v_mov_b32 v2, v4\n"
"s_nop 1\n"
"v_permlane16_swap_b32 v4, v2\n"
"v_max_f32 v4, v4, v2\n"
"v_mov_b32 v2, v5\n"
"s_nop 1\n"
"v_permlane16_swap_b32 v5, v2\n"
"v_max_f32 v5, v5, v2\n"
"v_mov_b32 v2, v6\n"
"s_nop 1\n"
"v_permlane16_swap_b32 v6, v2\n"
"v_max_f32 v6, v6, v2\n"
"v_mov_b32 v2, v7\n"
"s_nop 1\n"
"v_permlane16_swap_b32 v7, v2\n"
"v_max_f32 v7, v7, v2\n"
"v_mov_b32 v2, v4\n"
"s_nop 1\n"
"v_permlane32_swap_b32 v4, v2\n"
"v_max_f32 v4, v4, v2\n"
"v_mov_b32 v2, v5\n"
"s_nop 1\n"
"v_permlane32_swap_b32 v5, v2\n"
"v_max_f32 v5, v5, v2\n"
"v_mov_b32 v2, v6\n"
"s_nop 1\n"
"v_permlane32_swap_b32 v6, v2\n"
"v_max_f32 v6, v6, v2\n"
"v_mov_b32 v2, v7\n"
"s_nop 1\n"
"v_permlane32_swap_b32 v7, v2\n"
"v_max_f32 v7, v7, v2\n"
"ds_read_b64_tr_b16 a[196:197], v8 offset:0\n"
"ds_read_b64_tr_b16 a[198:199], v8 offset:2048\n"
"ds_read_b64_tr_b16 a[200:201], v9 offset:0\n"
"ds_read_b64_tr_b16 a[202:203], v9 offset:2048\n"
"s_xor_b32 s95, s17, 0\n"
"s_lshl_b32 s95, s95, 6\n"
"s_add_u32 s95, s95, 0\n"
"buffer_load_dwordx4 a[164:167], v1, s[52:55], s95 offen\n"
"s_xor_b32 s95, s17, 1\n"
"s_lshl_b32 s95, s95, 6\n"
"s_add_u32 s95, s95, 0\n"
"buffer_load_dwordx4 a[168:171], v1, s[52:55], s95 offen\n"
"s_xor_b32 s95, s17, 2\n"
"s_lshl_b32 s95, s95, 6\n"
"s_add_u32 s95, s95, 0\n"
"buffer_load_dwordx4 a[172:175], v1, s[52:55], s95 offen\n"
"s_xor_b32 s95, s17, 3\n"
"s_lshl_b32 s95, s95, 6\n"
"s_add_u32 s95, s95, 0\n"
"buffer_load_dwordx4 a[176:179], v1, s[52:55], s95 offen\n"
"s_waitcnt vmcnt(3)\n"
"v_mfma_f32_16x16x32_bf16 v[20:23], a[164:167], a[52:55], 0\n"
"s_waitcnt vmcnt(2)\n"
"v_mfma_f32_16x16x32_bf16 v[20:23], a[168:171], a[56:59], v[20:23]\n"
"s_waitcnt vmcnt(1)\n"
"v_mfma_f32_16x16x32_bf16 v[20:23], a[172:175], a[60:63], v[20:23]\n"
"s_waitcnt vmcnt(0)\n"
"v_mfma_f32_16x16x32_bf16 v[20:23], a[176:179], a[64:67], v[20:23]\n"
"s_nop 7\n"
"v_sub_f32 v20, v20, v4\n"
"v_sub_f32 v21, v21, v4\n"
"v_sub_f32 v22, v22, v4\n"
"v_sub_f32 v23, v23, v4\n"
"v_exp_f32 v20, v20\n"
"v_exp_f32 v21, v21\n"
"v_exp_f32 v22, v22\n"
"v_exp_f32 v23, v23\n"
"v_cvt_pk_bf16_f32 v2, v20, v21\n"
"v_accvgpr_write_b32 a116, v2\n"
"v_cvt_pk_bf16_f32 v3, v22, v23\n"
"v_accvgpr_write_b32 a117, v3\n"
"v_mfma_f32_16x16x32_bf16 v[28:31], a[164:167], a[68:71], 0\n"
"v_mfma_f32_16x16x32_bf16 v[28:31], a[168:171], a[72:75], v[28:31]\n"
"v_mfma_f32_16x16x32_bf16 v[28:31], a[172:175], a[76:79], v[28:31]\n"
"v_mfma_f32_16x16x32_bf16 v[28:31], a[176:179], a[80:83], v[28:31]\n"
"s_nop 7\n"
"v_sub_f32 v28, v28, v5\n"
"v_sub_f32 v29, v29, v5\n"
"v_sub_f32 v30, v30, v5\n"
"v_sub_f32 v31, v31, v5\n"
"v_exp_f32 v28, v28\n"
"v_exp_f32 v29, v29\n"
"v_exp_f32 v30, v30\n"
"v_exp_f32 v31, v31\n"
"v_cvt_pk_bf16_f32 v2, v28, v29\n"
"v_accvgpr_write_b32 a120, v2\n"
"v_cvt_pk_bf16_f32 v3, v30, v31\n"
"v_accvgpr_write_b32 a121, v3\n"
"v_mfma_f32_16x16x32_bf16 v[36:39], a[164:167], a[84:87], 0\n"
"v_mfma_f32_16x16x32_bf16 v[36:39], a[168:171], a[88:91], v[36:39]\n"
"v_mfma_f32_16x16x32_bf16 v[36:39], a[172:175], a[92:95], v[36:39]\n"
"v_mfma_f32_16x16x32_bf16 v[36:39], a[176:179], a[96:99], v[36:39]\n"
"s_nop 7\n"
"v_sub_f32 v36, v36, v6\n"
"v_sub_f32 v37, v37, v6\n"
"v_sub_f32 v38, v38, v6\n"
"v_sub_f32 v39, v39, v6\n"
"v_exp_f32 v36, v36\n"
"v_exp_f32 v37, v37\n"
"v_exp_f32 v38, v38\n"
"v_exp_f32 v39, v39\n"
"v_cvt_pk_bf16_f32 v2, v36, v37\n"
"v_accvgpr_write_b32 a124, v2\n"
"v_cvt_pk_bf16_f32 v3, v38, v39\n"
"v_accvgpr_write_b32 a125, v3\n"
"v_mfma_f32_16x16x32_bf16 v[20:23], a[164:167], a[100:103], 0\n"
"v_mfma_f32_16x16x32_bf16 v[20:23], a[168:171], a[104:107], v[20:23]\n"
"v_mfma_f32_16x16x32_bf16 v[20:23], a[172:175], a[108:111], v[20:23]\n"
"v_mfma_f32_16x16x32_bf16 v[20:23], a[176:179], a[112:115], v[20:23]\n"
"s_nop 7\n"
"v_sub_f32 v20, v20, v7\n"
"v_sub_f32 v21, v21, v7\n"
"v_sub_f32 v22, v22, v7\n"
"v_sub_f32 v23, v23, v7\n"
"v_exp_f32 v20, v20\n"
"v_exp_f32 v21, v21\n"
"v_exp_f32 v22, v22\n"
"v_exp_f32 v23, v23\n"
"v_cvt_pk_bf16_f32 v2, v20, v21\n"
"v_accvgpr_write_b32 a128, v2\n"
"v_cvt_pk_bf16_f32 v3, v22, v23\n"
"v_accvgpr_write_b32 a129, v3\n"
"s_xor_b32 s95, s17, 0\n"
"s_lshl_b32 s95, s95, 6\n"
"s_add_u32 s95, s95, 4096\n"
"buffer_load_dwordx4 a[164:167], v1, s[52:55], s95 offen\n"
"s_xor_b32 s95, s17, 1\n"
"s_lshl_b32 s95, s95, 6\n"
"s_add_u32 s95, s95, 4096\n"
"buffer_load_dwordx4 a[168:171], v1, s[52:55], s95 offen\n"
"s_xor_b32 s95, s17, 2\n"
"s_lshl_b32 s95, s95, 6\n"
"s_add_u32 s95, s95, 4096\n"
"buffer_load_dwordx4 a[172:175], v1, s[52:55], s95 offen\n"
"s_xor_b32 s95, s17, 3\n"
"s_lshl_b32 s95, s95, 6\n"
"s_add_u32 s95, s95, 4096\n"
"buffer_load_dwordx4 a[176:179], v1, s[52:55], s95 offen\n"
"s_waitcnt vmcnt(3)\n"
"v_mfma_f32_16x16x32_bf16 v[20:23], a[164:167], a[52:55], 0\n"
"s_waitcnt vmcnt(2)\n"
"v_mfma_f32_16x16x32_bf16 v[20:23], a[168:171], a[56:59], v[20:23]\n"
"s_waitcnt vmcnt(1)\n"
"v_mfma_f32_16x16x32_bf16 v[20:23], a[172:175], a[60:63], v[20:23]\n"
"s_waitcnt vmcnt(0)\n"
"v_mfma_f32_16x16x32_bf16 v[20:23], a[176:179], a[64:67], v[20:23]\n"
"s_nop 7\n"
"v_sub_f32 v20, v20, v4\n"
"v_sub_f32 v21, v21, v4\n"
"v_sub_f32 v22, v22, v4\n"
"v_sub_f32 v23, v23, v4\n"
"v_exp_f32 v20, v20\n"
"v_exp_f32 v21, v21\n"
"v_exp_f32 v22, v22\n"
"v_exp_f32 v23, v23\n"
"v_cvt_pk_bf16_f32 v2, v20, v21\n"
"v_accvgpr_write_b32 a118, v2\n"
"v_cvt_pk_bf16_f32 v3, v22, v23\n"
"v_accvgpr_write_b32 a119, v3\n"
"v_mfma_f32_16x16x32_bf16 v[28:31], a[164:167], a[68:71], 0\n"
"v_mfma_f32_16x16x32_bf16 v[28:31], a[168:171], a[72:75], v[28:31]\n"
"v_mfma_f32_16x16x32_bf16 v[28:31], a[172:175], a[76:79], v[28:31]\n"
"v_mfma_f32_16x16x32_bf16 v[28:31], a[176:179], a[80:83], v[28:31]\n"
"s_nop 7\n"
"v_sub_f32 v28, v28, v5\n"
"v_sub_f32 v29, v29, v5\n"
"v_sub_f32 v30, v30, v5\n"
"v_sub_f32 v31, v31, v5\n"
"v_exp_f32 v28, v28\n"
"v_exp_f32 v29, v29\n"
"v_exp_f32 v30, v30\n"
"v_exp_f32 v31, v31\n"
"v_cvt_pk_bf16_f32 v2, v28, v29\n"
"v_accvgpr_write_b32 a122, v2\n"
"v_cvt_pk_bf16_f32 v3, v30, v31\n"
"v_accvgpr_write_b32 a123, v3\n"
"v_mfma_f32_16x16x32_bf16 v[36:39], a[164:167], a[84:87], 0\n"
"v_mfma_f32_16x16x32_bf16 v[36:39], a[168:171], a[88:91], v[36:39]\n"
"v_mfma_f32_16x16x32_bf16 v[36:39], a[172:175], a[92:95], v[36:39]\n"
"v_mfma_f32_16x16x32_bf16 v[36:39], a[176:179], a[96:99], v[36:39]\n"
"s_nop 7\n"
"v_sub_f32 v36, v36, v6\n"
"v_sub_f32 v37, v37, v6\n"
"v_sub_f32 v38, v38, v6\n"
"v_sub_f32 v39, v39, v6\n"
"v_exp_f32 v36, v36\n"
"v_exp_f32 v37, v37\n"
"v_exp_f32 v38, v38\n"
"v_exp_f32 v39, v39\n"
"v_cvt_pk_bf16_f32 v2, v36, v37\n"
"v_accvgpr_write_b32 a126, v2\n"
"v_cvt_pk_bf16_f32 v3, v38, v39\n"
"v_accvgpr_write_b32 a127, v3\n"
"v_mfma_f32_16x16x32_bf16 v[20:23], a[164:167], a[100:103], 0\n"
"v_mfma_f32_16x16x32_bf16 v[20:23], a[168:171], a[104:107], v[20:23]\n"
"v_mfma_f32_16x16x32_bf16 v[20:23], a[172:175], a[108:111], v[20:23]\n"
"v_mfma_f32_16x16x32_bf16 v[20:23], a[176:179], a[112:115], v[20:23]\n"
"s_nop 7\n"
"v_sub_f32 v20, v20, v7\n"
"v_sub_f32 v21, v21, v7\n"
"v_sub_f32 v22, v22, v7\n"
"v_sub_f32 v23, v23, v7\n"
"v_exp_f32 v20, v20\n"
"v_exp_f32 v21, v21\n"
"v_exp_f32 v22, v22\n"
"v_exp_f32 v23, v23\n"
"v_cvt_pk_bf16_f32 v2, v20, v21\n"
"v_accvgpr_write_b32 a130, v2\n"
"v_cvt_pk_bf16_f32 v3, v22, v23\n"
"v_accvgpr_write_b32 a131, v3\n"
"s_waitcnt lgkmcnt(0)\n"
"v_mfma_f32_16x16x32_bf16 a[20:23], a[196:199], a[116:119], 0\n"
"v_mfma_f32_16x16x32_bf16 a[36:39], a[200:203], a[116:119], 0\n"
"v_mfma_f32_16x16x32_bf16 a[4:7], a[0:3], a[116:119], 0\n"
"v_mfma_f32_16x16x32_bf16 a[24:27], a[196:199], a[120:123], 0\n"
"v_mfma_f32_16x16x32_bf16 a[40:43], a[200:203], a[120:123], 0\n"
"v_mfma_f32_16x16x32_bf16 a[8:11], a[0:3], a[120:123], 0\n"
"v_mfma_f32_16x16x32_bf16 a[28:31], a[196:199], a[124:127], 0\n"
"v_mfma_f32_16x16x32_bf16 a[44:47], a[200:203], a[124:127], 0\n"
"v_mfma_f32_16x16x32_bf16 a[12:15], a[0:3], a[124:127], 0\n"
"v_mfma_f32_16x16x32_bf16 a[32:35], a[196:199], a[128:131], 0\n"
"v_mfma_f32_16x16x32_bf16 a[48:51], a[200:203], a[128:131], 0\n"
"v_mfma_f32_16x16x32_bf16 a[16:19], a[0:3], a[128:131], 0\n"
"ds_read_b64_tr_b16 a[196:197], v8 offset:4096\n"
"ds_read_b64_tr_b16 a[198:199], v8 offset:6144\n"
"ds_read_b64_tr_b16 a[200:201], v9 offset:4096\n"
"ds_read_b64_tr_b16 a[202:203], v9 offset:6144\n"
"s_xor_b32 s95, s17, 0\n"
"s_lshl_b32 s95, s95, 6\n"
"s_add_u32 s95, s95, 8192\n"
"buffer_load_dwordx4 a[164:167], v1, s[52:55], s95 offen\n"
"s_xor_b32 s95, s17, 1\n"
"s_lshl_b32 s95, s95, 6\n"
"s_add_u32 s95, s95, 8192\n"
"buffer_load_dwordx4 a[168:171], v1, s[52:55], s95 offen\n"
"s_xor_b32 s95, s17, 2\n"
"s_lshl_b32 s95, s95, 6\n"
"s_add_u32 s95, s95, 8192\n"
"buffer_load_dwordx4 a[172:175], v1, s[52:55], s95 offen\n"
"s_xor_b32 s95, s17, 3\n"
"s_lshl_b32 s95, s95, 6\n"
"s_add_u32 s95, s95, 8192\n"
"buffer_load_dwordx4 a[176:179], v1, s[52:55], s95 offen\n"
"s_waitcnt vmcnt(3)\n"
"v_mfma_f32_16x16x32_bf16 v[20:23], a[164:167], a[52:55], 0\n"
"s_waitcnt vmcnt(2)\n"
"v_mfma_f32_16x16x32_bf16 v[20:23], a[168:171], a[56:59], v[20:23]\n"
"s_waitcnt vmcnt(1)\n"
"v_mfma_f32_16x16x32_bf16 v[20:23], a[172:175], a[60:63], v[20:23]\n"
"s_waitcnt vmcnt(0)\n"
"v_mfma_f32_16x16x32_bf16 v[20:23], a[176:179], a[64:67], v[20:23]\n"
"s_nop 7\n"
"v_sub_f32 v20, v20, v4\n"
"v_sub_f32 v21, v21, v4\n"
"v_sub_f32 v22, v22, v4\n"
"v_sub_f32 v23, v23, v4\n"
"v_exp_f32 v20, v20\n"
"v_exp_f32 v21, v21\n"
"v_exp_f32 v22, v22\n"
"v_exp_f32 v23, v23\n"
"v_cvt_pk_bf16_f32 v2, v20, v21\n"
"v_accvgpr_write_b32 a132, v2\n"
"v_cvt_pk_bf16_f32 v3, v22, v23\n"
"v_accvgpr_write_b32 a133, v3\n"
"v_mfma_f32_16x16x32_bf16 v[28:31], a[164:167], a[68:71], 0\n"
"v_mfma_f32_16x16x32_bf16 v[28:31], a[168:171], a[72:75], v[28:31]\n"
"v_mfma_f32_16x16x32_bf16 v[28:31], a[172:175], a[76:79], v[28:31]\n"
"v_mfma_f32_16x16x32_bf16 v[28:31], a[176:179], a[80:83], v[28:31]\n"
"s_nop 7\n"
"v_sub_f32 v28, v28, v5\n"
"v_sub_f32 v29, v29, v5\n"
"v_sub_f32 v30, v30, v5\n"
"v_sub_f32 v31, v31, v5\n"
"v_exp_f32 v28, v28\n"
"v_exp_f32 v29, v29\n"
"v_exp_f32 v30, v30\n"
"v_exp_f32 v31, v31\n"
"v_cvt_pk_bf16_f32 v2, v28, v29\n"
"v_accvgpr_write_b32 a136, v2\n"
"v_cvt_pk_bf16_f32 v3, v30, v31\n"
"v_accvgpr_write_b32 a137, v3\n"
"v_mfma_f32_16x16x32_bf16 v[36:39], a[164:167], a[84:87], 0\n"
"v_mfma_f32_16x16x32_bf16 v[36:39], a[168:171], a[88:91], v[36:39]\n"
"v_mfma_f32_16x16x32_bf16 v[36:39], a[172:175], a[92:95], v[36:39]\n"
"v_mfma_f32_16x16x32_bf16 v[36:39], a[176:179], a[96:99], v[36:39]\n"
"s_nop 7\n"
"v_sub_f32 v36, v36, v6\n"
"v_sub_f32 v37, v37, v6\n"
"v_sub_f32 v38, v38, v6\n"
"v_sub_f32 v39, v39, v6\n"
"v_exp_f32 v36, v36\n"
"v_exp_f32 v37, v37\n"
"v_exp_f32 v38, v38\n"
"v_exp_f32 v39, v39\n"
"v_cvt_pk_bf16_f32 v2, v36, v37\n"
"v_accvgpr_write_b32 a140, v2\n"
"v_cvt_pk_bf16_f32 v3, v38, v39\n"
"v_accvgpr_write_b32 a141, v3\n"
"v_mfma_f32_16x16x32_bf16 v[20:23], a[164:167], a[100:103], 0\n"
"v_mfma_f32_16x16x32_bf16 v[20:23], a[168:171], a[104:107], v[20:23]\n"
"v_mfma_f32_16x16x32_bf16 v[20:23], a[172:175], a[108:111], v[20:23]\n"
"v_mfma_f32_16x16x32_bf16 v[20:23], a[176:179], a[112:115], v[20:23]\n"
"s_nop 7\n"
"v_sub_f32 v20, v20, v7\n"
"v_sub_f32 v21, v21, v7\n"
"v_sub_f32 v22, v22, v7\n"
"v_sub_f32 v23, v23, v7\n"
"v_exp_f32 v20, v20\n"
"v_exp_f32 v21, v21\n"
"v_exp_f32 v22, v22\n"
"v_exp_f32 v23, v23\n"
"v_cvt_pk_bf16_f32 v40, v20, v21\n"
"v_cvt_pk_bf16_f32 v41, v22, v23\n"
"s_xor_b32 s95, s17, 0\n"
"s_lshl_b32 s95, s95, 6\n"
"s_add_u32 s95, s95, 12288\n"
"buffer_load_dwordx4 a[164:167], v1, s[52:55], s95 offen\n"
"s_xor_b32 s95, s17, 1\n"
"s_lshl_b32 s95, s95, 6\n"
"s_add_u32 s95, s95, 12288\n"
"buffer_load_dwordx4 a[168:171], v1, s[52:55], s95 offen\n"
"s_xor_b32 s95, s17, 2\n"
"s_lshl_b32 s95, s95, 6\n"
"s_add_u32 s95, s95, 12288\n"
"buffer_load_dwordx4 a[172:175], v1, s[52:55], s95 offen\n"
"s_xor_b32 s95, s17, 3\n"
"s_lshl_b32 s95, s95, 6\n"
"s_add_u32 s95, s95, 12288\n"
"buffer_load_dwordx4 a[176:179], v1, s[52:55], s95 offen\n"
"s_waitcnt vmcnt(3)\n"
"v_mfma_f32_16x16x32_bf16 v[20:23], a[164:167], a[52:55], 0\n"
"s_waitcnt vmcnt(2)\n"
"v_mfma_f32_16x16x32_bf16 v[20:23], a[168:171], a[56:59], v[20:23]\n"
"s_waitcnt vmcnt(1)\n"
"v_mfma_f32_16x16x32_bf16 v[20:23], a[172:175], a[60:63], v[20:23]\n"
"s_waitcnt vmcnt(0)\n"
"v_mfma_f32_16x16x32_bf16 v[20:23], a[176:179], a[64:67], v[20:23]\n"
"s_nop 7\n"
"v_sub_f32 v20, v20, v4\n"
"v_sub_f32 v21, v21, v4\n"
"v_sub_f32 v22, v22, v4\n"
"v_sub_f32 v23, v23, v4\n"
"v_exp_f32 v20, v20\n"
"v_exp_f32 v21, v21\n"
"v_exp_f32 v22, v22\n"
"v_exp_f32 v23, v23\n"
"v_cvt_pk_bf16_f32 v2, v20, v21\n"
"v_accvgpr_write_b32 a134, v2\n"
"v_cvt_pk_bf16_f32 v3, v22, v23\n"
"v_accvgpr_write_b32 a135, v3\n"
"v_mfma_f32_16x16x32_bf16 v[28:31], a[164:167], a[68:71], 0\n"
"v_mfma_f32_16x16x32_bf16 v[28:31], a[168:171], a[72:75], v[28:31]\n"
"v_mfma_f32_16x16x32_bf16 v[28:31], a[172:175], a[76:79], v[28:31]\n"
"v_mfma_f32_16x16x32_bf16 v[28:31], a[176:179], a[80:83], v[28:31]\n"
"s_nop 7\n"
"v_sub_f32 v28, v28, v5\n"
"v_sub_f32 v29, v29, v5\n"
"v_sub_f32 v30, v30, v5\n"
"v_sub_f32 v31, v31, v5\n"
"v_exp_f32 v28, v28\n"
"v_exp_f32 v29, v29\n"
"v_exp_f32 v30, v30\n"
"v_exp_f32 v31, v31\n"
"v_cvt_pk_bf16_f32 v2, v28, v29\n"
"v_accvgpr_write_b32 a138, v2\n"
"v_cvt_pk_bf16_f32 v3, v30, v31\n"
"v_accvgpr_write_b32 a139, v3\n"
"v_mfma_f32_16x16x32_bf16 v[36:39], a[164:167], a[84:87], 0\n"
"v_mfma_f32_16x16x32_bf16 v[36:39], a[168:171], a[88:91], v[36:39]\n"
"v_mfma_f32_16x16x32_bf16 v[36:39], a[172:175], a[92:95], v[36:39]\n"
"v_mfma_f32_16x16x32_bf16 v[36:39], a[176:179], a[96:99], v[36:39]\n"
"s_nop 7\n"
"v_sub_f32 v36, v36, v6\n"
"v_sub_f32 v37, v37, v6\n"
"v_sub_f32 v38, v38, v6\n"
"v_sub_f32 v39, v39, v6\n"
"v_exp_f32 v36, v36\n"
"v_exp_f32 v37, v37\n"
"v_exp_f32 v38, v38\n"
"v_exp_f32 v39, v39\n"
"v_cvt_pk_bf16_f32 v2, v36, v37\n"
"v_accvgpr_write_b32 a142, v2\n"
"v_cvt_pk_bf16_f32 v3, v38, v39\n"
"v_accvgpr_write_b32 a143, v3\n"
"v_mfma_f32_16x16x32_bf16 v[20:23], a[164:167], a[100:103], 0\n"
"v_mfma_f32_16x16x32_bf16 v[20:23], a[168:171], a[104:107], v[20:23]\n"
"v_mfma_f32_16x16x32_bf16 v[20:23], a[172:175], a[108:111], v[20:23]\n"
"v_mfma_f32_16x16x32_bf16 v[20:23], a[176:179], a[112:115], v[20:23]\n"
"s_nop 7\n"
"v_sub_f32 v20, v20, v7\n"
"v_sub_f32 v21, v21, v7\n"
"v_sub_f32 v22, v22, v7\n"
"v_sub_f32 v23, v23, v7\n"
"v_exp_f32 v20, v20\n"
"v_exp_f32 v21, v21\n"
"v_exp_f32 v22, v22\n"
"v_exp_f32 v23, v23\n"
"v_cvt_pk_bf16_f32 v42, v20, v21\n"
"v_cvt_pk_bf16_f32 v43, v22, v23\n"
"s_waitcnt lgkmcnt(0)\n"
"v_mfma_f32_16x16x32_bf16 a[20:23], a[196:199], a[132:135], a[20:23]\n"
"v_mfma_f32_16x16x32_bf16 a[36:39], a[200:203], a[132:135], a[36:39]\n"
"v_mfma_f32_16x16x32_bf16 a[4:7], a[0:3], a[132:135], a[4:7]\n"
"v_mfma_f32_16x16x32_bf16 a[24:27], a[196:199], a[136:139], a[24:27]\n"
"v_mfma_f32_16x16x32_bf16 a[40:43], a[200:203], a[136:139], a[40:43]\n"
"v_mfma_f32_16x16x32_bf16 a[8:11], a[0:3], a[136:139], a[8:11]\n"
"v_mfma_f32_16x16x32_bf16 a[28:31], a[196:199], a[140:143], a[28:31]\n"
"v_mfma_f32_16x16x32_bf16 a[44:47], a[200:203], a[140:143], a[44:47]\n"
"v_mfma_f32_16x16x32_bf16 a[12:15], a[0:3], a[140:143], a[12:15]\n"
"v_mfma_f32_16x16x32_bf16 a[32:35], a[196:199], v[40:43], a[32:35]\n"
"v_mfma_f32_16x16x32_bf16 a[48:51], a[200:203], v[40:43], a[48:51]\n"
"v_mfma_f32_16x16x32_bf16 a[16:19], a[0:3], v[40:43], a[16:19]\n"
"ds_read_b64_tr_b16 a[196:197], v8 offset:8192\n"
"ds_read_b64_tr_b16 a[198:199], v8 offset:10240\n"
"ds_read_b64_tr_b16 a[200:201], v9 offset:8192\n"
"ds_read_b64_tr_b16 a[202:203], v9 offset:10240\n"
"s_xor_b32 s95, s17, 0\n"
"s_lshl_b32 s95, s95, 6\n"
"s_add_u32 s95, s95, 16384\n"
"buffer_load_dwordx4 a[164:167], v1, s[52:55], s95 offen\n"
"s_xor_b32 s95, s17, 1\n"
"s_lshl_b32 s95, s95, 6\n"
"s_add_u32 s95, s95, 16384\n"
"buffer_load_dwordx4 a[168:171], v1, s[52:55], s95 offen\n"
"s_xor_b32 s95, s17, 2\n"
"s_lshl_b32 s95, s95, 6\n"
"s_add_u32 s95, s95, 16384\n"
"buffer_load_dwordx4 a[172:175], v1, s[52:55], s95 offen\n"
"s_xor_b32 s95, s17, 3\n"
"s_lshl_b32 s95, s95, 6\n"
"s_add_u32 s95, s95, 16384\n"
"buffer_load_dwordx4 a[176:179], v1, s[52:55], s95 offen\n"
"s_waitcnt vmcnt(3)\n"
"v_mfma_f32_16x16x32_bf16 v[20:23], a[164:167], a[52:55], 0\n"
"s_waitcnt vmcnt(2)\n"
"v_mfma_f32_16x16x32_bf16 v[20:23], a[168:171], a[56:59], v[20:23]\n"
"s_waitcnt vmcnt(1)\n"
"v_mfma_f32_16x16x32_bf16 v[20:23], a[172:175], a[60:63], v[20:23]\n"
"s_waitcnt vmcnt(0)\n"
"v_mfma_f32_16x16x32_bf16 v[20:23], a[176:179], a[64:67], v[20:23]\n"
"s_nop 7\n"
"v_sub_f32 v20, v20, v4\n"
"v_sub_f32 v21, v21, v4\n"
"v_sub_f32 v22, v22, v4\n"
"v_sub_f32 v23, v23, v4\n"
"v_exp_f32 v20, v20\n"
"v_exp_f32 v21, v21\n"
"v_exp_f32 v22, v22\n"
"v_exp_f32 v23, v23\n"
"v_cvt_pk_bf16_f32 v44, v20, v21\n"
"v_cvt_pk_bf16_f32 v45, v22, v23\n"
"v_mfma_f32_16x16x32_bf16 v[28:31], a[164:167], a[68:71], 0\n"
"v_mfma_f32_16x16x32_bf16 v[28:31], a[168:171], a[72:75], v[28:31]\n"
"v_mfma_f32_16x16x32_bf16 v[28:31], a[172:175], a[76:79], v[28:31]\n"
"v_mfma_f32_16x16x32_bf16 v[28:31], a[176:179], a[80:83], v[28:31]\n"
"s_nop 7\n"
"v_sub_f32 v28, v28, v5\n"
"v_sub_f32 v29, v29, v5\n"
"v_sub_f32 v30, v30, v5\n"
"v_sub_f32 v31, v31, v5\n"
"v_exp_f32 v28, v28\n"
"v_exp_f32 v29, v29\n"
"v_exp_f32 v30, v30\n"
"v_exp_f32 v31, v31\n"
"v_cvt_pk_bf16_f32 v48, v28, v29\n"
"v_cvt_pk_bf16_f32 v49, v30, v31\n"
"v_mfma_f32_16x16x32_bf16 v[36:39], a[164:167], a[84:87], 0\n"
"v_mfma_f32_16x16x32_bf16 v[36:39], a[168:171], a[88:91], v[36:39]\n"
"v_mfma_f32_16x16x32_bf16 v[36:39], a[172:175], a[92:95], v[36:39]\n"
"v_mfma_f32_16x16x32_bf16 v[36:39], a[176:179], a[96:99], v[36:39]\n"
"s_nop 7\n"
"v_sub_f32 v36, v36, v6\n"
"v_sub_f32 v37, v37, v6\n"
"v_sub_f32 v38, v38, v6\n"
"v_sub_f32 v39, v39, v6\n"
"v_exp_f32 v36, v36\n"
"v_exp_f32 v37, v37\n"
"v_exp_f32 v38, v38\n"
"v_exp_f32 v39, v39\n"
"v_cvt_pk_bf16_f32 v52, v36, v37\n"
"v_cvt_pk_bf16_f32 v53, v38, v39\n"
"v_mfma_f32_16x16x32_bf16 v[20:23], a[164:167], a[100:103], 0\n"
"v_mfma_f32_16x16x32_bf16 v[20:23], a[168:171], a[104:107], v[20:23]\n"
"v_mfma_f32_16x16x32_bf16 v[20:23], a[172:175], a[108:111], v[20:23]\n"
"v_mfma_f32_16x16x32_bf16 v[20:23], a[176:179], a[112:115], v[20:23]\n"
"s_nop 7\n"
"v_sub_f32 v20, v20, v7\n"
"v_sub_f32 v21, v21, v7\n"
"v_sub_f32 v22, v22, v7\n"
"v_sub_f32 v23, v23, v7\n"
"v_exp_f32 v20, v20\n"
"v_exp_f32 v21, v21\n"
"v_exp_f32 v22, v22\n"
"v_exp_f32 v23, v23\n"
"v_cvt_pk_bf16_f32 v56, v20, v21\n"
"v_cvt_pk_bf16_f32 v57, v22, v23\n"
"s_xor_b32 s95, s17, 0\n"
"s_lshl_b32 s95, s95, 6\n"
"s_add_u32 s95, s95, 20480\n"
"buffer_load_dwordx4 a[164:167], v1, s[52:55], s95 offen\n"
"s_xor_b32 s95, s17, 1\n"
"s_lshl_b32 s95, s95, 6\n"
"s_add_u32 s95, s95, 20480\n"
"buffer_load_dwordx4 a[168:171], v1, s[52:55], s95 offen\n"
"s_xor_b32 s95, s17, 2\n"
"s_lshl_b32 s95, s95, 6\n"
"s_add_u32 s95, s95, 20480\n"
"buffer_load_dwordx4 a[172:175], v1, s[52:55], s95 offen\n"
"s_xor_b32 s95, s17, 3\n"
"s_lshl_b32 s95, s95, 6\n"
"s_add_u32 s95, s95, 20480\n"
"buffer_load_dwordx4 a[176:179], v1, s[52:55], s95 offen\n"
"s_waitcnt vmcnt(3)\n"
"v_mfma_f32_16x16x32_bf16 v[20:23], a[164:167], a[52:55], 0\n"
"s_waitcnt vmcnt(2)\n"
"v_mfma_f32_16x16x32_bf16 v[20:23], a[168:171], a[56:59], v[20:23]\n"
"s_waitcnt vmcnt(1)\n"
"v_mfma_f32_16x16x32_bf16 v[20:23], a[172:175], a[60:63], v[20:23]\n"
"s_waitcnt vmcnt(0)\n"
"v_mfma_f32_16x16x32_bf16 v[20:23], a[176:179], a[64:67], v[20:23]\n"
"s_nop 7\n"
"v_sub_f32 v20, v20, v4\n"
"v_sub_f32 v21, v21, v4\n"
"v_sub_f32 v22, v22, v4\n"
"v_sub_f32 v23, v23, v4\n"
"v_exp_f32 v20, v20\n"
"v_exp_f32 v21, v21\n"
"v_exp_f32 v22, v22\n"
"v_exp_f32 v23, v23\n"
"v_cvt_pk_bf16_f32 v46, v20, v21\n"
"v_cvt_pk_bf16_f32 v47, v22, v23\n"
"v_mfma_f32_16x16x32_bf16 v[28:31], a[164:167], a[68:71], 0\n"
"v_mfma_f32_16x16x32_bf16 v[28:31], a[168:171], a[72:75], v[28:31]\n"
"v_mfma_f32_16x16x32_bf16 v[28:31], a[172:175], a[76:79], v[28:31]\n"
"v_mfma_f32_16x16x32_bf16 v[28:31], a[176:179], a[80:83], v[28:31]\n"
"s_nop 7\n"
"v_sub_f32 v28, v28, v5\n"
"v_sub_f32 v29, v29, v5\n"
"v_sub_f32 v30, v30, v5\n"
"v_sub_f32 v31, v31, v5\n"
"v_exp_f32 v28, v28\n"
"v_exp_f32 v29, v29\n"
"v_exp_f32 v30, v30\n"
"v_exp_f32 v31, v31\n"
"v_cvt_pk_bf16_f32 v50, v28, v29\n"
"v_cvt_pk_bf16_f32 v51, v30, v31\n"
"v_mfma_f32_16x16x32_bf16 v[36:39], a[164:167], a[84:87], 0\n"
"v_mfma_f32_16x16x32_bf16 v[36:39], a[168:171], a[88:91], v[36:39]\n"
"v_mfma_f32_16x16x32_bf16 v[36:39], a[172:175], a[92:95], v[36:39]\n"
"v_mfma_f32_16x16x32_bf16 v[36:39], a[176:179], a[96:99], v[36:39]\n"
"s_nop 7\n"
"v_sub_f32 v36, v36, v6\n"
"v_sub_f32 v37, v37, v6\n"
"v_sub_f32 v38, v38, v6\n"
"v_sub_f32 v39, v39, v6\n"
"v_exp_f32 v36, v36\n"
"v_exp_f32 v37, v37\n"
"v_exp_f32 v38, v38\n"
"v_exp_f32 v39, v39\n"
"v_cvt_pk_bf16_f32 v54, v36, v37\n"
"v_cvt_pk_bf16_f32 v55, v38, v39\n"
"v_mfma_f32_16x16x32_bf16 v[20:23], a[164:167], a[100:103], 0\n"
"v_mfma_f32_16x16x32_bf16 v[20:23], a[168:171], a[104:107], v[20:23]\n"
"v_mfma_f32_16x16x32_bf16 v[20:23], a[172:175], a[108:111], v[20:23]\n"
"v_mfma_f32_16x16x32_bf16 v[20:23], a[176:179], a[112:115], v[20:23]\n"
"s_nop 7\n"
"v_sub_f32 v20, v20, v7\n"
"v_sub_f32 v21, v21, v7\n"
"v_sub_f32 v22, v22, v7\n"
"v_sub_f32 v23, v23, v7\n"
"v_exp_f32 v20, v20\n"
"v_exp_f32 v21, v21\n"
"v_exp_f32 v22, v22\n"
"v_exp_f32 v23, v23\n"
"v_cvt_pk_bf16_f32 v58, v20, v21\n"
"v_cvt_pk_bf16_f32 v59, v22, v23\n"
"s_waitcnt lgkmcnt(0)\n"
"v_mfma_f32_16x16x32_bf16 a[20:23], a[196:199], v[44:47], a[20:23]\n"
"v_mfma_f32_16x16x32_bf16 a[36:39], a[200:203], v[44:47], a[36:39]\n"
"v_mfma_f32_16x16x32_bf16 a[4:7], a[0:3], v[44:47], a[4:7]\n"
"v_mfma_f32_16x16x32_bf16 a[24:27], a[196:199], v[48:51], a[24:27]\n"
"v_mfma_f32_16x16x32_bf16 a[40:43], a[200:203], v[48:51], a[40:43]\n"
"v_mfma_f32_16x16x32_bf16 a[8:11], a[0:3], v[48:51], a[8:11]\n"
"v_mfma_f32_16x16x32_bf16 a[28:31], a[196:199], v[52:55], a[28:31]\n"
"v_mfma_f32_16x16x32_bf16 a[44:47], a[200:203], v[52:55], a[44:47]\n"
"v_mfma_f32_16x16x32_bf16 a[12:15], a[0:3], v[52:55], a[12:15]\n"
"v_mfma_f32_16x16x32_bf16 a[32:35], a[196:199], v[56:59], a[32:35]\n"
"v_mfma_f32_16x16x32_bf16 a[48:51], a[200:203], v[56:59], a[48:51]\n"
"v_mfma_f32_16x16x32_bf16 a[16:19], a[0:3], v[56:59], a[16:19]\n"
"ds_read_b64_tr_b16 a[196:197], v8 offset:12288\n"
"ds_read_b64_tr_b16 a[198:199], v8 offset:14336\n"
"ds_read_b64_tr_b16 a[200:201], v9 offset:12288\n"
"ds_read_b64_tr_b16 a[202:203], v9 offset:14336\n"
"s_xor_b32 s95, s17, 0\n"
"s_lshl_b32 s95, s95, 6\n"
"s_add_u32 s95, s95, 24576\n"
"buffer_load_dwordx4 a[164:167], v1, s[52:55], s95 offen\n"
"s_xor_b32 s95, s17, 1\n"
"s_lshl_b32 s95, s95, 6\n"
"s_add_u32 s95, s95, 24576\n"
"buffer_load_dwordx4 a[168:171], v1, s[52:55], s95 offen\n"
"s_xor_b32 s95, s17, 2\n"
"s_lshl_b32 s95, s95, 6\n"
"s_add_u32 s95, s95, 24576\n"
"buffer_load_dwordx4 a[172:175], v1, s[52:55], s95 offen\n"
"s_xor_b32 s95, s17, 3\n"
"s_lshl_b32 s95, s95, 6\n"
"s_add_u32 s95, s95, 24576\n"
"buffer_load_dwordx4 a[176:179], v1, s[52:55], s95 offen\n"
"s_waitcnt vmcnt(3)\n"
"v_mfma_f32_16x16x32_bf16 v[20:23], a[164:167], a[52:55], 0\n"
"s_waitcnt vmcnt(2)\n"
"v_mfma_f32_16x16x32_bf16 v[20:23], a[168:171], a[56:59], v[20:23]\n"
"s_waitcnt vmcnt(1)\n"
"v_mfma_f32_16x16x32_bf16 v[20:23], a[172:175], a[60:63], v[20:23]\n"
"s_waitcnt vmcnt(0)\n"
"v_mfma_f32_16x16x32_bf16 v[20:23], a[176:179], a[64:67], v[20:23]\n"
"s_nop 7\n"
"v_sub_f32 v20, v20, v4\n"
"v_sub_f32 v21, v21, v4\n"
"v_sub_f32 v22, v22, v4\n"
"v_sub_f32 v23, v23, v4\n"
"v_exp_f32 v20, v20\n"
"v_exp_f32 v21, v21\n"
"v_exp_f32 v22, v22\n"
"v_exp_f32 v23, v23\n"
"v_cvt_pk_bf16_f32 v60, v20, v21\n"
"v_cvt_pk_bf16_f32 v61, v22, v23\n"
"v_mfma_f32_16x16x32_bf16 v[28:31], a[164:167], a[68:71], 0\n"
"v_mfma_f32_16x16x32_bf16 v[28:31], a[168:171], a[72:75], v[28:31]\n"
"v_mfma_f32_16x16x32_bf16 v[28:31], a[172:175], a[76:79], v[28:31]\n"
"v_mfma_f32_16x16x32_bf16 v[28:31], a[176:179], a[80:83], v[28:31]\n"
"s_nop 7\n"
"v_sub_f32 v28, v28, v5\n"
"v_sub_f32 v29, v29, v5\n"
"v_sub_f32 v30, v30, v5\n"
"v_sub_f32 v31, v31, v5\n"
"v_exp_f32 v28, v28\n"
"v_exp_f32 v29, v29\n"
"v_exp_f32 v30, v30\n"
"v_exp_f32 v31, v31\n"
"v_cvt_pk_bf16_f32 v64, v28, v29\n"
"v_cvt_pk_bf16_f32 v65, v30, v31\n"
"v_mfma_f32_16x16x32_bf16 v[36:39], a[164:167], a[84:87], 0\n"
"v_mfma_f32_16x16x32_bf16 v[36:39], a[168:171], a[88:91], v[36:39]\n"
"v_mfma_f32_16x16x32_bf16 v[36:39], a[172:175], a[92:95], v[36:39]\n"
"v_mfma_f32_16x16x32_bf16 v[36:39], a[176:179], a[96:99], v[36:39]\n"
"s_nop 7\n"
"v_sub_f32 v36, v36, v6\n"
"v_sub_f32 v37, v37, v6\n"
"v_sub_f32 v38, v38, v6\n"
"v_sub_f32 v39, v39, v6\n"
"v_exp_f32 v36, v36\n"
"v_exp_f32 v37, v37\n"
"v_exp_f32 v38, v38\n"
"v_exp_f32 v39, v39\n"
"v_cvt_pk_bf16_f32 v68, v36, v37\n"
"v_cvt_pk_bf16_f32 v69, v38, v39\n"
"v_mfma_f32_16x16x32_bf16 v[20:23], a[164:167], a[100:103], 0\n"
"v_mfma_f32_16x16x32_bf16 v[20:23], a[168:171], a[104:107], v[20:23]\n"
"v_mfma_f32_16x16x32_bf16 v[20:23], a[172:175], a[108:111], v[20:23]\n"
"v_mfma_f32_16x16x32_bf16 v[20:23], a[176:179], a[112:115], v[20:23]\n"
"s_nop 7\n"
"v_sub_f32 v20, v20, v7\n"
"v_sub_f32 v21, v21, v7\n"
"v_sub_f32 v22, v22, v7\n"
"v_sub_f32 v23, v23, v7\n"
"v_exp_f32 v20, v20\n"
"v_exp_f32 v21, v21\n"
"v_exp_f32 v22, v22\n"
"v_exp_f32 v23, v23\n"
"v_cvt_pk_bf16_f32 v72, v20, v21\n"
"v_cvt_pk_bf16_f32 v73, v22, v23\n"
"s_xor_b32 s95, s17, 0\n"
"s_lshl_b32 s95, s95, 6\n"
"s_add_u32 s95, s95, 28672\n"
"buffer_load_dwordx4 a[164:167], v1, s[52:55], s95 offen\n"
"s_xor_b32 s95, s17, 1\n"
"s_lshl_b32 s95, s95, 6\n"
"s_add_u32 s95, s95, 28672\n"
"buffer_load_dwordx4 a[168:171], v1, s[52:55], s95 offen\n"
"s_xor_b32 s95, s17, 2\n"
"s_lshl_b32 s95, s95, 6\n"
"s_add_u32 s95, s95, 28672\n"
"buffer_load_dwordx4 a[172:175], v1, s[52:55], s95 offen\n"
"s_xor_b32 s95, s17, 3\n"
"s_lshl_b32 s95, s95, 6\n"
"s_add_u32 s95, s95, 28672\n"
"buffer_load_dwordx4 a[176:179], v1, s[52:55], s95 offen\n"
"s_waitcnt vmcnt(3)\n"
"v_mfma_f32_16x16x32_bf16 v[20:23], a[164:167], a[52:55], 0\n"
"s_waitcnt vmcnt(2)\n"
"v_mfma_f32_16x16x32_bf16 v[20:23], a[168:171], a[56:59], v[20:23]\n"
"s_waitcnt vmcnt(1)\n"
"v_mfma_f32_16x16x32_bf16 v[20:23], a[172:175], a[60:63], v[20:23]\n"
"s_waitcnt vmcnt(0)\n"
"v_mfma_f32_16x16x32_bf16 v[20:23], a[176:179], a[64:67], v[20:23]\n"
"s_nop 7\n"
"v_sub_f32 v20, v20, v4\n"
"v_sub_f32 v21, v21, v4\n"
"v_sub_f32 v22, v22, v4\n"
"v_sub_f32 v23, v23, v4\n"
"v_exp_f32 v20, v20\n"
"v_exp_f32 v21, v21\n"
"v_exp_f32 v22, v22\n"
"v_exp_f32 v23, v23\n"
"v_cvt_pk_bf16_f32 v62, v20, v21\n"
"v_cvt_pk_bf16_f32 v63, v22, v23\n"
"v_mfma_f32_16x16x32_bf16 v[28:31], a[164:167], a[68:71], 0\n"
"v_mfma_f32_16x16x32_bf16 v[28:31], a[168:171], a[72:75], v[28:31]\n"
"v_mfma_f32_16x16x32_bf16 v[28:31], a[172:175], a[76:79], v[28:31]\n"
"v_mfma_f32_16x16x32_bf16 v[28:31], a[176:179], a[80:83], v[28:31]\n"
"s_nop 7\n"
"v_sub_f32 v28, v28, v5\n"
"v_sub_f32 v29, v29, v5\n"
"v_sub_f32 v30, v30, v5\n"
"v_sub_f32 v31, v31, v5\n"
"v_exp_f32 v28, v28\n"
"v_exp_f32 v29, v29\n"
"v_exp_f32 v30, v30\n"
"v_exp_f32 v31, v31\n"
"v_cvt_pk_bf16_f32 v66, v28, v29\n"
"v_cvt_pk_bf16_f32 v67, v30, v31\n"
"v_mfma_f32_16x16x32_bf16 v[36:39], a[164:167], a[84:87], 0\n"
"v_mfma_f32_16x16x32_bf16 v[36:39], a[168:171], a[88:91], v[36:39]\n"
"v_mfma_f32_16x16x32_bf16 v[36:39], a[172:175], a[92:95], v[36:39]\n"
"v_mfma_f32_16x16x32_bf16 v[36:39], a[176:179], a[96:99], v[36:39]\n"
"s_nop 7\n"
"v_sub_f32 v36, v36, v6\n"
"v_sub_f32 v37, v37, v6\n"
"v_sub_f32 v38, v38, v6\n"
"v_sub_f32 v39, v39, v6\n"
"v_exp_f32 v36, v36\n"
"v_exp_f32 v37, v37\n"
"v_exp_f32 v38, v38\n"
"v_exp_f32 v39, v39\n"
"v_cvt_pk_bf16_f32 v70, v36, v37\n"
"v_cvt_pk_bf16_f32 v71, v38, v39\n"
"v_mfma_f32_16x16x32_bf16 v[20:23], a[164:167], a[100:103], 0\n"
"v_mfma_f32_16x16x32_bf16 v[20:23], a[168:171], a[104:107], v[20:23]\n"
"v_mfma_f32_16x16x32_bf16 v[20:23], a[172:175], a[108:111], v[20:23]\n"
"v_mfma_f32_16x16x32_bf16 v[20:23], a[176:179], a[112:115], v[20:23]\n"
"s_nop 7\n"
"v_sub_f32 v20, v20, v7\n"
"v_sub_f32 v21, v21, v7\n"
"v_sub_f32 v22, v22, v7\n"
"v_sub_f32 v23, v23, v7\n"
"v_exp_f32 v20, v20\n"
"v_exp_f32 v21, v21\n"
"v_exp_f32 v22, v22\n"
"v_exp_f32 v23, v23\n"
"v_cvt_pk_bf16_f32 v74, v20, v21\n"
"v_cvt_pk_bf16_f32 v75, v22, v23\n"
"s_waitcnt lgkmcnt(0)\n"
"v_mfma_f32_16x16x32_bf16 a[20:23], a[196:199], v[60:63], a[20:23]\n"
"v_mfma_f32_16x16x32_bf16 a[36:39], a[200:203], v[60:63], a[36:39]\n"
"v_mfma_f32_16x16x32_bf16 a[4:7], a[0:3], v[60:63], a[4:7]\n"
"v_mfma_f32_16x16x32_bf16 a[24:27], a[196:199], v[64:67], a[24:27]\n"
"v_mfma_f32_16x16x32_bf16 a[40:43], a[200:203], v[64:67], a[40:43]\n"
"v_mfma_f32_16x16x32_bf16 a[8:11], a[0:3], v[64:67], a[8:11]\n"
"v_mfma_f32_16x16x32_bf16 a[28:31], a[196:199], v[68:71], a[28:31]\n"
"v_mfma_f32_16x16x32_bf16 a[44:47], a[200:203], v[68:71], a[44:47]\n"
"v_mfma_f32_16x16x32_bf16 a[12:15], a[0:3], v[68:71], a[12:15]\n"
"v_mfma_f32_16x16x32_bf16 a[32:35], a[196:199], v[72:75], a[32:35]\n"
"v_mfma_f32_16x16x32_bf16 a[48:51], a[200:203], v[72:75], a[48:51]\n"
"v_mfma_f32_16x16x32_bf16 a[16:19], a[0:3], v[72:75], a[16:19]\n"
"ds_read_b64_tr_b16 a[196:197], v8 offset:16384\n"
"ds_read_b64_tr_b16 a[198:199], v8 offset:18432\n"
"ds_read_b64_tr_b16 a[200:201], v9 offset:16384\n"
"ds_read_b64_tr_b16 a[202:203], v9 offset:18432\n"
"s_xor_b32 s95, s17, 0\n"
"s_lshl_b32 s95, s95, 6\n"
"s_add_u32 s95, s95, 32768\n"
"buffer_load_dwordx4 a[164:167], v1, s[52:55], s95 offen\n"
"s_xor_b32 s95, s17, 1\n"
"s_lshl_b32 s95, s95, 6\n"
"s_add_u32 s95, s95, 32768\n"
"buffer_load_dwordx4 a[168:171], v1, s[52:55], s95 offen\n"
"s_xor_b32 s95, s17, 2\n"
"s_lshl_b32 s95, s95, 6\n"
"s_add_u32 s95, s95, 32768\n"
"buffer_load_dwordx4 a[172:175], v1, s[52:55], s95 offen\n"
"s_xor_b32 s95, s17, 3\n"
"s_lshl_b32 s95, s95, 6\n"
"s_add_u32 s95, s95, 32768\n"
"buffer_load_dwordx4 a[176:179], v1, s[52:55], s95 offen\n"
"s_waitcnt vmcnt(3)\n"
"v_mfma_f32_16x16x32_bf16 v[20:23], a[164:167], a[52:55], 0\n"
"s_waitcnt vmcnt(2)\n"
"v_mfma_f32_16x16x32_bf16 v[20:23], a[168:171], a[56:59], v[20:23]\n"
"s_waitcnt vmcnt(1)\n"
"v_mfma_f32_16x16x32_bf16 v[20:23], a[172:175], a[60:63], v[20:23]\n"
"s_waitcnt vmcnt(0)\n"
"v_mfma_f32_16x16x32_bf16 v[20:23], a[176:179], a[64:67], v[20:23]\n"
"s_nop 7\n"
"v_sub_f32 v20, v20, v4\n"
"v_sub_f32 v21, v21, v4\n"
"v_sub_f32 v22, v22, v4\n"
"v_sub_f32 v23, v23, v4\n"
"v_exp_f32 v20, v20\n"
"v_exp_f32 v21, v21\n"
"v_exp_f32 v22, v22\n"
"v_exp_f32 v23, v23\n"
"v_cvt_pk_bf16_f32 v76, v20, v21\n"
"v_cvt_pk_bf16_f32 v77, v22, v23\n"
"v_mfma_f32_16x16x32_bf16 v[28:31], a[164:167], a[68:71], 0\n"
"v_mfma_f32_16x16x32_bf16 v[28:31], a[168:171], a[72:75], v[28:31]\n"
"v_mfma_f32_16x16x32_bf16 v[28:31], a[172:175], a[76:79], v[28:31]\n"
"v_mfma_f32_16x16x32_bf16 v[28:31], a[176:179], a[80:83], v[28:31]\n"
"s_nop 7\n"
"v_sub_f32 v28, v28, v5\n"
"v_sub_f32 v29, v29, v5\n"
"v_sub_f32 v30, v30, v5\n"
"v_sub_f32 v31, v31, v5\n"
"v_exp_f32 v28, v28\n"
"v_exp_f32 v29, v29\n"
"v_exp_f32 v30, v30\n"
"v_exp_f32 v31, v31\n"
"v_cvt_pk_bf16_f32 v80, v28, v29\n"
"v_cvt_pk_bf16_f32 v81, v30, v31\n"
"v_mfma_f32_16x16x32_bf16 v[36:39], a[164:167], a[84:87], 0\n"
"v_mfma_f32_16x16x32_bf16 v[36:39], a[168:171], a[88:91], v[36:39]\n"
"v_mfma_f32_16x16x32_bf16 v[36:39], a[172:175], a[92:95], v[36:39]\n"
"v_mfma_f32_16x16x32_bf16 v[36:39], a[176:179], a[96:99], v[36:39]\n"
"s_nop 7\n"
"v_sub_f32 v36, v36, v6\n"
"v_sub_f32 v37, v37, v6\n"
"v_sub_f32 v38, v38, v6\n"
"v_sub_f32 v39, v39, v6\n"
"v_exp_f32 v36, v36\n"
"v_exp_f32 v37, v37\n"
"v_exp_f32 v38, v38\n"
"v_exp_f32 v39, v39\n"
"v_cvt_pk_bf16_f32 v84, v36, v37\n"
"v_cvt_pk_bf16_f32 v85, v38, v39\n"
"v_mfma_f32_16x16x32_bf16 v[20:23], a[164:167], a[100:103], 0\n"
"v_mfma_f32_16x16x32_bf16 v[20:23], a[168:171], a[104:107], v[20:23]\n"
"v_mfma_f32_16x16x32_bf16 v[20:23], a[172:175], a[108:111], v[20:23]\n"
"v_mfma_f32_16x16x32_bf16 v[20:23], a[176:179], a[112:115], v[20:23]\n"
"s_nop 7\n"
"v_sub_f32 v20, v20, v7\n"
"v_sub_f32 v21, v21, v7\n"
"v_sub_f32 v22, v22, v7\n"
"v_sub_f32 v23, v23, v7\n"
"v_exp_f32 v20, v20\n"
"v_exp_f32 v21, v21\n"
"v_exp_f32 v22, v22\n"
"v_exp_f32 v23, v23\n"
"v_cvt_pk_bf16_f32 v88, v20, v21\n"
"v_cvt_pk_bf16_f32 v89, v22, v23\n"
"s_xor_b32 s95, s17, 0\n"
"s_lshl_b32 s95, s95, 6\n"
"s_add_u32 s95, s95, 36864\n"
"buffer_load_dwordx4 a[164:167], v1, s[52:55], s95 offen\n"
"s_xor_b32 s95, s17, 1\n"
"s_lshl_b32 s95, s95, 6\n"
"s_add_u32 s95, s95, 36864\n"
"buffer_load_dwordx4 a[168:171], v1, s[52:55], s95 offen\n"
"s_xor_b32 s95, s17, 2\n"
"s_lshl_b32 s95, s95, 6\n"
"s_add_u32 s95, s95, 36864\n"
"buffer_load_dwordx4 a[172:175], v1, s[52:55], s95 offen\n"
"s_xor_b32 s95, s17, 3\n"
"s_lshl_b32 s95, s95, 6\n"
"s_add_u32 s95, s95, 36864\n"
"buffer_load_dwordx4 a[176:179], v1, s[52:55], s95 offen\n"
"s_waitcnt vmcnt(3)\n"
"v_mfma_f32_16x16x32_bf16 v[20:23], a[164:167], a[52:55], 0\n"
"s_waitcnt vmcnt(2)\n"
"v_mfma_f32_16x16x32_bf16 v[20:23], a[168:171], a[56:59], v[20:23]\n"
"s_waitcnt vmcnt(1)\n"
"v_mfma_f32_16x16x32_bf16 v[20:23], a[172:175], a[60:63], v[20:23]\n"
"s_waitcnt vmcnt(0)\n"
"v_mfma_f32_16x16x32_bf16 v[20:23], a[176:179], a[64:67], v[20:23]\n"
"s_nop 7\n"
"v_sub_f32 v20, v20, v4\n"
"v_sub_f32 v21, v21, v4\n"
"v_sub_f32 v22, v22, v4\n"
"v_sub_f32 v23, v23, v4\n"
"v_exp_f32 v20, v20\n"
"v_exp_f32 v21, v21\n"
"v_exp_f32 v22, v22\n"
"v_exp_f32 v23, v23\n"
"v_cvt_pk_bf16_f32 v78, v20, v21\n"
"v_cvt_pk_bf16_f32 v79, v22, v23\n"
"v_mfma_f32_16x16x32_bf16 v[28:31], a[164:167], a[68:71], 0\n"
"v_mfma_f32_16x16x32_bf16 v[28:31], a[168:171], a[72:75], v[28:31]\n"
"v_mfma_f32_16x16x32_bf16 v[28:31], a[172:175], a[76:79], v[28:31]\n"
"v_mfma_f32_16x16x32_bf16 v[28:31], a[176:179], a[80:83], v[28:31]\n"
"s_nop 7\n"
"v_sub_f32 v28, v28, v5\n"
"v_sub_f32 v29, v29, v5\n"
"v_sub_f32 v30, v30, v5\n"
"v_sub_f32 v31, v31, v5\n"
"v_exp_f32 v28, v28\n"
"v_exp_f32 v29, v29\n"
"v_exp_f32 v30, v30\n"
"v_exp_f32 v31, v31\n"
"v_cvt_pk_bf16_f32 v82, v28, v29\n"
"v_cvt_pk_bf16_f32 v83, v30, v31\n"
"v_mfma_f32_16x16x32_bf16 v[36:39], a[164:167], a[84:87], 0\n"
"v_mfma_f32_16x16x32_bf16 v[36:39], a[168:171], a[88:91], v[36:39]\n"
"v_mfma_f32_16x16x32_bf16 v[36:39], a[172:175], a[92:95], v[36:39]\n"
"v_mfma_f32_16x16x32_bf16 v[36:39], a[176:179], a[96:99], v[36:39]\n"
"s_nop 7\n"
"v_sub_f32 v36, v36, v6\n"
"v_sub_f32 v37, v37, v6\n"
"v_sub_f32 v38, v38, v6\n"
"v_sub_f32 v39, v39, v6\n"
"v_exp_f32 v36, v36\n"
"v_exp_f32 v37, v37\n"
"v_exp_f32 v38, v38\n"
"v_exp_f32 v39, v39\n"
"v_cvt_pk_bf16_f32 v86, v36, v37\n"
"v_cvt_pk_bf16_f32 v87, v38, v39\n"
"v_mfma_f32_16x16x32_bf16 v[20:23], a[164:167], a[100:103], 0\n"
"v_mfma_f32_16x16x32_bf16 v[20:23], a[168:171], a[104:107], v[20:23]\n"
"v_mfma_f32_16x16x32_bf16 v[20:23], a[172:175], a[108:111], v[20:23]\n"
"v_mfma_f32_16x16x32_bf16 v[20:23], a[176:179], a[112:115], v[20:23]\n"
"s_nop 7\n"
"v_sub_f32 v20, v20, v7\n"
"v_sub_f32 v21, v21, v7\n"
"v_sub_f32 v22, v22, v7\n"
"v_sub_f32 v23, v23, v7\n"
"v_exp_f32 v20, v20\n"
"v_exp_f32 v21, v21\n"
"v_exp_f32 v22, v22\n"
"v_exp_f32 v23, v23\n"
"v_cvt_pk_bf16_f32 v90, v20, v21\n"
"v_cvt_pk_bf16_f32 v91, v22, v23\n"
"s_waitcnt lgkmcnt(0)\n"
"v_mfma_f32_16x16x32_bf16 a[20:23], a[196:199], v[76:79], a[20:23]\n"
"v_mfma_f32_16x16x32_bf16 a[36:39], a[200:203], v[76:79], a[36:39]\n"
"v_mfma_f32_16x16x32_bf16 a[4:7], a[0:3], v[76:79], a[4:7]\n"
"v_mfma_f32_16x16x32_bf16 a[24:27], a[196:199], v[80:83], a[24:27]\n"
"v_mfma_f32_16x16x32_bf16 a[40:43], a[200:203], v[80:83], a[40:43]\n"
"v_mfma_f32_16x16x32_bf16 a[8:11], a[0:3], v[80:83], a[8:11]\n"
"v_mfma_f32_16x16x32_bf16 a[28:31], a[196:199], v[84:87], a[28:31]\n"
"v_mfma_f32_16x16x32_bf16 a[44:47], a[200:203], v[84:87], a[44:47]\n"
"v_mfma_f32_16x16x32_bf16 a[12:15], a[0:3], v[84:87], a[12:15]\n"
"v_mfma_f32_16x16x32_bf16 a[32:35], a[196:199], v[88:91], a[32:35]\n"
"v_mfma_f32_16x16x32_bf16 a[48:51], a[200:203], v[88:91], a[48:51]\n"
"v_mfma_f32_16x16x32_bf16 a[16:19], a[0:3], v[88:91], a[16:19]\n"
"ds_read_b64_tr_b16 a[196:197], v8 offset:20480\n"
"ds_read_b64_tr_b16 a[198:199], v8 offset:22528\n"
"ds_read_b64_tr_b16 a[200:201], v9 offset:20480\n"
"ds_read_b64_tr_b16 a[202:203], v9 offset:22528\n"
"s_xor_b32 s95, s17, 0\n"
"s_lshl_b32 s95, s95, 6\n"
"s_add_u32 s95, s95, 40960\n"
"buffer_load_dwordx4 a[164:167], v1, s[52:55], s95 offen\n"
"s_xor_b32 s95, s17, 1\n"
"s_lshl_b32 s95, s95, 6\n"
"s_add_u32 s95, s95, 40960\n"
"buffer_load_dwordx4 a[168:171], v1, s[52:55], s95 offen\n"
"s_xor_b32 s95, s17, 2\n"
"s_lshl_b32 s95, s95, 6\n"
"s_add_u32 s95, s95, 40960\n"
"buffer_load_dwordx4 a[172:175], v1, s[52:55], s95 offen\n"
"s_xor_b32 s95, s17, 3\n"
"s_lshl_b32 s95, s95, 6\n"
"s_add_u32 s95, s95, 40960\n"
"buffer_load_dwordx4 a[176:179], v1, s[52:55], s95 offen\n"
"s_waitcnt vmcnt(3)\n"
"v_mfma_f32_16x16x32_bf16 v[20:23], a[164:167], a[52:55], 0\n"
"s_waitcnt vmcnt(2)\n"
"v_mfma_f32_16x16x32_bf16 v[20:23], a[168:171], a[56:59], v[20:23]\n"
"s_waitcnt vmcnt(1)\n"
"v_mfma_f32_16x16x32_bf16 v[20:23], a[172:175], a[60:63], v[20:23]\n"
"s_waitcnt vmcnt(0)\n"
"v_mfma_f32_16x16x32_bf16 v[20:23], a[176:179], a[64:67], v[20:23]\n"
"s_nop 7\n"
"v_sub_f32 v20, v20, v4\n"
"v_sub_f32 v21, v21, v4\n"
"v_sub_f32 v22, v22, v4\n"
"v_sub_f32 v23, v23, v4\n"
"v_exp_f32 v20, v20\n"
"v_exp_f32 v21, v21\n"
"v_exp_f32 v22, v22\n"
"v_exp_f32 v23, v23\n"
"v_cvt_pk_bf16_f32 v92, v20, v21\n"
"v_cvt_pk_bf16_f32 v93, v22, v23\n"
"v_mfma_f32_16x16x32_bf16 v[28:31], a[164:167], a[68:71], 0\n"
"v_mfma_f32_16x16x32_bf16 v[28:31], a[168:171], a[72:75], v[28:31]\n"
"v_mfma_f32_16x16x32_bf16 v[28:31], a[172:175], a[76:79], v[28:31]\n"
"v_mfma_f32_16x16x32_bf16 v[28:31], a[176:179], a[80:83], v[28:31]\n"
"s_nop 7\n"
"v_sub_f32 v28, v28, v5\n"
"v_sub_f32 v29, v29, v5\n"
"v_sub_f32 v30, v30, v5\n"
"v_sub_f32 v31, v31, v5\n"
"v_exp_f32 v28, v28\n"
"v_exp_f32 v29, v29\n"
"v_exp_f32 v30, v30\n"
"v_exp_f32 v31, v31\n"
"v_cvt_pk_bf16_f32 v96, v28, v29\n"
"v_cvt_pk_bf16_f32 v97, v30, v31\n"
"v_mfma_f32_16x16x32_bf16 v[36:39], a[164:167], a[84:87], 0\n"
"v_mfma_f32_16x16x32_bf16 v[36:39], a[168:171], a[88:91], v[36:39]\n"
"v_mfma_f32_16x16x32_bf16 v[36:39], a[172:175], a[92:95], v[36:39]\n"
"v_mfma_f32_16x16x32_bf16 v[36:39], a[176:179], a[96:99], v[36:39]\n"
"s_nop 7\n"
"v_sub_f32 v36, v36, v6\n"
"v_sub_f32 v37, v37, v6\n"
"v_sub_f32 v38, v38, v6\n"
"v_sub_f32 v39, v39, v6\n"
"v_exp_f32 v36, v36\n"
"v_exp_f32 v37, v37\n"
"v_exp_f32 v38, v38\n"
"v_exp_f32 v39, v39\n"
"v_cvt_pk_bf16_f32 v100, v36, v37\n"
"v_cvt_pk_bf16_f32 v101, v38, v39\n"
"v_mfma_f32_16x16x32_bf16 v[20:23], a[164:167], a[100:103], 0\n"
"v_mfma_f32_16x16x32_bf16 v[20:23], a[168:171], a[104:107], v[20:23]\n"
"v_mfma_f32_16x16x32_bf16 v[20:23], a[172:175], a[108:111], v[20:23]\n"
"v_mfma_f32_16x16x32_bf16 v[20:23], a[176:179], a[112:115], v[20:23]\n"
"s_nop 7\n"
"v_sub_f32 v20, v20, v7\n"
"v_sub_f32 v21, v21, v7\n"
"v_sub_f32 v22, v22, v7\n"
"v_sub_f32 v23, v23, v7\n"
"v_exp_f32 v20, v20\n"
"v_exp_f32 v21, v21\n"
"v_exp_f32 v22, v22\n"
"v_exp_f32 v23, v23\n"
"v_cvt_pk_bf16_f32 v104, v20, v21\n"
"v_cvt_pk_bf16_f32 v105, v22, v23\n"
"s_xor_b32 s95, s17, 0\n"
"s_lshl_b32 s95, s95, 6\n"
"s_add_u32 s95, s95, 45056\n"
"buffer_load_dwordx4 a[164:167], v1, s[52:55], s95 offen\n"
"s_xor_b32 s95, s17, 1\n"
"s_lshl_b32 s95, s95, 6\n"
"s_add_u32 s95, s95, 45056\n"
"buffer_load_dwordx4 a[168:171], v1, s[52:55], s95 offen\n"
"s_xor_b32 s95, s17, 2\n"
"s_lshl_b32 s95, s95, 6\n"
"s_add_u32 s95, s95, 45056\n"
"buffer_load_dwordx4 a[172:175], v1, s[52:55], s95 offen\n"
"s_xor_b32 s95, s17, 3\n"
"s_lshl_b32 s95, s95, 6\n"
"s_add_u32 s95, s95, 45056\n"
"buffer_load_dwordx4 a[176:179], v1, s[52:55], s95 offen\n"
"s_waitcnt vmcnt(3)\n"
"v_mfma_f32_16x16x32_bf16 v[20:23], a[164:167], a[52:55], 0\n"
"s_waitcnt vmcnt(2)\n"
"v_mfma_f32_16x16x32_bf16 v[20:23], a[168:171], a[56:59], v[20:23]\n"
"s_waitcnt vmcnt(1)\n"
"v_mfma_f32_16x16x32_bf16 v[20:23], a[172:175], a[60:63], v[20:23]\n"
"s_waitcnt vmcnt(0)\n"
"v_mfma_f32_16x16x32_bf16 v[20:23], a[176:179], a[64:67], v[20:23]\n"
"s_nop 7\n"
"v_sub_f32 v20, v20, v4\n"
"v_sub_f32 v21, v21, v4\n"
"v_sub_f32 v22, v22, v4\n"
"v_sub_f32 v23, v23, v4\n"
"v_exp_f32 v20, v20\n"
"v_exp_f32 v21, v21\n"
"v_exp_f32 v22, v22\n"
"v_exp_f32 v23, v23\n"
"v_cvt_pk_bf16_f32 v94, v20, v21\n"
"v_cvt_pk_bf16_f32 v95, v22, v23\n"
"v_mfma_f32_16x16x32_bf16 v[28:31], a[164:167], a[68:71], 0\n"
"v_mfma_f32_16x16x32_bf16 v[28:31], a[168:171], a[72:75], v[28:31]\n"
"v_mfma_f32_16x16x32_bf16 v[28:31], a[172:175], a[76:79], v[28:31]\n"
"v_mfma_f32_16x16x32_bf16 v[28:31], a[176:179], a[80:83], v[28:31]\n"
"s_nop 7\n"
"v_sub_f32 v28, v28, v5\n"
"v_sub_f32 v29, v29, v5\n"
"v_sub_f32 v30, v30, v5\n"
"v_sub_f32 v31, v31, v5\n"
"v_exp_f32 v28, v28\n"
"v_exp_f32 v29, v29\n"
"v_exp_f32 v30, v30\n"
"v_exp_f32 v31, v31\n"
"v_cvt_pk_bf16_f32 v98, v28, v29\n"
"v_cvt_pk_bf16_f32 v99, v30, v31\n"
"v_mfma_f32_16x16x32_bf16 v[36:39], a[164:167], a[84:87], 0\n"
"v_mfma_f32_16x16x32_bf16 v[36:39], a[168:171], a[88:91], v[36:39]\n"
"v_mfma_f32_16x16x32_bf16 v[36:39], a[172:175], a[92:95], v[36:39]\n"
"v_mfma_f32_16x16x32_bf16 v[36:39], a[176:179], a[96:99], v[36:39]\n"
"s_nop 7\n"
"v_sub_f32 v36, v36, v6\n"
"v_sub_f32 v37, v37, v6\n"
"v_sub_f32 v38, v38, v6\n"
"v_sub_f32 v39, v39, v6\n"
"v_exp_f32 v36, v36\n"
"v_exp_f32 v37, v37\n"
"v_exp_f32 v38, v38\n"
"v_exp_f32 v39, v39\n"
"v_cvt_pk_bf16_f32 v102, v36, v37\n"
"v_cvt_pk_bf16_f32 v103, v38, v39\n"
"v_mfma_f32_16x16x32_bf16 v[20:23], a[164:167], a[100:103], 0\n"
"v_mfma_f32_16x16x32_bf16 v[20:23], a[168:171], a[104:107], v[20:23]\n"
"v_mfma_f32_16x16x32_bf16 v[20:23], a[172:175], a[108:111], v[20:23]\n"
"v_mfma_f32_16x16x32_bf16 v[20:23], a[176:179], a[112:115], v[20:23]\n"
"s_nop 7\n"
"v_sub_f32 v20, v20, v7\n"
"v_sub_f32 v21, v21, v7\n"
"v_sub_f32 v22, v22, v7\n"
"v_sub_f32 v23, v23, v7\n"
"v_exp_f32 v20, v20\n"
"v_exp_f32 v21, v21\n"
"v_exp_f32 v22, v22\n"
"v_exp_f32 v23, v23\n"
"v_cvt_pk_bf16_f32 v106, v20, v21\n"
"v_cvt_pk_bf16_f32 v107, v22, v23\n"
"s_waitcnt lgkmcnt(0)\n"
"v_mfma_f32_16x16x32_bf16 a[20:23], a[196:199], v[92:95], a[20:23]\n"
"v_mfma_f32_16x16x32_bf16 a[36:39], a[200:203], v[92:95], a[36:39]\n"
"v_mfma_f32_16x16x32_bf16 a[4:7], a[0:3], v[92:95], a[4:7]\n"
"v_mfma_f32_16x16x32_bf16 a[24:27], a[196:199], v[96:99], a[24:27]\n"
"v_mfma_f32_16x16x32_bf16 a[40:43], a[200:203], v[96:99], a[40:43]\n"
"v_mfma_f32_16x16x32_bf16 a[8:11], a[0:3], v[96:99], a[8:11]\n"
"v_mfma_f32_16x16x32_bf16 a[28:31], a[196:199], v[100:103], a[28:31]\n"
"v_mfma_f32_16x16x32_bf16 a[44:47], a[200:203], v[100:103], a[44:47]\n"
"v_mfma_f32_16x16x32_bf16 a[12:15], a[0:3], v[100:103], a[12:15]\n"
"v_mfma_f32_16x16x32_bf16 a[32:35], a[196:199], v[104:107], a[32:35]\n"
"v_mfma_f32_16x16x32_bf16 a[48:51], a[200:203], v[104:107], a[48:51]\n"
"v_mfma_f32_16x16x32_bf16 a[16:19], a[0:3], v[104:107], a[16:19]\n"
"ds_read_b64_tr_b16 a[196:197], v8 offset:24576\n"
"ds_read_b64_tr_b16 a[198:199], v8 offset:26624\n"
"ds_read_b64_tr_b16 a[200:201], v9 offset:24576\n"
"ds_read_b64_tr_b16 a[202:203], v9 offset:26624\n"
"s_xor_b32 s95, s17, 0\n"
"s_lshl_b32 s95, s95, 6\n"
"s_add_u32 s95, s95, 49152\n"
"buffer_load_dwordx4 a[164:167], v1, s[52:55], s95 offen\n"
"s_xor_b32 s95, s17, 1\n"
"s_lshl_b32 s95, s95, 6\n"
"s_add_u32 s95, s95, 49152\n"
"buffer_load_dwordx4 a[168:171], v1, s[52:55], s95 offen\n"
"s_xor_b32 s95, s17, 2\n"
"s_lshl_b32 s95, s95, 6\n"
"s_add_u32 s95, s95, 49152\n"
"buffer_load_dwordx4 a[172:175], v1, s[52:55], s95 offen\n"
"s_xor_b32 s95, s17, 3\n"
"s_lshl_b32 s95, s95, 6\n"
"s_add_u32 s95, s95, 49152\n"
"buffer_load_dwordx4 a[176:179], v1, s[52:55], s95 offen\n"
"s_waitcnt vmcnt(3)\n"
"v_mfma_f32_16x16x32_bf16 v[20:23], a[164:167], a[52:55], 0\n"
"s_waitcnt vmcnt(2)\n"
"v_mfma_f32_16x16x32_bf16 v[20:23], a[168:171], a[56:59], v[20:23]\n"
"s_waitcnt vmcnt(1)\n"
"v_mfma_f32_16x16x32_bf16 v[20:23], a[172:175], a[60:63], v[20:23]\n"
"s_waitcnt vmcnt(0)\n"
"v_mfma_f32_16x16x32_bf16 v[20:23], a[176:179], a[64:67], v[20:23]\n"
"s_nop 7\n"
"v_sub_f32 v20, v20, v4\n"
"v_sub_f32 v21, v21, v4\n"
"v_sub_f32 v22, v22, v4\n"
"v_sub_f32 v23, v23, v4\n"
"v_exp_f32 v20, v20\n"
"v_exp_f32 v21, v21\n"
"v_exp_f32 v22, v22\n"
"v_exp_f32 v23, v23\n"
"v_cvt_pk_bf16_f32 v108, v20, v21\n"
"v_cvt_pk_bf16_f32 v109, v22, v23\n"
"v_mfma_f32_16x16x32_bf16 v[28:31], a[164:167], a[68:71], 0\n"
"v_mfma_f32_16x16x32_bf16 v[28:31], a[168:171], a[72:75], v[28:31]\n"
"v_mfma_f32_16x16x32_bf16 v[28:31], a[172:175], a[76:79], v[28:31]\n"
"v_mfma_f32_16x16x32_bf16 v[28:31], a[176:179], a[80:83], v[28:31]\n"
"s_nop 7\n"
"v_sub_f32 v28, v28, v5\n"
"v_sub_f32 v29, v29, v5\n"
"v_sub_f32 v30, v30, v5\n"
"v_sub_f32 v31, v31, v5\n"
"v_exp_f32 v28, v28\n"
"v_exp_f32 v29, v29\n"
"v_exp_f32 v30, v30\n"
"v_exp_f32 v31, v31\n"
"v_cvt_pk_bf16_f32 v112, v28, v29\n"
"v_cvt_pk_bf16_f32 v113, v30, v31\n"
"v_mfma_f32_16x16x32_bf16 v[36:39], a[164:167], a[84:87], 0\n"
"v_mfma_f32_16x16x32_bf16 v[36:39], a[168:171], a[88:91], v[36:39]\n"
"v_mfma_f32_16x16x32_bf16 v[36:39], a[172:175], a[92:95], v[36:39]\n"
"v_mfma_f32_16x16x32_bf16 v[36:39], a[176:179], a[96:99], v[36:39]\n"
"s_nop 7\n"
"v_sub_f32 v36, v36, v6\n"
"v_sub_f32 v37, v37, v6\n"
"v_sub_f32 v38, v38, v6\n"
"v_sub_f32 v39, v39, v6\n"
"v_exp_f32 v36, v36\n"
"v_exp_f32 v37, v37\n"
"v_exp_f32 v38, v38\n"
"v_exp_f32 v39, v39\n"
"v_cvt_pk_bf16_f32 v116, v36, v37\n"
"v_cvt_pk_bf16_f32 v117, v38, v39\n"
"v_mfma_f32_16x16x32_bf16 v[20:23], a[164:167], a[100:103], 0\n"
"v_mfma_f32_16x16x32_bf16 v[20:23], a[168:171], a[104:107], v[20:23]\n"
"v_mfma_f32_16x16x32_bf16 v[20:23], a[172:175], a[108:111], v[20:23]\n"
"v_mfma_f32_16x16x32_bf16 v[20:23], a[176:179], a[112:115], v[20:23]\n"
"s_nop 7\n"
"v_sub_f32 v20, v20, v7\n"
"v_sub_f32 v21, v21, v7\n"
"v_sub_f32 v22, v22, v7\n"
"v_sub_f32 v23, v23, v7\n"
"v_exp_f32 v20, v20\n"
"v_exp_f32 v21, v21\n"
"v_exp_f32 v22, v22\n"
"v_exp_f32 v23, v23\n"
"v_cvt_pk_bf16_f32 v120, v20, v21\n"
"v_cvt_pk_bf16_f32 v121, v22, v23\n"
"s_xor_b32 s95, s17, 0\n"
"s_lshl_b32 s95, s95, 6\n"
"s_add_u32 s95, s95, 53248\n"
"buffer_load_dwordx4 a[164:167], v1, s[52:55], s95 offen\n"
"s_xor_b32 s95, s17, 1\n"
"s_lshl_b32 s95, s95, 6\n"
"s_add_u32 s95, s95, 53248\n"
"buffer_load_dwordx4 a[168:171], v1, s[52:55], s95 offen\n"
"s_xor_b32 s95, s17, 2\n"
"s_lshl_b32 s95, s95, 6\n"
"s_add_u32 s95, s95, 53248\n"
"buffer_load_dwordx4 a[172:175], v1, s[52:55], s95 offen\n"
"s_xor_b32 s95, s17, 3\n"
"s_lshl_b32 s95, s95, 6\n"
"s_add_u32 s95, s95, 53248\n"
"buffer_load_dwordx4 a[176:179], v1, s[52:55], s95 offen\n"
"s_waitcnt vmcnt(3)\n"
"v_mfma_f32_16x16x32_bf16 v[20:23], a[164:167], a[52:55], 0\n"
"s_waitcnt vmcnt(2)\n"
"v_mfma_f32_16x16x32_bf16 v[20:23], a[168:171], a[56:59], v[20:23]\n"
"s_waitcnt vmcnt(1)\n"
"v_mfma_f32_16x16x32_bf16 v[20:23], a[172:175], a[60:63], v[20:23]\n"
"s_waitcnt vmcnt(0)\n"
"v_mfma_f32_16x16x32_bf16 v[20:23], a[176:179], a[64:67], v[20:23]\n"
"s_nop 7\n"
"v_sub_f32 v20, v20, v4\n"
"v_sub_f32 v21, v21, v4\n"
"v_sub_f32 v22, v22, v4\n"
"v_sub_f32 v23, v23, v4\n"
"v_exp_f32 v20, v20\n"
"v_exp_f32 v21, v21\n"
"v_exp_f32 v22, v22\n"
"v_exp_f32 v23, v23\n"
"v_cvt_pk_bf16_f32 v110, v20, v21\n"
"v_cvt_pk_bf16_f32 v111, v22, v23\n"
"v_mfma_f32_16x16x32_bf16 v[28:31], a[164:167], a[68:71], 0\n"
"v_mfma_f32_16x16x32_bf16 v[28:31], a[168:171], a[72:75], v[28:31]\n"
"v_mfma_f32_16x16x32_bf16 v[28:31], a[172:175], a[76:79], v[28:31]\n"
"v_mfma_f32_16x16x32_bf16 v[28:31], a[176:179], a[80:83], v[28:31]\n"
"s_nop 7\n"
"v_sub_f32 v28, v28, v5\n"
"v_sub_f32 v29, v29, v5\n"
"v_sub_f32 v30, v30, v5\n"
"v_sub_f32 v31, v31, v5\n"
"v_exp_f32 v28, v28\n"
"v_exp_f32 v29, v29\n"
"v_exp_f32 v30, v30\n"
"v_exp_f32 v31, v31\n"
"v_cvt_pk_bf16_f32 v114, v28, v29\n"
"v_cvt_pk_bf16_f32 v115, v30, v31\n"
"v_mfma_f32_16x16x32_bf16 v[36:39], a[164:167], a[84:87], 0\n"
"v_mfma_f32_16x16x32_bf16 v[36:39], a[168:171], a[88:91], v[36:39]\n"
"v_mfma_f32_16x16x32_bf16 v[36:39], a[172:175], a[92:95], v[36:39]\n"
"v_mfma_f32_16x16x32_bf16 v[36:39], a[176:179], a[96:99], v[36:39]\n"
"s_nop 7\n"
"v_sub_f32 v36, v36, v6\n"
"v_sub_f32 v37, v37, v6\n"
"v_sub_f32 v38, v38, v6\n"
"v_sub_f32 v39, v39, v6\n"
"v_exp_f32 v36, v36\n"
"v_exp_f32 v37, v37\n"
"v_exp_f32 v38, v38\n"
"v_exp_f32 v39, v39\n"
"v_cvt_pk_bf16_f32 v118, v36, v37\n"
"v_cvt_pk_bf16_f32 v119, v38, v39\n"
"v_mfma_f32_16x16x32_bf16 v[20:23], a[164:167], a[100:103], 0\n"
"v_mfma_f32_16x16x32_bf16 v[20:23], a[168:171], a[104:107], v[20:23]\n"
"v_mfma_f32_16x16x32_bf16 v[20:23], a[172:175], a[108:111], v[20:23]\n"
"v_mfma_f32_16x16x32_bf16 v[20:23], a[176:179], a[112:115], v[20:23]\n"
"s_nop 7\n"
"v_sub_f32 v20, v20, v7\n"
"v_sub_f32 v21, v21, v7\n"
"v_sub_f32 v22, v22, v7\n"
"v_sub_f32 v23, v23, v7\n"
"v_exp_f32 v20, v20\n"
"v_exp_f32 v21, v21\n"
"v_exp_f32 v22, v22\n"
"v_exp_f32 v23, v23\n"
"v_cvt_pk_bf16_f32 v122, v20, v21\n"
"v_cvt_pk_bf16_f32 v123, v22, v23\n"
"s_waitcnt lgkmcnt(0)\n"
"v_mfma_f32_16x16x32_bf16 a[20:23], a[196:199], v[108:111], a[20:23]\n"
"v_mfma_f32_16x16x32_bf16 a[36:39], a[200:203], v[108:111], a[36:39]\n"
"v_mfma_f32_16x16x32_bf16 a[4:7], a[0:3], v[108:111], a[4:7]\n"
"v_mfma_f32_16x16x32_bf16 a[24:27], a[196:199], v[112:115], a[24:27]\n"
"v_mfma_f32_16x16x32_bf16 a[40:43], a[200:203], v[112:115], a[40:43]\n"
"v_mfma_f32_16x16x32_bf16 a[8:11], a[0:3], v[112:115], a[8:11]\n"
"v_mfma_f32_16x16x32_bf16 a[28:31], a[196:199], v[116:119], a[28:31]\n"
"v_mfma_f32_16x16x32_bf16 a[44:47], a[200:203], v[116:119], a[44:47]\n"
"v_mfma_f32_16x16x32_bf16 a[12:15], a[0:3], v[116:119], a[12:15]\n"
"v_mfma_f32_16x16x32_bf16 a[32:35], a[196:199], v[120:123], a[32:35]\n"
"v_mfma_f32_16x16x32_bf16 a[48:51], a[200:203], v[120:123], a[48:51]\n"
"v_mfma_f32_16x16x32_bf16 a[16:19], a[0:3], v[120:123], a[16:19]\n"
"ds_read_b64_tr_b16 a[196:197], v8 offset:28672\n"
"ds_read_b64_tr_b16 a[198:199], v8 offset:30720\n"
"ds_read_b64_tr_b16 a[200:201], v9 offset:28672\n"
"ds_read_b64_tr_b16 a[202:203], v9 offset:30720\n"
"s_xor_b32 s95, s17, 0\n"
"s_lshl_b32 s95, s95, 6\n"
"s_add_u32 s95, s95, 57344\n"
"buffer_load_dwordx4 a[164:167], v1, s[52:55], s95 offen\n"
"s_xor_b32 s95, s17, 1\n"
"s_lshl_b32 s95, s95, 6\n"
"s_add_u32 s95, s95, 57344\n"
"buffer_load_dwordx4 a[168:171], v1, s[52:55], s95 offen\n"
"s_xor_b32 s95, s17, 2\n"
"s_lshl_b32 s95, s95, 6\n"
"s_add_u32 s95, s95, 57344\n"
"buffer_load_dwordx4 a[172:175], v1, s[52:55], s95 offen\n"
"s_xor_b32 s95, s17, 3\n"
"s_lshl_b32 s95, s95, 6\n"
"s_add_u32 s95, s95, 57344\n"
"buffer_load_dwordx4 a[176:179], v1, s[52:55], s95 offen\n"
"s_waitcnt vmcnt(3)\n"
"v_mfma_f32_16x16x32_bf16 v[20:23], a[164:167], a[52:55], 0\n"
"s_waitcnt vmcnt(2)\n"
"v_mfma_f32_16x16x32_bf16 v[20:23], a[168:171], a[56:59], v[20:23]\n"
"s_waitcnt vmcnt(1)\n"
"v_mfma_f32_16x16x32_bf16 v[20:23], a[172:175], a[60:63], v[20:23]\n"
"s_waitcnt vmcnt(0)\n"
"v_mfma_f32_16x16x32_bf16 v[20:23], a[176:179], a[64:67], v[20:23]\n"
"s_nop 7\n"
"v_sub_f32 v20, v20, v4\n"
"v_sub_f32 v21, v21, v4\n"
"v_sub_f32 v22, v22, v4\n"
"v_sub_f32 v23, v23, v4\n"
"v_exp_f32 v20, v20\n"
"v_exp_f32 v21, v21\n"
"v_exp_f32 v22, v22\n"
"v_exp_f32 v23, v23\n"
"v_cvt_pk_bf16_f32 v124, v20, v21\n"
"v_cvt_pk_bf16_f32 v125, v22, v23\n"
"v_mfma_f32_16x16x32_bf16 v[28:31], a[164:167], a[68:71], 0\n"
"v_mfma_f32_16x16x32_bf16 v[28:31], a[168:171], a[72:75], v[28:31]\n"
"v_mfma_f32_16x16x32_bf16 v[28:31], a[172:175], a[76:79], v[28:31]\n"
"v_mfma_f32_16x16x32_bf16 v[28:31], a[176:179], a[80:83], v[28:31]\n"
"s_nop 7\n"
"v_sub_f32 v28, v28, v5\n"
"v_sub_f32 v29, v29, v5\n"
"v_sub_f32 v30, v30, v5\n"
"v_sub_f32 v31, v31, v5\n"
"v_exp_f32 v28, v28\n"
"v_exp_f32 v29, v29\n"
"v_exp_f32 v30, v30\n"
"v_exp_f32 v31, v31\n"
"v_cvt_pk_bf16_f32 v128, v28, v29\n"
"v_cvt_pk_bf16_f32 v129, v30, v31\n"
"v_mfma_f32_16x16x32_bf16 v[36:39], a[164:167], a[84:87], 0\n"
"v_mfma_f32_16x16x32_bf16 v[36:39], a[168:171], a[88:91], v[36:39]\n"
"v_mfma_f32_16x16x32_bf16 v[36:39], a[172:175], a[92:95], v[36:39]\n"
"v_mfma_f32_16x16x32_bf16 v[36:39], a[176:179], a[96:99], v[36:39]\n"
"s_nop 7\n"
"v_sub_f32 v36, v36, v6\n"
"v_sub_f32 v37, v37, v6\n"
"v_sub_f32 v38, v38, v6\n"
"v_sub_f32 v39, v39, v6\n"
"v_exp_f32 v36, v36\n"
"v_exp_f32 v37, v37\n"
"v_exp_f32 v38, v38\n"
"v_exp_f32 v39, v39\n"
"v_cvt_pk_bf16_f32 v132, v36, v37\n"
"v_cvt_pk_bf16_f32 v133, v38, v39\n"
"v_mfma_f32_16x16x32_bf16 v[20:23], a[164:167], a[100:103], 0\n"
"v_mfma_f32_16x16x32_bf16 v[20:23], a[168:171], a[104:107], v[20:23]\n"
"v_mfma_f32_16x16x32_bf16 v[20:23], a[172:175], a[108:111], v[20:23]\n"
"v_mfma_f32_16x16x32_bf16 v[20:23], a[176:179], a[112:115], v[20:23]\n"
"s_nop 7\n"
"v_sub_f32 v20, v20, v7\n"
"v_sub_f32 v21, v21, v7\n"
"v_sub_f32 v22, v22, v7\n"
"v_sub_f32 v23, v23, v7\n"
"v_exp_f32 v20, v20\n"
"v_exp_f32 v21, v21\n"
"v_exp_f32 v22, v22\n"
"v_exp_f32 v23, v23\n"
"v_cvt_pk_bf16_f32 v136, v20, v21\n"
"v_cvt_pk_bf16_f32 v137, v22, v23\n"
"s_xor_b32 s95, s17, 0\n"
"s_lshl_b32 s95, s95, 6\n"
"s_add_u32 s95, s95, 61440\n"
"buffer_load_dwordx4 a[164:167], v1, s[52:55], s95 offen\n"
"s_xor_b32 s95, s17, 1\n"
"s_lshl_b32 s95, s95, 6\n"
"s_add_u32 s95, s95, 61440\n"
"buffer_load_dwordx4 a[168:171], v1, s[52:55], s95 offen\n"
"s_xor_b32 s95, s17, 2\n"
"s_lshl_b32 s95, s95, 6\n"
"s_add_u32 s95, s95, 61440\n"
"buffer_load_dwordx4 a[172:175], v1, s[52:55], s95 offen\n"
"s_xor_b32 s95, s17, 3\n"
"s_lshl_b32 s95, s95, 6\n"
"s_add_u32 s95, s95, 61440\n"
"buffer_load_dwordx4 a[176:179], v1, s[52:55], s95 offen\n"
"s_waitcnt vmcnt(3)\n"
"v_mfma_f32_16x16x32_bf16 v[20:23], a[164:167], a[52:55], 0\n"
"s_waitcnt vmcnt(2)\n"
"v_mfma_f32_16x16x32_bf16 v[20:23], a[168:171], a[56:59], v[20:23]\n"
"s_waitcnt vmcnt(1)\n"
"v_mfma_f32_16x16x32_bf16 v[20:23], a[172:175], a[60:63], v[20:23]\n"
"s_waitcnt vmcnt(0)\n"
"v_mfma_f32_16x16x32_bf16 v[20:23], a[176:179], a[64:67], v[20:23]\n"
"s_nop 7\n"
"v_sub_f32 v20, v20, v4\n"
"v_sub_f32 v21, v21, v4\n"
"v_sub_f32 v22, v22, v4\n"
"v_sub_f32 v23, v23, v4\n"
"v_exp_f32 v20, v20\n"
"v_exp_f32 v21, v21\n"
"v_exp_f32 v22, v22\n"
"v_exp_f32 v23, v23\n"
"v_cvt_pk_bf16_f32 v126, v20, v21\n"
"v_cvt_pk_bf16_f32 v127, v22, v23\n"
"v_mfma_f32_16x16x32_bf16 v[28:31], a[164:167], a[68:71], 0\n"
"v_mfma_f32_16x16x32_bf16 v[28:31], a[168:171], a[72:75], v[28:31]\n"
"v_mfma_f32_16x16x32_bf16 v[28:31], a[172:175], a[76:79], v[28:31]\n"
"v_mfma_f32_16x16x32_bf16 v[28:31], a[176:179], a[80:83], v[28:31]\n"
"s_nop 7\n"
"v_sub_f32 v28, v28, v5\n"
"v_sub_f32 v29, v29, v5\n"
"v_sub_f32 v30, v30, v5\n"
"v_sub_f32 v31, v31, v5\n"
"v_exp_f32 v28, v28\n"
"v_exp_f32 v29, v29\n"
"v_exp_f32 v30, v30\n"
"v_exp_f32 v31, v31\n"
"v_cvt_pk_bf16_f32 v130, v28, v29\n"
"v_cvt_pk_bf16_f32 v131, v30, v31\n"
"v_mfma_f32_16x16x32_bf16 v[36:39], a[164:167], a[84:87], 0\n"
"v_mfma_f32_16x16x32_bf16 v[36:39], a[168:171], a[88:91], v[36:39]\n"
"v_mfma_f32_16x16x32_bf16 v[36:39], a[172:175], a[92:95], v[36:39]\n"
"v_mfma_f32_16x16x32_bf16 v[36:39], a[176:179], a[96:99], v[36:39]\n"
"s_nop 7\n"
"v_sub_f32 v36, v36, v6\n"
"v_sub_f32 v37, v37, v6\n"
"v_sub_f32 v38, v38, v6\n"
"v_sub_f32 v39, v39, v6\n"
"v_exp_f32 v36, v36\n"
"v_exp_f32 v37, v37\n"
"v_exp_f32 v38, v38\n"
"v_exp_f32 v39, v39\n"
"v_cvt_pk_bf16_f32 v134, v36, v37\n"
"v_cvt_pk_bf16_f32 v135, v38, v39\n"
"v_mfma_f32_16x16x32_bf16 v[20:23], a[164:167], a[100:103], 0\n"
"v_mfma_f32_16x16x32_bf16 v[20:23], a[168:171], a[104:107], v[20:23]\n"
"v_mfma_f32_16x16x32_bf16 v[20:23], a[172:175], a[108:111], v[20:23]\n"
"v_mfma_f32_16x16x32_bf16 v[20:23], a[176:179], a[112:115], v[20:23]\n"
"s_nop 7\n"
"v_sub_f32 v20, v20, v7\n"
"v_sub_f32 v21, v21, v7\n"
"v_sub_f32 v22, v22, v7\n"
"v_sub_f32 v23, v23, v7\n"
"v_exp_f32 v20, v20\n"
"v_exp_f32 v21, v21\n"
"v_exp_f32 v22, v22\n"
"v_exp_f32 v23, v23\n"
"v_cvt_pk_bf16_f32 v138, v20, v21\n"
"v_cvt_pk_bf16_f32 v139, v22, v23\n"
"s_waitcnt lgkmcnt(0)\n"
"v_mfma_f32_16x16x32_bf16 a[20:23], a[196:199], v[124:127], a[20:23]\n"
"v_mfma_f32_16x16x32_bf16 a[36:39], a[200:203], v[124:127], a[36:39]\n"
"v_mfma_f32_16x16x32_bf16 a[4:7], a[0:3], v[124:127], a[4:7]\n"
"v_mfma_f32_16x16x32_bf16 a[24:27], a[196:199], v[128:131], a[24:27]\n"
"v_mfma_f32_16x16x32_bf16 a[40:43], a[200:203], v[128:131], a[40:43]\n"
"v_mfma_f32_16x16x32_bf16 a[8:11], a[0:3], v[128:131], a[8:11]\n"
"v_mfma_f32_16x16x32_bf16 a[28:31], a[196:199], v[132:135], a[28:31]\n"
"v_mfma_f32_16x16x32_bf16 a[44:47], a[200:203], v[132:135], a[44:47]\n"
"v_mfma_f32_16x16x32_bf16 a[12:15], a[0:3], v[132:135], a[12:15]\n"
"v_mfma_f32_16x16x32_bf16 a[32:35], a[196:199], v[136:139], a[32:35]\n"
"v_mfma_f32_16x16x32_bf16 a[48:51], a[200:203], v[136:139], a[48:51]\n"
"v_mfma_f32_16x16x32_bf16 a[16:19], a[0:3], v[136:139], a[16:19]\n"
"ds_read_b64_tr_b16 a[196:197], v8 offset:32768\n"
"ds_read_b64_tr_b16 a[198:199], v8 offset:34816\n"
"ds_read_b64_tr_b16 a[200:201], v9 offset:32768\n"
"ds_read_b64_tr_b16 a[202:203], v9 offset:34816\n"
"s_xor_b32 s95, s17, 0\n"
"s_lshl_b32 s95, s95, 6\n"
"s_add_u32 s95, s95, 65536\n"
"buffer_load_dwordx4 a[164:167], v1, s[52:55], s95 offen\n"
"s_xor_b32 s95, s17, 1\n"
"s_lshl_b32 s95, s95, 6\n"
"s_add_u32 s95, s95, 65536\n"
"buffer_load_dwordx4 a[168:171], v1, s[52:55], s95 offen\n"
"s_xor_b32 s95, s17, 2\n"
"s_lshl_b32 s95, s95, 6\n"
"s_add_u32 s95, s95, 65536\n"
"buffer_load_dwordx4 a[172:175], v1, s[52:55], s95 offen\n"
"s_xor_b32 s95, s17, 3\n"
"s_lshl_b32 s95, s95, 6\n"
"s_add_u32 s95, s95, 65536\n"
"buffer_load_dwordx4 a[176:179], v1, s[52:55], s95 offen\n"
"s_waitcnt vmcnt(3)\n"
"v_mfma_f32_16x16x32_bf16 v[20:23], a[164:167], a[52:55], 0\n"
"s_waitcnt vmcnt(2)\n"
"v_mfma_f32_16x16x32_bf16 v[20:23], a[168:171], a[56:59], v[20:23]\n"
"s_waitcnt vmcnt(1)\n"
"v_mfma_f32_16x16x32_bf16 v[20:23], a[172:175], a[60:63], v[20:23]\n"
"s_waitcnt vmcnt(0)\n"
"v_mfma_f32_16x16x32_bf16 v[20:23], a[176:179], a[64:67], v[20:23]\n"
"s_nop 7\n"
"v_sub_f32 v20, v20, v4\n"
"v_sub_f32 v21, v21, v4\n"
"v_sub_f32 v22, v22, v4\n"
"v_sub_f32 v23, v23, v4\n"
"v_exp_f32 v20, v20\n"
"v_exp_f32 v21, v21\n"
"v_exp_f32 v22, v22\n"
"v_exp_f32 v23, v23\n"
"v_cvt_pk_bf16_f32 v140, v20, v21\n"
"v_cvt_pk_bf16_f32 v141, v22, v23\n"
"v_mfma_f32_16x16x32_bf16 v[28:31], a[164:167], a[68:71], 0\n"
"v_mfma_f32_16x16x32_bf16 v[28:31], a[168:171], a[72:75], v[28:31]\n"
"v_mfma_f32_16x16x32_bf16 v[28:31], a[172:175], a[76:79], v[28:31]\n"
"v_mfma_f32_16x16x32_bf16 v[28:31], a[176:179], a[80:83], v[28:31]\n"
"s_nop 7\n"
"v_sub_f32 v28, v28, v5\n"
"v_sub_f32 v29, v29, v5\n"
"v_sub_f32 v30, v30, v5\n"
"v_sub_f32 v31, v31, v5\n"
"v_exp_f32 v28, v28\n"
"v_exp_f32 v29, v29\n"
"v_exp_f32 v30, v30\n"
"v_exp_f32 v31, v31\n"
"v_cvt_pk_bf16_f32 v144, v28, v29\n"
"v_cvt_pk_bf16_f32 v145, v30, v31\n"
"v_mfma_f32_16x16x32_bf16 v[36:39], a[164:167], a[84:87], 0\n"
"v_mfma_f32_16x16x32_bf16 v[36:39], a[168:171], a[88:91], v[36:39]\n"
"v_mfma_f32_16x16x32_bf16 v[36:39], a[172:175], a[92:95], v[36:39]\n"
"v_mfma_f32_16x16x32_bf16 v[36:39], a[176:179], a[96:99], v[36:39]\n"
"s_nop 7\n"
"v_sub_f32 v36, v36, v6\n"
"v_sub_f32 v37, v37, v6\n"
"v_sub_f32 v38, v38, v6\n"
"v_sub_f32 v39, v39, v6\n"
"v_exp_f32 v36, v36\n"
"v_exp_f32 v37, v37\n"
"v_exp_f32 v38, v38\n"
"v_exp_f32 v39, v39\n"
"v_cvt_pk_bf16_f32 v148, v36, v37\n"
"v_cvt_pk_bf16_f32 v149, v38, v39\n"
"v_mfma_f32_16x16x32_bf16 v[20:23], a[164:167], a[100:103], 0\n"
"v_mfma_f32_16x16x32_bf16 v[20:23], a[168:171], a[104:107], v[20:23]\n"
"v_mfma_f32_16x16x32_bf16 v[20:23], a[172:175], a[108:111], v[20:23]\n"
"v_mfma_f32_16x16x32_bf16 v[20:23], a[176:179], a[112:115], v[20:23]\n"
"s_nop 7\n"
"v_sub_f32 v20, v20, v7\n"
"v_sub_f32 v21, v21, v7\n"
"v_sub_f32 v22, v22, v7\n"
"v_sub_f32 v23, v23, v7\n"
"v_exp_f32 v20, v20\n"
"v_exp_f32 v21, v21\n"
"v_exp_f32 v22, v22\n"
"v_exp_f32 v23, v23\n"
"v_cvt_pk_bf16_f32 v152, v20, v21\n"
"v_cvt_pk_bf16_f32 v153, v22, v23\n"
"s_xor_b32 s95, s17, 0\n"
"s_lshl_b32 s95, s95, 6\n"
"s_add_u32 s95, s95, 69632\n"
"buffer_load_dwordx4 a[164:167], v1, s[52:55], s95 offen\n"
"s_xor_b32 s95, s17, 1\n"
"s_lshl_b32 s95, s95, 6\n"
"s_add_u32 s95, s95, 69632\n"
"buffer_load_dwordx4 a[168:171], v1, s[52:55], s95 offen\n"
"s_xor_b32 s95, s17, 2\n"
"s_lshl_b32 s95, s95, 6\n"
"s_add_u32 s95, s95, 69632\n"
"buffer_load_dwordx4 a[172:175], v1, s[52:55], s95 offen\n"
"s_xor_b32 s95, s17, 3\n"
"s_lshl_b32 s95, s95, 6\n"
"s_add_u32 s95, s95, 69632\n"
"buffer_load_dwordx4 a[176:179], v1, s[52:55], s95 offen\n"
"s_waitcnt vmcnt(3)\n"
"v_mfma_f32_16x16x32_bf16 v[20:23], a[164:167], a[52:55], 0\n"
"s_waitcnt vmcnt(2)\n"
"v_mfma_f32_16x16x32_bf16 v[20:23], a[168:171], a[56:59], v[20:23]\n"
"s_waitcnt vmcnt(1)\n"
"v_mfma_f32_16x16x32_bf16 v[20:23], a[172:175], a[60:63], v[20:23]\n"
"s_waitcnt vmcnt(0)\n"
"v_mfma_f32_16x16x32_bf16 v[20:23], a[176:179], a[64:67], v[20:23]\n"
"s_nop 7\n"
"v_sub_f32 v20, v20, v4\n"
"v_sub_f32 v21, v21, v4\n"
"v_sub_f32 v22, v22, v4\n"
"v_sub_f32 v23, v23, v4\n"
"v_exp_f32 v20, v20\n"
"v_exp_f32 v21, v21\n"
"v_exp_f32 v22, v22\n"
"v_exp_f32 v23, v23\n"
"v_cvt_pk_bf16_f32 v142, v20, v21\n"
"v_cvt_pk_bf16_f32 v143, v22, v23\n"
"v_mfma_f32_16x16x32_bf16 v[28:31], a[164:167], a[68:71], 0\n"
"v_mfma_f32_16x16x32_bf16 v[28:31], a[168:171], a[72:75], v[28:31]\n"
"v_mfma_f32_16x16x32_bf16 v[28:31], a[172:175], a[76:79], v[28:31]\n"
"v_mfma_f32_16x16x32_bf16 v[28:31], a[176:179], a[80:83], v[28:31]\n"
"s_nop 7\n"
"v_sub_f32 v28, v28, v5\n"
"v_sub_f32 v29, v29, v5\n"
"v_sub_f32 v30, v30, v5\n"
"v_sub_f32 v31, v31, v5\n"
"v_exp_f32 v28, v28\n"
"v_exp_f32 v29, v29\n"
"v_exp_f32 v30, v30\n"
"v_exp_f32 v31, v31\n"
"v_cvt_pk_bf16_f32 v146, v28, v29\n"
"v_cvt_pk_bf16_f32 v147, v30, v31\n"
"v_mfma_f32_16x16x32_bf16 v[36:39], a[164:167], a[84:87], 0\n"
"v_mfma_f32_16x16x32_bf16 v[36:39], a[168:171], a[88:91], v[36:39]\n"
"v_mfma_f32_16x16x32_bf16 v[36:39], a[172:175], a[92:95], v[36:39]\n"
"v_mfma_f32_16x16x32_bf16 v[36:39], a[176:179], a[96:99], v[36:39]\n"
"s_nop 7\n"
"v_sub_f32 v36, v36, v6\n"
"v_sub_f32 v37, v37, v6\n"
"v_sub_f32 v38, v38, v6\n"
"v_sub_f32 v39, v39, v6\n"
"v_exp_f32 v36, v36\n"
"v_exp_f32 v37, v37\n"
"v_exp_f32 v38, v38\n"
"v_exp_f32 v39, v39\n"
"v_cvt_pk_bf16_f32 v150, v36, v37\n"
"v_cvt_pk_bf16_f32 v151, v38, v39\n"
"v_mfma_f32_16x16x32_bf16 v[20:23], a[164:167], a[100:103], 0\n"
"v_mfma_f32_16x16x32_bf16 v[20:23], a[168:171], a[104:107], v[20:23]\n"
"v_mfma_f32_16x16x32_bf16 v[20:23], a[172:175], a[108:111], v[20:23]\n"
"v_mfma_f32_16x16x32_bf16 v[20:23], a[176:179], a[112:115], v[20:23]\n"
"s_nop 7\n"
"v_sub_f32 v20, v20, v7\n"
"v_sub_f32 v21, v21, v7\n"
"v_sub_f32 v22, v22, v7\n"
"v_sub_f32 v23, v23, v7\n"
"v_exp_f32 v20, v20\n"
"v_exp_f32 v21, v21\n"
"v_exp_f32 v22, v22\n"
"v_exp_f32 v23, v23\n"
"v_cvt_pk_bf16_f32 v154, v20, v21\n"
"v_cvt_pk_bf16_f32 v155, v22, v23\n"
"s_waitcnt lgkmcnt(0)\n"
"v_mfma_f32_16x16x32_bf16 a[20:23], a[196:199], v[140:143], a[20:23]\n"
"v_mfma_f32_16x16x32_bf16 a[36:39], a[200:203], v[140:143], a[36:39]\n"
"v_mfma_f32_16x16x32_bf16 a[4:7], a[0:3], v[140:143], a[4:7]\n"
"v_mfma_f32_16x16x32_bf16 a[24:27], a[196:199], v[144:147], a[24:27]\n"
"v_mfma_f32_16x16x32_bf16 a[40:43], a[200:203], v[144:147], a[40:43]\n"
"v_mfma_f32_16x16x32_bf16 a[8:11], a[0:3], v[144:147], a[8:11]\n"
"v_mfma_f32_16x16x32_bf16 a[28:31], a[196:199], v[148:151], a[28:31]\n"
"v_mfma_f32_16x16x32_bf16 a[44:47], a[200:203], v[148:151], a[44:47]\n"
"v_mfma_f32_16x16x32_bf16 a[12:15], a[0:3], v[148:151], a[12:15]\n"
"v_mfma_f32_16x16x32_bf16 a[32:35], a[196:199], v[152:155], a[32:35]\n"
"v_mfma_f32_16x16x32_bf16 a[48:51], a[200:203], v[152:155], a[48:51]\n"
"v_mfma_f32_16x16x32_bf16 a[16:19], a[0:3], v[152:155], a[16:19]\n"
"ds_read_b64_tr_b16 a[196:197], v8 offset:36864\n"
"ds_read_b64_tr_b16 a[198:199], v8 offset:38912\n"
"ds_read_b64_tr_b16 a[200:201], v9 offset:36864\n"
"ds_read_b64_tr_b16 a[202:203], v9 offset:38912\n"
"s_xor_b32 s95, s17, 0\n"
"s_lshl_b32 s95, s95, 6\n"
"s_add_u32 s95, s95, 73728\n"
"buffer_load_dwordx4 a[164:167], v1, s[52:55], s95 offen\n"
"s_xor_b32 s95, s17, 1\n"
"s_lshl_b32 s95, s95, 6\n"
"s_add_u32 s95, s95, 73728\n"
"buffer_load_dwordx4 a[168:171], v1, s[52:55], s95 offen\n"
"s_xor_b32 s95, s17, 2\n"
"s_lshl_b32 s95, s95, 6\n"
"s_add_u32 s95, s95, 73728\n"
"buffer_load_dwordx4 a[172:175], v1, s[52:55], s95 offen\n"
"s_xor_b32 s95, s17, 3\n"
"s_lshl_b32 s95, s95, 6\n"
"s_add_u32 s95, s95, 73728\n"
"buffer_load_dwordx4 a[176:179], v1, s[52:55], s95 offen\n"
"s_waitcnt vmcnt(3)\n"
"v_mfma_f32_16x16x32_bf16 v[20:23], a[164:167], a[52:55], 0\n"
"s_waitcnt vmcnt(2)\n"
"v_mfma_f32_16x16x32_bf16 v[20:23], a[168:171], a[56:59], v[20:23]\n"
"s_waitcnt vmcnt(1)\n"
"v_mfma_f32_16x16x32_bf16 v[20:23], a[172:175], a[60:63], v[20:23]\n"
"s_waitcnt vmcnt(0)\n"
"v_mfma_f32_16x16x32_bf16 v[20:23], a[176:179], a[64:67], v[20:23]\n"
"s_nop 7\n"
"v_sub_f32 v20, v20, v4\n"
"v_sub_f32 v21, v21, v4\n"
"v_sub_f32 v22, v22, v4\n"
"v_sub_f32 v23, v23, v4\n"
"v_exp_f32 v20, v20\n"
"v_exp_f32 v21, v21\n"
"v_exp_f32 v22, v22\n"
"v_exp_f32 v23, v23\n"
"v_cvt_pk_bf16_f32 v156, v20, v21\n"
"v_cvt_pk_bf16_f32 v157, v22, v23\n"
"v_mfma_f32_16x16x32_bf16 v[28:31], a[164:167], a[68:71], 0\n"
"v_mfma_f32_16x16x32_bf16 v[28:31], a[168:171], a[72:75], v[28:31]\n"
"v_mfma_f32_16x16x32_bf16 v[28:31], a[172:175], a[76:79], v[28:31]\n"
"v_mfma_f32_16x16x32_bf16 v[28:31], a[176:179], a[80:83], v[28:31]\n"
"s_nop 7\n"
"v_sub_f32 v28, v28, v5\n"
"v_sub_f32 v29, v29, v5\n"
"v_sub_f32 v30, v30, v5\n"
"v_sub_f32 v31, v31, v5\n"
"v_exp_f32 v28, v28\n"
"v_exp_f32 v29, v29\n"
"v_exp_f32 v30, v30\n"
"v_exp_f32 v31, v31\n"
"v_cvt_pk_bf16_f32 v160, v28, v29\n"
"v_cvt_pk_bf16_f32 v161, v30, v31\n"
"v_mfma_f32_16x16x32_bf16 v[36:39], a[164:167], a[84:87], 0\n"
"v_mfma_f32_16x16x32_bf16 v[36:39], a[168:171], a[88:91], v[36:39]\n"
"v_mfma_f32_16x16x32_bf16 v[36:39], a[172:175], a[92:95], v[36:39]\n"
"v_mfma_f32_16x16x32_bf16 v[36:39], a[176:179], a[96:99], v[36:39]\n"
"s_nop 7\n"
"v_sub_f32 v36, v36, v6\n"
"v_sub_f32 v37, v37, v6\n"
"v_sub_f32 v38, v38, v6\n"
"v_sub_f32 v39, v39, v6\n"
"v_exp_f32 v36, v36\n"
"v_exp_f32 v37, v37\n"
"v_exp_f32 v38, v38\n"
"v_exp_f32 v39, v39\n"
"v_cvt_pk_bf16_f32 v164, v36, v37\n"
"v_cvt_pk_bf16_f32 v165, v38, v39\n"
"v_mfma_f32_16x16x32_bf16 v[20:23], a[164:167], a[100:103], 0\n"
"v_mfma_f32_16x16x32_bf16 v[20:23], a[168:171], a[104:107], v[20:23]\n"
"v_mfma_f32_16x16x32_bf16 v[20:23], a[172:175], a[108:111], v[20:23]\n"
"v_mfma_f32_16x16x32_bf16 v[20:23], a[176:179], a[112:115], v[20:23]\n"
"s_nop 7\n"
"v_sub_f32 v20, v20, v7\n"
"v_sub_f32 v21, v21, v7\n"
"v_sub_f32 v22, v22, v7\n"
"v_sub_f32 v23, v23, v7\n"
"v_exp_f32 v20, v20\n"
"v_exp_f32 v21, v21\n"
"v_exp_f32 v22, v22\n"
"v_exp_f32 v23, v23\n"
"v_cvt_pk_bf16_f32 v168, v20, v21\n"
"v_cvt_pk_bf16_f32 v169, v22, v23\n"
"s_xor_b32 s95, s17, 0\n"
"s_lshl_b32 s95, s95, 6\n"
"s_add_u32 s95, s95, 77824\n"
"buffer_load_dwordx4 a[164:167], v1, s[52:55], s95 offen\n"
"s_xor_b32 s95, s17, 1\n"
"s_lshl_b32 s95, s95, 6\n"
"s_add_u32 s95, s95, 77824\n"
"buffer_load_dwordx4 a[168:171], v1, s[52:55], s95 offen\n"
"s_xor_b32 s95, s17, 2\n"
"s_lshl_b32 s95, s95, 6\n"
"s_add_u32 s95, s95, 77824\n"
"buffer_load_dwordx4 a[172:175], v1, s[52:55], s95 offen\n"
"s_xor_b32 s95, s17, 3\n"
"s_lshl_b32 s95, s95, 6\n"
"s_add_u32 s95, s95, 77824\n"
"buffer_load_dwordx4 a[176:179], v1, s[52:55], s95 offen\n"
"s_waitcnt vmcnt(3)\n"
"v_mfma_f32_16x16x32_bf16 v[20:23], a[164:167], a[52:55], 0\n"
"s_waitcnt vmcnt(2)\n"
"v_mfma_f32_16x16x32_bf16 v[20:23], a[168:171], a[56:59], v[20:23]\n"
"s_waitcnt vmcnt(1)\n"
"v_mfma_f32_16x16x32_bf16 v[20:23], a[172:175], a[60:63], v[20:23]\n"
"s_waitcnt vmcnt(0)\n"
"v_mfma_f32_16x16x32_bf16 v[20:23], a[176:179], a[64:67], v[20:23]\n"
"s_nop 7\n"
"v_sub_f32 v20, v20, v4\n"
"v_sub_f32 v21, v21, v4\n"
"v_sub_f32 v22, v22, v4\n"
"v_sub_f32 v23, v23, v4\n"
"v_exp_f32 v20, v20\n"
"v_exp_f32 v21, v21\n"
"v_exp_f32 v22, v22\n"
"v_exp_f32 v23, v23\n"
"v_cvt_pk_bf16_f32 v158, v20, v21\n"
"v_cvt_pk_bf16_f32 v159, v22, v23\n"
"v_mfma_f32_16x16x32_bf16 v[28:31], a[164:167], a[68:71], 0\n"
"v_mfma_f32_16x16x32_bf16 v[28:31], a[168:171], a[72:75], v[28:31]\n"
"v_mfma_f32_16x16x32_bf16 v[28:31], a[172:175], a[76:79], v[28:31]\n"
"v_mfma_f32_16x16x32_bf16 v[28:31], a[176:179], a[80:83], v[28:31]\n"
"s_nop 7\n"
"v_sub_f32 v28, v28, v5\n"
"v_sub_f32 v29, v29, v5\n"
"v_sub_f32 v30, v30, v5\n"
"v_sub_f32 v31, v31, v5\n"
"v_exp_f32 v28, v28\n"
"v_exp_f32 v29, v29\n"
"v_exp_f32 v30, v30\n"
"v_exp_f32 v31, v31\n"
"v_cvt_pk_bf16_f32 v162, v28, v29\n"
"v_cvt_pk_bf16_f32 v163, v30, v31\n"
"v_mfma_f32_16x16x32_bf16 v[36:39], a[164:167], a[84:87], 0\n"
"v_mfma_f32_16x16x32_bf16 v[36:39], a[168:171], a[88:91], v[36:39]\n"
"v_mfma_f32_16x16x32_bf16 v[36:39], a[172:175], a[92:95], v[36:39]\n"
"v_mfma_f32_16x16x32_bf16 v[36:39], a[176:179], a[96:99], v[36:39]\n"
"s_nop 7\n"
"v_sub_f32 v36, v36, v6\n"
"v_sub_f32 v37, v37, v6\n"
"v_sub_f32 v38, v38, v6\n"
"v_sub_f32 v39, v39, v6\n"
"v_exp_f32 v36, v36\n"
"v_exp_f32 v37, v37\n"
"v_exp_f32 v38, v38\n"
"v_exp_f32 v39, v39\n"
"v_cvt_pk_bf16_f32 v166, v36, v37\n"
"v_cvt_pk_bf16_f32 v167, v38, v39\n"
"v_mfma_f32_16x16x32_bf16 v[20:23], a[164:167], a[100:103], 0\n"
"v_mfma_f32_16x16x32_bf16 v[20:23], a[168:171], a[104:107], v[20:23]\n"
"v_mfma_f32_16x16x32_bf16 v[20:23], a[172:175], a[108:111], v[20:23]\n"
"v_mfma_f32_16x16x32_bf16 v[20:23], a[176:179], a[112:115], v[20:23]\n"
"s_nop 7\n"
"v_sub_f32 v20, v20, v7\n"
"v_sub_f32 v21, v21, v7\n"
"v_sub_f32 v22, v22, v7\n"
"v_sub_f32 v23, v23, v7\n"
"v_exp_f32 v20, v20\n"
"v_exp_f32 v21, v21\n"
"v_exp_f32 v22, v22\n"
"v_exp_f32 v23, v23\n"
"v_cvt_pk_bf16_f32 v170, v20, v21\n"
"v_cvt_pk_bf16_f32 v171, v22, v23\n"
"s_waitcnt lgkmcnt(0)\n"
"v_mfma_f32_16x16x32_bf16 a[20:23], a[196:199], v[156:159], a[20:23]\n"
"v_mfma_f32_16x16x32_bf16 a[36:39], a[200:203], v[156:159], a[36:39]\n"
"v_mfma_f32_16x16x32_bf16 a[4:7], a[0:3], v[156:159], a[4:7]\n"
"v_mfma_f32_16x16x32_bf16 a[24:27], a[196:199], v[160:163], a[24:27]\n"
"v_mfma_f32_16x16x32_bf16 a[40:43], a[200:203], v[160:163], a[40:43]\n"
"v_mfma_f32_16x16x32_bf16 a[8:11], a[0:3], v[160:163], a[8:11]\n"
"v_mfma_f32_16x16x32_bf16 a[28:31], a[196:199], v[164:167], a[28:31]\n"
"v_mfma_f32_16x16x32_bf16 a[44:47], a[200:203], v[164:167], a[44:47]\n"
"v_mfma_f32_16x16x32_bf16 a[12:15], a[0:3], v[164:167], a[12:15]\n"
"v_mfma_f32_16x16x32_bf16 a[32:35], a[196:199], v[168:171], a[32:35]\n"
"v_mfma_f32_16x16x32_bf16 a[48:51], a[200:203], v[168:171], a[48:51]\n"
"v_mfma_f32_16x16x32_bf16 a[16:19], a[0:3], v[168:171], a[16:19]\n"
"ds_read_b64_tr_b16 a[196:197], v8 offset:40960\n"
"ds_read_b64_tr_b16 a[198:199], v8 offset:43008\n"
"ds_read_b64_tr_b16 a[200:201], v9 offset:40960\n"
"ds_read_b64_tr_b16 a[202:203], v9 offset:43008\n"
"s_xor_b32 s95, s17, 0\n"
"s_lshl_b32 s95, s95, 6\n"
"s_add_u32 s95, s95, 81920\n"
"buffer_load_dwordx4 a[164:167], v1, s[52:55], s95 offen\n"
"s_xor_b32 s95, s17, 1\n"
"s_lshl_b32 s95, s95, 6\n"
"s_add_u32 s95, s95, 81920\n"
"buffer_load_dwordx4 a[168:171], v1, s[52:55], s95 offen\n"
"s_xor_b32 s95, s17, 2\n"
"s_lshl_b32 s95, s95, 6\n"
"s_add_u32 s95, s95, 81920\n"
"buffer_load_dwordx4 a[172:175], v1, s[52:55], s95 offen\n"
"s_xor_b32 s95, s17, 3\n"
"s_lshl_b32 s95, s95, 6\n"
"s_add_u32 s95, s95, 81920\n"
"buffer_load_dwordx4 a[176:179], v1, s[52:55], s95 offen\n"
"s_waitcnt vmcnt(3)\n"
"v_mfma_f32_16x16x32_bf16 v[20:23], a[164:167], a[52:55], 0\n"
"s_waitcnt vmcnt(2)\n"
"v_mfma_f32_16x16x32_bf16 v[20:23], a[168:171], a[56:59], v[20:23]\n"
"s_waitcnt vmcnt(1)\n"
"v_mfma_f32_16x16x32_bf16 v[20:23], a[172:175], a[60:63], v[20:23]\n"
"s_waitcnt vmcnt(0)\n"
"v_mfma_f32_16x16x32_bf16 v[20:23], a[176:179], a[64:67], v[20:23]\n"
"s_nop 7\n"
"v_sub_f32 v20, v20, v4\n"
"v_sub_f32 v21, v21, v4\n"
"v_sub_f32 v22, v22, v4\n"
"v_sub_f32 v23, v23, v4\n"
"v_exp_f32 v20, v20\n"
"v_exp_f32 v21, v21\n"
"v_exp_f32 v22, v22\n"
"v_exp_f32 v23, v23\n"
"v_cvt_pk_bf16_f32 v172, v20, v21\n"
"v_cvt_pk_bf16_f32 v173, v22, v23\n"
"v_mfma_f32_16x16x32_bf16 v[28:31], a[164:167], a[68:71], 0\n"
"v_mfma_f32_16x16x32_bf16 v[28:31], a[168:171], a[72:75], v[28:31]\n"
"v_mfma_f32_16x16x32_bf16 v[28:31], a[172:175], a[76:79], v[28:31]\n"
"v_mfma_f32_16x16x32_bf16 v[28:31], a[176:179], a[80:83], v[28:31]\n"
"s_nop 7\n"
"v_sub_f32 v28, v28, v5\n"
"v_sub_f32 v29, v29, v5\n"
"v_sub_f32 v30, v30, v5\n"
"v_sub_f32 v31, v31, v5\n"
"v_exp_f32 v28, v28\n"
"v_exp_f32 v29, v29\n"
"v_exp_f32 v30, v30\n"
"v_exp_f32 v31, v31\n"
"v_cvt_pk_bf16_f32 v176, v28, v29\n"
"v_cvt_pk_bf16_f32 v177, v30, v31\n"
"v_mfma_f32_16x16x32_bf16 v[36:39], a[164:167], a[84:87], 0\n"
"v_mfma_f32_16x16x32_bf16 v[36:39], a[168:171], a[88:91], v[36:39]\n"
"v_mfma_f32_16x16x32_bf16 v[36:39], a[172:175], a[92:95], v[36:39]\n"
"v_mfma_f32_16x16x32_bf16 v[36:39], a[176:179], a[96:99], v[36:39]\n"
"s_nop 7\n"
"v_sub_f32 v36, v36, v6\n"
"v_sub_f32 v37, v37, v6\n"
"v_sub_f32 v38, v38, v6\n"
"v_sub_f32 v39, v39, v6\n"
"v_exp_f32 v36, v36\n"
"v_exp_f32 v37, v37\n"
"v_exp_f32 v38, v38\n"
"v_exp_f32 v39, v39\n"
"v_cvt_pk_bf16_f32 v180, v36, v37\n"
"v_cvt_pk_bf16_f32 v181, v38, v39\n"
"v_mfma_f32_16x16x32_bf16 v[20:23], a[164:167], a[100:103], 0\n"
"v_mfma_f32_16x16x32_bf16 v[20:23], a[168:171], a[104:107], v[20:23]\n"
"v_mfma_f32_16x16x32_bf16 v[20:23], a[172:175], a[108:111], v[20:23]\n"
"v_mfma_f32_16x16x32_bf16 v[20:23], a[176:179], a[112:115], v[20:23]\n"
"s_nop 7\n"
"v_sub_f32 v20, v20, v7\n"
"v_sub_f32 v21, v21, v7\n"
"v_sub_f32 v22, v22, v7\n"
"v_sub_f32 v23, v23, v7\n"
"v_exp_f32 v20, v20\n"
"v_exp_f32 v21, v21\n"
"v_exp_f32 v22, v22\n"
"v_exp_f32 v23, v23\n"
"v_cvt_pk_bf16_f32 v184, v20, v21\n"
"v_cvt_pk_bf16_f32 v185, v22, v23\n"
"s_xor_b32 s95, s17, 0\n"
"s_lshl_b32 s95, s95, 6\n"
"s_add_u32 s95, s95, 86016\n"
"buffer_load_dwordx4 a[164:167], v1, s[52:55], s95 offen\n"
"s_xor_b32 s95, s17, 1\n"
"s_lshl_b32 s95, s95, 6\n"
"s_add_u32 s95, s95, 86016\n"
"buffer_load_dwordx4 a[168:171], v1, s[52:55], s95 offen\n"
"s_xor_b32 s95, s17, 2\n"
"s_lshl_b32 s95, s95, 6\n"
"s_add_u32 s95, s95, 86016\n"
"buffer_load_dwordx4 a[172:175], v1, s[52:55], s95 offen\n"
"s_xor_b32 s95, s17, 3\n"
"s_lshl_b32 s95, s95, 6\n"
"s_add_u32 s95, s95, 86016\n"
"buffer_load_dwordx4 a[176:179], v1, s[52:55], s95 offen\n"
"s_waitcnt vmcnt(3)\n"
"v_mfma_f32_16x16x32_bf16 v[20:23], a[164:167], a[52:55], 0\n"
"s_waitcnt vmcnt(2)\n"
"v_mfma_f32_16x16x32_bf16 v[20:23], a[168:171], a[56:59], v[20:23]\n"
"s_waitcnt vmcnt(1)\n"
"v_mfma_f32_16x16x32_bf16 v[20:23], a[172:175], a[60:63], v[20:23]\n"
"s_waitcnt vmcnt(0)\n"
"v_mfma_f32_16x16x32_bf16 v[20:23], a[176:179], a[64:67], v[20:23]\n"
"s_nop 7\n"
"v_sub_f32 v20, v20, v4\n"
"v_sub_f32 v21, v21, v4\n"
"v_sub_f32 v22, v22, v4\n"
"v_sub_f32 v23, v23, v4\n"
"v_exp_f32 v20, v20\n"
"v_exp_f32 v21, v21\n"
"v_exp_f32 v22, v22\n"
"v_exp_f32 v23, v23\n"
"v_cvt_pk_bf16_f32 v174, v20, v21\n"
"v_cvt_pk_bf16_f32 v175, v22, v23\n"
"v_mfma_f32_16x16x32_bf16 v[28:31], a[164:167], a[68:71], 0\n"
"v_mfma_f32_16x16x32_bf16 v[28:31], a[168:171], a[72:75], v[28:31]\n"
"v_mfma_f32_16x16x32_bf16 v[28:31], a[172:175], a[76:79], v[28:31]\n"
"v_mfma_f32_16x16x32_bf16 v[28:31], a[176:179], a[80:83], v[28:31]\n"
"s_nop 7\n"
"v_sub_f32 v28, v28, v5\n"
"v_sub_f32 v29, v29, v5\n"
"v_sub_f32 v30, v30, v5\n"
"v_sub_f32 v31, v31, v5\n"
"v_exp_f32 v28, v28\n"
"v_exp_f32 v29, v29\n"
"v_exp_f32 v30, v30\n"
"v_exp_f32 v31, v31\n"
"v_cvt_pk_bf16_f32 v178, v28, v29\n"
"v_cvt_pk_bf16_f32 v179, v30, v31\n"
"v_mfma_f32_16x16x32_bf16 v[36:39], a[164:167], a[84:87], 0\n"
"v_mfma_f32_16x16x32_bf16 v[36:39], a[168:171], a[88:91], v[36:39]\n"
"v_mfma_f32_16x16x32_bf16 v[36:39], a[172:175], a[92:95], v[36:39]\n"
"v_mfma_f32_16x16x32_bf16 v[36:39], a[176:179], a[96:99], v[36:39]\n"
"s_nop 7\n"
"v_sub_f32 v36, v36, v6\n"
"v_sub_f32 v37, v37, v6\n"
"v_sub_f32 v38, v38, v6\n"
"v_sub_f32 v39, v39, v6\n"
"v_exp_f32 v36, v36\n"
"v_exp_f32 v37, v37\n"
"v_exp_f32 v38, v38\n"
"v_exp_f32 v39, v39\n"
"v_cvt_pk_bf16_f32 v182, v36, v37\n"
"v_cvt_pk_bf16_f32 v183, v38, v39\n"
"v_mfma_f32_16x16x32_bf16 v[20:23], a[164:167], a[100:103], 0\n"
"v_mfma_f32_16x16x32_bf16 v[20:23], a[168:171], a[104:107], v[20:23]\n"
"v_mfma_f32_16x16x32_bf16 v[20:23], a[172:175], a[108:111], v[20:23]\n"
"v_mfma_f32_16x16x32_bf16 v[20:23], a[176:179], a[112:115], v[20:23]\n"
"s_nop 7\n"
"v_sub_f32 v20, v20, v7\n"
"v_sub_f32 v21, v21, v7\n"
"v_sub_f32 v22, v22, v7\n"
"v_sub_f32 v23, v23, v7\n"
"v_exp_f32 v20, v20\n"
"v_exp_f32 v21, v21\n"
"v_exp_f32 v22, v22\n"
"v_exp_f32 v23, v23\n"
"v_cvt_pk_bf16_f32 v186, v20, v21\n"
"v_cvt_pk_bf16_f32 v187, v22, v23\n"
"s_waitcnt lgkmcnt(0)\n"
"v_mfma_f32_16x16x32_bf16 a[20:23], a[196:199], v[172:175], a[20:23]\n"
"v_mfma_f32_16x16x32_bf16 a[36:39], a[200:203], v[172:175], a[36:39]\n"
"v_mfma_f32_16x16x32_bf16 a[4:7], a[0:3], v[172:175], a[4:7]\n"
"v_mfma_f32_16x16x32_bf16 a[24:27], a[196:199], v[176:179], a[24:27]\n"
"v_mfma_f32_16x16x32_bf16 a[40:43], a[200:203], v[176:179], a[40:43]\n"
"v_mfma_f32_16x16x32_bf16 a[8:11], a[0:3], v[176:179], a[8:11]\n"
"v_mfma_f32_16x16x32_bf16 a[28:31], a[196:199], v[180:183], a[28:31]\n"
"v_mfma_f32_16x16x32_bf16 a[44:47], a[200:203], v[180:183], a[44:47]\n"
"v_mfma_f32_16x16x32_bf16 a[12:15], a[0:3], v[180:183], a[12:15]\n"
"v_mfma_f32_16x16x32_bf16 a[32:35], a[196:199], v[184:187], a[32:35]\n"
"v_mfma_f32_16x16x32_bf16 a[48:51], a[200:203], v[184:187], a[48:51]\n"
"v_mfma_f32_16x16x32_bf16 a[16:19], a[0:3], v[184:187], a[16:19]\n"
"ds_read_b64_tr_b16 a[196:197], v8 offset:45056\n"
"ds_read_b64_tr_b16 a[198:199], v8 offset:47104\n"
"ds_read_b64_tr_b16 a[200:201], v9 offset:45056\n"
"ds_read_b64_tr_b16 a[202:203], v9 offset:47104\n"
"s_xor_b32 s95, s17, 0\n"
"s_lshl_b32 s95, s95, 6\n"
"s_add_u32 s95, s95, 90112\n"
"buffer_load_dwordx4 a[164:167], v1, s[52:55], s95 offen\n"
"s_xor_b32 s95, s17, 1\n"
"s_lshl_b32 s95, s95, 6\n"
"s_add_u32 s95, s95, 90112\n"
"buffer_load_dwordx4 a[168:171], v1, s[52:55], s95 offen\n"
"s_xor_b32 s95, s17, 2\n"
"s_lshl_b32 s95, s95, 6\n"
"s_add_u32 s95, s95, 90112\n"
"buffer_load_dwordx4 a[172:175], v1, s[52:55], s95 offen\n"
"s_xor_b32 s95, s17, 3\n"
"s_lshl_b32 s95, s95, 6\n"
"s_add_u32 s95, s95, 90112\n"
"buffer_load_dwordx4 a[176:179], v1, s[52:55], s95 offen\n"
"s_waitcnt vmcnt(3)\n"
"v_mfma_f32_16x16x32_bf16 v[20:23], a[164:167], a[52:55], 0\n"
"s_waitcnt vmcnt(2)\n"
"v_mfma_f32_16x16x32_bf16 v[20:23], a[168:171], a[56:59], v[20:23]\n"
"s_waitcnt vmcnt(1)\n"
"v_mfma_f32_16x16x32_bf16 v[20:23], a[172:175], a[60:63], v[20:23]\n"
"s_waitcnt vmcnt(0)\n"
"v_mfma_f32_16x16x32_bf16 v[20:23], a[176:179], a[64:67], v[20:23]\n"
"s_nop 7\n"
"v_sub_f32 v20, v20, v4\n"
"v_sub_f32 v21, v21, v4\n"
"v_sub_f32 v22, v22, v4\n"
"v_sub_f32 v23, v23, v4\n"
"v_exp_f32 v20, v20\n"
"v_exp_f32 v21, v21\n"
"v_exp_f32 v22, v22\n"
"v_exp_f32 v23, v23\n"
"v_cvt_pk_bf16_f32 v188, v20, v21\n"
"v_cvt_pk_bf16_f32 v189, v22, v23\n"
"v_mfma_f32_16x16x32_bf16 v[28:31], a[164:167], a[68:71], 0\n"
"v_mfma_f32_16x16x32_bf16 v[28:31], a[168:171], a[72:75], v[28:31]\n"
"v_mfma_f32_16x16x32_bf16 v[28:31], a[172:175], a[76:79], v[28:31]\n"
"v_mfma_f32_16x16x32_bf16 v[28:31], a[176:179], a[80:83], v[28:31]\n"
"s_nop 7\n"
"v_sub_f32 v28, v28, v5\n"
"v_sub_f32 v29, v29, v5\n"
"v_sub_f32 v30, v30, v5\n"
"v_sub_f32 v31, v31, v5\n"
"v_exp_f32 v28, v28\n"
"v_exp_f32 v29, v29\n"
"v_exp_f32 v30, v30\n"
"v_exp_f32 v31, v31\n"
"v_cvt_pk_bf16_f32 v192, v28, v29\n"
"v_cvt_pk_bf16_f32 v193, v30, v31\n"
"v_mfma_f32_16x16x32_bf16 v[36:39], a[164:167], a[84:87], 0\n"
"v_mfma_f32_16x16x32_bf16 v[36:39], a[168:171], a[88:91], v[36:39]\n"
"v_mfma_f32_16x16x32_bf16 v[36:39], a[172:175], a[92:95], v[36:39]\n"
"v_mfma_f32_16x16x32_bf16 v[36:39], a[176:179], a[96:99], v[36:39]\n"
"s_nop 7\n"
"v_sub_f32 v36, v36, v6\n"
"v_sub_f32 v37, v37, v6\n"
"v_sub_f32 v38, v38, v6\n"
"v_sub_f32 v39, v39, v6\n"
"v_exp_f32 v36, v36\n"
"v_exp_f32 v37, v37\n"
"v_exp_f32 v38, v38\n"
"v_exp_f32 v39, v39\n"
"v_cvt_pk_bf16_f32 v196, v36, v37\n"
"v_cvt_pk_bf16_f32 v197, v38, v39\n"
"v_mfma_f32_16x16x32_bf16 v[20:23], a[164:167], a[100:103], 0\n"
"v_mfma_f32_16x16x32_bf16 v[20:23], a[168:171], a[104:107], v[20:23]\n"
"v_mfma_f32_16x16x32_bf16 v[20:23], a[172:175], a[108:111], v[20:23]\n"
"v_mfma_f32_16x16x32_bf16 v[20:23], a[176:179], a[112:115], v[20:23]\n"
"s_nop 7\n"
"v_sub_f32 v20, v20, v7\n"
"v_sub_f32 v21, v21, v7\n"
"v_sub_f32 v22, v22, v7\n"
"v_sub_f32 v23, v23, v7\n"
"v_exp_f32 v20, v20\n"
"v_exp_f32 v21, v21\n"
"v_exp_f32 v22, v22\n"
"v_exp_f32 v23, v23\n"
"v_cvt_pk_bf16_f32 v200, v20, v21\n"
"v_cvt_pk_bf16_f32 v201, v22, v23\n"
"s_xor_b32 s95, s17, 0\n"
"s_lshl_b32 s95, s95, 6\n"
"s_add_u32 s95, s95, 94208\n"
"buffer_load_dwordx4 a[164:167], v1, s[52:55], s95 offen\n"
"s_xor_b32 s95, s17, 1\n"
"s_lshl_b32 s95, s95, 6\n"
"s_add_u32 s95, s95, 94208\n"
"buffer_load_dwordx4 a[168:171], v1, s[52:55], s95 offen\n"
"s_xor_b32 s95, s17, 2\n"
"s_lshl_b32 s95, s95, 6\n"
"s_add_u32 s95, s95, 94208\n"
"buffer_load_dwordx4 a[172:175], v1, s[52:55], s95 offen\n"
"s_xor_b32 s95, s17, 3\n"
"s_lshl_b32 s95, s95, 6\n"
"s_add_u32 s95, s95, 94208\n"
"buffer_load_dwordx4 a[176:179], v1, s[52:55], s95 offen\n"
"s_waitcnt vmcnt(3)\n"
"v_mfma_f32_16x16x32_bf16 v[20:23], a[164:167], a[52:55], 0\n"
"s_waitcnt vmcnt(2)\n"
"v_mfma_f32_16x16x32_bf16 v[20:23], a[168:171], a[56:59], v[20:23]\n"
"s_waitcnt vmcnt(1)\n"
"v_mfma_f32_16x16x32_bf16 v[20:23], a[172:175], a[60:63], v[20:23]\n"
"s_waitcnt vmcnt(0)\n"
"v_mfma_f32_16x16x32_bf16 v[20:23], a[176:179], a[64:67], v[20:23]\n"
"s_nop 7\n"
"v_sub_f32 v20, v20, v4\n"
"v_sub_f32 v21, v21, v4\n"
"v_sub_f32 v22, v22, v4\n"
"v_sub_f32 v23, v23, v4\n"
"v_exp_f32 v20, v20\n"
"v_exp_f32 v21, v21\n"
"v_exp_f32 v22, v22\n"
"v_exp_f32 v23, v23\n"
"v_cvt_pk_bf16_f32 v190, v20, v21\n"
"v_cvt_pk_bf16_f32 v191, v22, v23\n"
"v_mfma_f32_16x16x32_bf16 v[28:31], a[164:167], a[68:71], 0\n"
"v_mfma_f32_16x16x32_bf16 v[28:31], a[168:171], a[72:75], v[28:31]\n"
"v_mfma_f32_16x16x32_bf16 v[28:31], a[172:175], a[76:79], v[28:31]\n"
"v_mfma_f32_16x16x32_bf16 v[28:31], a[176:179], a[80:83], v[28:31]\n"
"s_nop 7\n"
"v_sub_f32 v28, v28, v5\n"
"v_sub_f32 v29, v29, v5\n"
"v_sub_f32 v30, v30, v5\n"
"v_sub_f32 v31, v31, v5\n"
"v_exp_f32 v28, v28\n"
"v_exp_f32 v29, v29\n"
"v_exp_f32 v30, v30\n"
"v_exp_f32 v31, v31\n"
"v_cvt_pk_bf16_f32 v194, v28, v29\n"
"v_cvt_pk_bf16_f32 v195, v30, v31\n"
"v_mfma_f32_16x16x32_bf16 v[36:39], a[164:167], a[84:87], 0\n"
"v_mfma_f32_16x16x32_bf16 v[36:39], a[168:171], a[88:91], v[36:39]\n"
"v_mfma_f32_16x16x32_bf16 v[36:39], a[172:175], a[92:95], v[36:39]\n"
"v_mfma_f32_16x16x32_bf16 v[36:39], a[176:179], a[96:99], v[36:39]\n"
"s_nop 7\n"
"v_sub_f32 v36, v36, v6\n"
"v_sub_f32 v37, v37, v6\n"
"v_sub_f32 v38, v38, v6\n"
"v_sub_f32 v39, v39, v6\n"
"v_exp_f32 v36, v36\n"
"v_exp_f32 v37, v37\n"
"v_exp_f32 v38, v38\n"
"v_exp_f32 v39, v39\n"
"v_cvt_pk_bf16_f32 v198, v36, v37\n"
"v_cvt_pk_bf16_f32 v199, v38, v39\n"
"v_mfma_f32_16x16x32_bf16 v[20:23], a[164:167], a[100:103], 0\n"
"v_mfma_f32_16x16x32_bf16 v[20:23], a[168:171], a[104:107], v[20:23]\n"
"v_mfma_f32_16x16x32_bf16 v[20:23], a[172:175], a[108:111], v[20:23]\n"
"v_mfma_f32_16x16x32_bf16 v[20:23], a[176:179], a[112:115], v[20:23]\n"
"s_nop 7\n"
"v_sub_f32 v20, v20, v7\n"
"v_sub_f32 v21, v21, v7\n"
"v_sub_f32 v22, v22, v7\n"
"v_sub_f32 v23, v23, v7\n"
"v_exp_f32 v20, v20\n"
"v_exp_f32 v21, v21\n"
"v_exp_f32 v22, v22\n"
"v_exp_f32 v23, v23\n"
"v_cvt_pk_bf16_f32 v202, v20, v21\n"
"v_cvt_pk_bf16_f32 v203, v22, v23\n"
"s_waitcnt lgkmcnt(0)\n"
"v_mfma_f32_16x16x32_bf16 a[20:23], a[196:199], v[188:191], a[20:23]\n"
"v_mfma_f32_16x16x32_bf16 a[36:39], a[200:203], v[188:191], a[36:39]\n"
"v_mfma_f32_16x16x32_bf16 a[4:7], a[0:3], v[188:191], a[4:7]\n"
"v_mfma_f32_16x16x32_bf16 a[24:27], a[196:199], v[192:195], a[24:27]\n"
"v_mfma_f32_16x16x32_bf16 a[40:43], a[200:203], v[192:195], a[40:43]\n"
"v_mfma_f32_16x16x32_bf16 a[8:11], a[0:3], v[192:195], a[8:11]\n"
"v_mfma_f32_16x16x32_bf16 a[28:31], a[196:199], v[196:199], a[28:31]\n"
"v_mfma_f32_16x16x32_bf16 a[44:47], a[200:203], v[196:199], a[44:47]\n"
"v_mfma_f32_16x16x32_bf16 a[12:15], a[0:3], v[196:199], a[12:15]\n"
"v_mfma_f32_16x16x32_bf16 a[32:35], a[196:199], v[200:203], a[32:35]\n"
"v_mfma_f32_16x16x32_bf16 a[48:51], a[200:203], v[200:203], a[48:51]\n"
"v_mfma_f32_16x16x32_bf16 a[16:19], a[0:3], v[200:203], a[16:19]\n"
"ds_read_b64_tr_b16 a[196:197], v8 offset:49152\n"
"ds_read_b64_tr_b16 a[198:199], v8 offset:51200\n"
"ds_read_b64_tr_b16 a[200:201], v9 offset:49152\n"
"ds_read_b64_tr_b16 a[202:203], v9 offset:51200\n"
"s_xor_b32 s95, s17, 0\n"
"s_lshl_b32 s95, s95, 6\n"
"s_add_u32 s95, s95, 98304\n"
"buffer_load_dwordx4 a[164:167], v1, s[52:55], s95 offen\n"
"s_xor_b32 s95, s17, 1\n"
"s_lshl_b32 s95, s95, 6\n"
"s_add_u32 s95, s95, 98304\n"
"buffer_load_dwordx4 a[168:171], v1, s[52:55], s95 offen\n"
"s_xor_b32 s95, s17, 2\n"
"s_lshl_b32 s95, s95, 6\n"
"s_add_u32 s95, s95, 98304\n"
"buffer_load_dwordx4 a[172:175], v1, s[52:55], s95 offen\n"
"s_xor_b32 s95, s17, 3\n"
"s_lshl_b32 s95, s95, 6\n"
"s_add_u32 s95, s95, 98304\n"
"buffer_load_dwordx4 a[176:179], v1, s[52:55], s95 offen\n"
"s_waitcnt vmcnt(3)\n"
"v_mfma_f32_16x16x32_bf16 v[20:23], a[164:167], a[52:55], 0\n"
"s_waitcnt vmcnt(2)\n"
"v_mfma_f32_16x16x32_bf16 v[20:23], a[168:171], a[56:59], v[20:23]\n"
"s_waitcnt vmcnt(1)\n"
"v_mfma_f32_16x16x32_bf16 v[20:23], a[172:175], a[60:63], v[20:23]\n"
"s_waitcnt vmcnt(0)\n"
"v_mfma_f32_16x16x32_bf16 v[20:23], a[176:179], a[64:67], v[20:23]\n"
"s_nop 7\n"
"v_sub_f32 v20, v20, v4\n"
"v_sub_f32 v21, v21, v4\n"
"v_sub_f32 v22, v22, v4\n"
"v_sub_f32 v23, v23, v4\n"
"v_exp_f32 v20, v20\n"
"v_exp_f32 v21, v21\n"
"v_exp_f32 v22, v22\n"
"v_exp_f32 v23, v23\n"
"v_cvt_pk_bf16_f32 v204, v20, v21\n"
"v_cvt_pk_bf16_f32 v205, v22, v23\n"
"v_mfma_f32_16x16x32_bf16 v[28:31], a[164:167], a[68:71], 0\n"
"v_mfma_f32_16x16x32_bf16 v[28:31], a[168:171], a[72:75], v[28:31]\n"
"v_mfma_f32_16x16x32_bf16 v[28:31], a[172:175], a[76:79], v[28:31]\n"
"v_mfma_f32_16x16x32_bf16 v[28:31], a[176:179], a[80:83], v[28:31]\n"
"s_nop 7\n"
"v_sub_f32 v28, v28, v5\n"
"v_sub_f32 v29, v29, v5\n"
"v_sub_f32 v30, v30, v5\n"
"v_sub_f32 v31, v31, v5\n"
"v_exp_f32 v28, v28\n"
"v_exp_f32 v29, v29\n"
"v_exp_f32 v30, v30\n"
"v_exp_f32 v31, v31\n"
"v_cvt_pk_bf16_f32 v208, v28, v29\n"
"v_cvt_pk_bf16_f32 v209, v30, v31\n"
"v_mfma_f32_16x16x32_bf16 v[36:39], a[164:167], a[84:87], 0\n"
"v_mfma_f32_16x16x32_bf16 v[36:39], a[168:171], a[88:91], v[36:39]\n"
"v_mfma_f32_16x16x32_bf16 v[36:39], a[172:175], a[92:95], v[36:39]\n"
"v_mfma_f32_16x16x32_bf16 v[36:39], a[176:179], a[96:99], v[36:39]\n"
"s_nop 7\n"
"v_sub_f32 v36, v36, v6\n"
"v_sub_f32 v37, v37, v6\n"
"v_sub_f32 v38, v38, v6\n"
"v_sub_f32 v39, v39, v6\n"
"v_exp_f32 v36, v36\n"
"v_exp_f32 v37, v37\n"
"v_exp_f32 v38, v38\n"
"v_exp_f32 v39, v39\n"
"v_cvt_pk_bf16_f32 v212, v36, v37\n"
"v_cvt_pk_bf16_f32 v213, v38, v39\n"
"v_mfma_f32_16x16x32_bf16 v[20:23], a[164:167], a[100:103], 0\n"
"v_mfma_f32_16x16x32_bf16 v[20:23], a[168:171], a[104:107], v[20:23]\n"
"v_mfma_f32_16x16x32_bf16 v[20:23], a[172:175], a[108:111], v[20:23]\n"
"v_mfma_f32_16x16x32_bf16 v[20:23], a[176:179], a[112:115], v[20:23]\n"
"s_nop 7\n"
"v_sub_f32 v20, v20, v7\n"
"v_sub_f32 v21, v21, v7\n"
"v_sub_f32 v22, v22, v7\n"
"v_sub_f32 v23, v23, v7\n"
"v_exp_f32 v20, v20\n"
"v_exp_f32 v21, v21\n"
"v_exp_f32 v22, v22\n"
"v_exp_f32 v23, v23\n"
"v_cvt_pk_bf16_f32 v216, v20, v21\n"
"v_cvt_pk_bf16_f32 v217, v22, v23\n"
"s_xor_b32 s95, s17, 0\n"
"s_lshl_b32 s95, s95, 6\n"
"s_add_u32 s95, s95, 102400\n"
"buffer_load_dwordx4 a[164:167], v1, s[52:55], s95 offen\n"
"s_xor_b32 s95, s17, 1\n"
"s_lshl_b32 s95, s95, 6\n"
"s_add_u32 s95, s95, 102400\n"
"buffer_load_dwordx4 a[168:171], v1, s[52:55], s95 offen\n"
"s_xor_b32 s95, s17, 2\n"
"s_lshl_b32 s95, s95, 6\n"
"s_add_u32 s95, s95, 102400\n"
"buffer_load_dwordx4 a[172:175], v1, s[52:55], s95 offen\n"
"s_xor_b32 s95, s17, 3\n"
"s_lshl_b32 s95, s95, 6\n"
"s_add_u32 s95, s95, 102400\n"
"buffer_load_dwordx4 a[176:179], v1, s[52:55], s95 offen\n"
"s_waitcnt vmcnt(3)\n"
"v_mfma_f32_16x16x32_bf16 v[20:23], a[164:167], a[52:55], 0\n"
"s_waitcnt vmcnt(2)\n"
"v_mfma_f32_16x16x32_bf16 v[20:23], a[168:171], a[56:59], v[20:23]\n"
"s_waitcnt vmcnt(1)\n"
"v_mfma_f32_16x16x32_bf16 v[20:23], a[172:175], a[60:63], v[20:23]\n"
"s_waitcnt vmcnt(0)\n"
"v_mfma_f32_16x16x32_bf16 v[20:23], a[176:179], a[64:67], v[20:23]\n"
"s_nop 7\n"
"v_sub_f32 v20, v20, v4\n"
"v_sub_f32 v21, v21, v4\n"
"v_sub_f32 v22, v22, v4\n"
"v_sub_f32 v23, v23, v4\n"
"v_exp_f32 v20, v20\n"
"v_exp_f32 v21, v21\n"
"v_exp_f32 v22, v22\n"
"v_exp_f32 v23, v23\n"
"v_cvt_pk_bf16_f32 v206, v20, v21\n"
"v_cvt_pk_bf16_f32 v207, v22, v23\n"
"v_mfma_f32_16x16x32_bf16 v[28:31], a[164:167], a[68:71], 0\n"
"v_mfma_f32_16x16x32_bf16 v[28:31], a[168:171], a[72:75], v[28:31]\n"
"v_mfma_f32_16x16x32_bf16 v[28:31], a[172:175], a[76:79], v[28:31]\n"
"v_mfma_f32_16x16x32_bf16 v[28:31], a[176:179], a[80:83], v[28:31]\n"
"s_nop 7\n"
"v_sub_f32 v28, v28, v5\n"
"v_sub_f32 v29, v29, v5\n"
"v_sub_f32 v30, v30, v5\n"
"v_sub_f32 v31, v31, v5\n"
"v_exp_f32 v28, v28\n"
"v_exp_f32 v29, v29\n"
"v_exp_f32 v30, v30\n"
"v_exp_f32 v31, v31\n"
"v_cvt_pk_bf16_f32 v210, v28, v29\n"
"v_cvt_pk_bf16_f32 v211, v30, v31\n"
"v_mfma_f32_16x16x32_bf16 v[36:39], a[164:167], a[84:87], 0\n"
"v_mfma_f32_16x16x32_bf16 v[36:39], a[168:171], a[88:91], v[36:39]\n"
"v_mfma_f32_16x16x32_bf16 v[36:39], a[172:175], a[92:95], v[36:39]\n"
"v_mfma_f32_16x16x32_bf16 v[36:39], a[176:179], a[96:99], v[36:39]\n"
"s_nop 7\n"
"v_sub_f32 v36, v36, v6\n"
"v_sub_f32 v37, v37, v6\n"
"v_sub_f32 v38, v38, v6\n"
"v_sub_f32 v39, v39, v6\n"
"v_exp_f32 v36, v36\n"
"v_exp_f32 v37, v37\n"
"v_exp_f32 v38, v38\n"
"v_exp_f32 v39, v39\n"
"v_cvt_pk_bf16_f32 v214, v36, v37\n"
"v_cvt_pk_bf16_f32 v215, v38, v39\n"
"v_mfma_f32_16x16x32_bf16 v[20:23], a[164:167], a[100:103], 0\n"
"v_mfma_f32_16x16x32_bf16 v[20:23], a[168:171], a[104:107], v[20:23]\n"
"v_mfma_f32_16x16x32_bf16 v[20:23], a[172:175], a[108:111], v[20:23]\n"
"v_mfma_f32_16x16x32_bf16 v[20:23], a[176:179], a[112:115], v[20:23]\n"
"s_nop 7\n"
"v_sub_f32 v20, v20, v7\n"
"v_sub_f32 v21, v21, v7\n"
"v_sub_f32 v22, v22, v7\n"
"v_sub_f32 v23, v23, v7\n"
"v_exp_f32 v20, v20\n"
"v_exp_f32 v21, v21\n"
"v_exp_f32 v22, v22\n"
"v_exp_f32 v23, v23\n"
"v_cvt_pk_bf16_f32 v218, v20, v21\n"
"v_cvt_pk_bf16_f32 v219, v22, v23\n"
"s_waitcnt lgkmcnt(0)\n"
"v_mfma_f32_16x16x32_bf16 a[20:23], a[196:199], v[204:207], a[20:23]\n"
"v_mfma_f32_16x16x32_bf16 a[36:39], a[200:203], v[204:207], a[36:39]\n"
"v_mfma_f32_16x16x32_bf16 a[4:7], a[0:3], v[204:207], a[4:7]\n"
"v_mfma_f32_16x16x32_bf16 a[24:27], a[196:199], v[208:211], a[24:27]\n"
"v_mfma_f32_16x16x32_bf16 a[40:43], a[200:203], v[208:211], a[40:43]\n"
"v_mfma_f32_16x16x32_bf16 a[8:11], a[0:3], v[208:211], a[8:11]\n"
"v_mfma_f32_16x16x32_bf16 a[28:31], a[196:199], v[212:215], a[28:31]\n"
"v_mfma_f32_16x16x32_bf16 a[44:47], a[200:203], v[212:215], a[44:47]\n"
"v_mfma_f32_16x16x32_bf16 a[12:15], a[0:3], v[212:215], a[12:15]\n"
"v_mfma_f32_16x16x32_bf16 a[32:35], a[196:199], v[216:219], a[32:35]\n"
"v_mfma_f32_16x16x32_bf16 a[48:51], a[200:203], v[216:219], a[48:51]\n"
"v_mfma_f32_16x16x32_bf16 a[16:19], a[0:3], v[216:219], a[16:19]\n"
"ds_read_b64_tr_b16 a[196:197], v8 offset:53248\n"
"ds_read_b64_tr_b16 a[198:199], v8 offset:55296\n"
"ds_read_b64_tr_b16 a[200:201], v9 offset:53248\n"
"ds_read_b64_tr_b16 a[202:203], v9 offset:55296\n"
"s_xor_b32 s95, s17, 0\n"
"s_lshl_b32 s95, s95, 6\n"
"s_add_u32 s95, s95, 106496\n"
"buffer_load_dwordx4 a[164:167], v1, s[52:55], s95 offen\n"
"s_xor_b32 s95, s17, 1\n"
"s_lshl_b32 s95, s95, 6\n"
"s_add_u32 s95, s95, 106496\n"
"buffer_load_dwordx4 a[168:171], v1, s[52:55], s95 offen\n"
"s_xor_b32 s95, s17, 2\n"
"s_lshl_b32 s95, s95, 6\n"
"s_add_u32 s95, s95, 106496\n"
"buffer_load_dwordx4 a[172:175], v1, s[52:55], s95 offen\n"
"s_xor_b32 s95, s17, 3\n"
"s_lshl_b32 s95, s95, 6\n"
"s_add_u32 s95, s95, 106496\n"
"buffer_load_dwordx4 a[176:179], v1, s[52:55], s95 offen\n"
"s_waitcnt vmcnt(3)\n"
"v_mfma_f32_16x16x32_bf16 v[20:23], a[164:167], a[52:55], 0\n"
"s_waitcnt vmcnt(2)\n"
"v_mfma_f32_16x16x32_bf16 v[20:23], a[168:171], a[56:59], v[20:23]\n"
"s_waitcnt vmcnt(1)\n"
"v_mfma_f32_16x16x32_bf16 v[20:23], a[172:175], a[60:63], v[20:23]\n"
"s_waitcnt vmcnt(0)\n"
"v_mfma_f32_16x16x32_bf16 v[20:23], a[176:179], a[64:67], v[20:23]\n"
"s_nop 7\n"
"v_sub_f32 v20, v20, v4\n"
"v_sub_f32 v21, v21, v4\n"
"v_sub_f32 v22, v22, v4\n"
"v_sub_f32 v23, v23, v4\n"
"v_exp_f32 v20, v20\n"
"v_exp_f32 v21, v21\n"
"v_exp_f32 v22, v22\n"
"v_exp_f32 v23, v23\n"
"v_cvt_pk_bf16_f32 v220, v20, v21\n"
"v_cvt_pk_bf16_f32 v221, v22, v23\n"
"v_mfma_f32_16x16x32_bf16 v[28:31], a[164:167], a[68:71], 0\n"
"v_mfma_f32_16x16x32_bf16 v[28:31], a[168:171], a[72:75], v[28:31]\n"
"v_mfma_f32_16x16x32_bf16 v[28:31], a[172:175], a[76:79], v[28:31]\n"
"v_mfma_f32_16x16x32_bf16 v[28:31], a[176:179], a[80:83], v[28:31]\n"
"s_nop 7\n"
"v_sub_f32 v28, v28, v5\n"
"v_sub_f32 v29, v29, v5\n"
"v_sub_f32 v30, v30, v5\n"
"v_sub_f32 v31, v31, v5\n"
"v_exp_f32 v28, v28\n"
"v_exp_f32 v29, v29\n"
"v_exp_f32 v30, v30\n"
"v_exp_f32 v31, v31\n"
"v_cvt_pk_bf16_f32 v224, v28, v29\n"
"v_cvt_pk_bf16_f32 v225, v30, v31\n"
"v_mfma_f32_16x16x32_bf16 v[36:39], a[164:167], a[84:87], 0\n"
"v_mfma_f32_16x16x32_bf16 v[36:39], a[168:171], a[88:91], v[36:39]\n"
"v_mfma_f32_16x16x32_bf16 v[36:39], a[172:175], a[92:95], v[36:39]\n"
"v_mfma_f32_16x16x32_bf16 v[36:39], a[176:179], a[96:99], v[36:39]\n"
"s_nop 7\n"
"v_sub_f32 v36, v36, v6\n"
"v_sub_f32 v37, v37, v6\n"
"v_sub_f32 v38, v38, v6\n"
"v_sub_f32 v39, v39, v6\n"
"v_exp_f32 v36, v36\n"
"v_exp_f32 v37, v37\n"
"v_exp_f32 v38, v38\n"
"v_exp_f32 v39, v39\n"
"v_cvt_pk_bf16_f32 v228, v36, v37\n"
"v_cvt_pk_bf16_f32 v229, v38, v39\n"
"v_mfma_f32_16x16x32_bf16 v[20:23], a[164:167], a[100:103], 0\n"
"v_mfma_f32_16x16x32_bf16 v[20:23], a[168:171], a[104:107], v[20:23]\n"
"v_mfma_f32_16x16x32_bf16 v[20:23], a[172:175], a[108:111], v[20:23]\n"
"v_mfma_f32_16x16x32_bf16 v[20:23], a[176:179], a[112:115], v[20:23]\n"
"s_nop 7\n"
"v_sub_f32 v20, v20, v7\n"
"v_sub_f32 v21, v21, v7\n"
"v_sub_f32 v22, v22, v7\n"
"v_sub_f32 v23, v23, v7\n"
"v_exp_f32 v20, v20\n"
"v_exp_f32 v21, v21\n"
"v_exp_f32 v22, v22\n"
"v_exp_f32 v23, v23\n"
"v_cvt_pk_bf16_f32 v232, v20, v21\n"
"v_cvt_pk_bf16_f32 v233, v22, v23\n"
"s_xor_b32 s95, s17, 0\n"
"s_lshl_b32 s95, s95, 6\n"
"s_add_u32 s95, s95, 110592\n"
"buffer_load_dwordx4 a[164:167], v1, s[52:55], s95 offen\n"
"s_xor_b32 s95, s17, 1\n"
"s_lshl_b32 s95, s95, 6\n"
"s_add_u32 s95, s95, 110592\n"
"buffer_load_dwordx4 a[168:171], v1, s[52:55], s95 offen\n"
"s_xor_b32 s95, s17, 2\n"
"s_lshl_b32 s95, s95, 6\n"
"s_add_u32 s95, s95, 110592\n"
"buffer_load_dwordx4 a[172:175], v1, s[52:55], s95 offen\n"
"s_xor_b32 s95, s17, 3\n"
"s_lshl_b32 s95, s95, 6\n"
"s_add_u32 s95, s95, 110592\n"
"buffer_load_dwordx4 a[176:179], v1, s[52:55], s95 offen\n"
"s_waitcnt vmcnt(3)\n"
"v_mfma_f32_16x16x32_bf16 v[20:23], a[164:167], a[52:55], 0\n"
"s_waitcnt vmcnt(2)\n"
"v_mfma_f32_16x16x32_bf16 v[20:23], a[168:171], a[56:59], v[20:23]\n"
"s_waitcnt vmcnt(1)\n"
"v_mfma_f32_16x16x32_bf16 v[20:23], a[172:175], a[60:63], v[20:23]\n"
"s_waitcnt vmcnt(0)\n"
"v_mfma_f32_16x16x32_bf16 v[20:23], a[176:179], a[64:67], v[20:23]\n"
"s_nop 7\n"
"v_sub_f32 v20, v20, v4\n"
"v_sub_f32 v21, v21, v4\n"
"v_sub_f32 v22, v22, v4\n"
"v_sub_f32 v23, v23, v4\n"
"v_exp_f32 v20, v20\n"
"v_exp_f32 v21, v21\n"
"v_exp_f32 v22, v22\n"
"v_exp_f32 v23, v23\n"
"v_cvt_pk_bf16_f32 v222, v20, v21\n"
"v_cvt_pk_bf16_f32 v223, v22, v23\n"
"v_mfma_f32_16x16x32_bf16 v[28:31], a[164:167], a[68:71], 0\n"
"v_mfma_f32_16x16x32_bf16 v[28:31], a[168:171], a[72:75], v[28:31]\n"
"v_mfma_f32_16x16x32_bf16 v[28:31], a[172:175], a[76:79], v[28:31]\n"
"v_mfma_f32_16x16x32_bf16 v[28:31], a[176:179], a[80:83], v[28:31]\n"
"s_nop 7\n"
"v_sub_f32 v28, v28, v5\n"
"v_sub_f32 v29, v29, v5\n"
"v_sub_f32 v30, v30, v5\n"
"v_sub_f32 v31, v31, v5\n"
"v_exp_f32 v28, v28\n"
"v_exp_f32 v29, v29\n"
"v_exp_f32 v30, v30\n"
"v_exp_f32 v31, v31\n"
"v_cvt_pk_bf16_f32 v226, v28, v29\n"
"v_cvt_pk_bf16_f32 v227, v30, v31\n"
"v_mfma_f32_16x16x32_bf16 v[36:39], a[164:167], a[84:87], 0\n"
"v_mfma_f32_16x16x32_bf16 v[36:39], a[168:171], a[88:91], v[36:39]\n"
"v_mfma_f32_16x16x32_bf16 v[36:39], a[172:175], a[92:95], v[36:39]\n"
"v_mfma_f32_16x16x32_bf16 v[36:39], a[176:179], a[96:99], v[36:39]\n"
"s_nop 7\n"
"v_sub_f32 v36, v36, v6\n"
"v_sub_f32 v37, v37, v6\n"
"v_sub_f32 v38, v38, v6\n"
"v_sub_f32 v39, v39, v6\n"
"v_exp_f32 v36, v36\n"
"v_exp_f32 v37, v37\n"
"v_exp_f32 v38, v38\n"
"v_exp_f32 v39, v39\n"
"v_cvt_pk_bf16_f32 v230, v36, v37\n"
"v_cvt_pk_bf16_f32 v231, v38, v39\n"
"v_mfma_f32_16x16x32_bf16 v[20:23], a[164:167], a[100:103], 0\n"
"v_mfma_f32_16x16x32_bf16 v[20:23], a[168:171], a[104:107], v[20:23]\n"
"v_mfma_f32_16x16x32_bf16 v[20:23], a[172:175], a[108:111], v[20:23]\n"
"v_mfma_f32_16x16x32_bf16 v[20:23], a[176:179], a[112:115], v[20:23]\n"
"s_nop 7\n"
"v_sub_f32 v20, v20, v7\n"
"v_sub_f32 v21, v21, v7\n"
"v_sub_f32 v22, v22, v7\n"
"v_sub_f32 v23, v23, v7\n"
"v_exp_f32 v20, v20\n"
"v_exp_f32 v21, v21\n"
"v_exp_f32 v22, v22\n"
"v_exp_f32 v23, v23\n"
"v_cvt_pk_bf16_f32 v234, v20, v21\n"
"v_cvt_pk_bf16_f32 v235, v22, v23\n"
"s_waitcnt lgkmcnt(0)\n"
"v_mfma_f32_16x16x32_bf16 a[20:23], a[196:199], v[220:223], a[20:23]\n"
"v_mfma_f32_16x16x32_bf16 a[36:39], a[200:203], v[220:223], a[36:39]\n"
"v_mfma_f32_16x16x32_bf16 a[4:7], a[0:3], v[220:223], a[4:7]\n"
"v_mfma_f32_16x16x32_bf16 a[24:27], a[196:199], v[224:227], a[24:27]\n"
"v_mfma_f32_16x16x32_bf16 a[40:43], a[200:203], v[224:227], a[40:43]\n"
"v_mfma_f32_16x16x32_bf16 a[8:11], a[0:3], v[224:227], a[8:11]\n"
"v_mfma_f32_16x16x32_bf16 a[28:31], a[196:199], v[228:231], a[28:31]\n"
"v_mfma_f32_16x16x32_bf16 a[44:47], a[200:203], v[228:231], a[44:47]\n"
"v_mfma_f32_16x16x32_bf16 a[12:15], a[0:3], v[228:231], a[12:15]\n"
"v_mfma_f32_16x16x32_bf16 a[32:35], a[196:199], v[232:235], a[32:35]\n"
"v_mfma_f32_16x16x32_bf16 a[48:51], a[200:203], v[232:235], a[48:51]\n"
"v_mfma_f32_16x16x32_bf16 a[16:19], a[0:3], v[232:235], a[16:19]\n"
"ds_read_b64_tr_b16 a[196:197], v8 offset:57344\n"
"ds_read_b64_tr_b16 a[198:199], v8 offset:59392\n"
"ds_read_b64_tr_b16 a[200:201], v9 offset:57344\n"
"ds_read_b64_tr_b16 a[202:203], v9 offset:59392\n"
"s_xor_b32 s95, s17, 0\n"
"s_lshl_b32 s95, s95, 6\n"
"s_add_u32 s95, s95, 114688\n"
"buffer_load_dwordx4 a[164:167], v1, s[52:55], s95 offen\n"
"s_xor_b32 s95, s17, 1\n"
"s_lshl_b32 s95, s95, 6\n"
"s_add_u32 s95, s95, 114688\n"
"buffer_load_dwordx4 a[168:171], v1, s[52:55], s95 offen\n"
"s_xor_b32 s95, s17, 2\n"
"s_lshl_b32 s95, s95, 6\n"
"s_add_u32 s95, s95, 114688\n"
"buffer_load_dwordx4 a[172:175], v1, s[52:55], s95 offen\n"
"s_xor_b32 s95, s17, 3\n"
"s_lshl_b32 s95, s95, 6\n"
"s_add_u32 s95, s95, 114688\n"
"buffer_load_dwordx4 a[176:179], v1, s[52:55], s95 offen\n"
"s_waitcnt vmcnt(3)\n"
"v_mfma_f32_16x16x32_bf16 v[20:23], a[164:167], a[52:55], 0\n"
"s_waitcnt vmcnt(2)\n"
"v_mfma_f32_16x16x32_bf16 v[20:23], a[168:171], a[56:59], v[20:23]\n"
"s_waitcnt vmcnt(1)\n"
"v_mfma_f32_16x16x32_bf16 v[20:23], a[172:175], a[60:63], v[20:23]\n"
"s_waitcnt vmcnt(0)\n"
"v_mfma_f32_16x16x32_bf16 v[20:23], a[176:179], a[64:67], v[20:23]\n"
"s_nop 7\n"
"v_sub_f32 v20, v20, v4\n"
"v_sub_f32 v21, v21, v4\n"
"v_sub_f32 v22, v22, v4\n"
"v_sub_f32 v23, v23, v4\n"
"v_exp_f32 v20, v20\n"
"v_exp_f32 v21, v21\n"
"v_exp_f32 v22, v22\n"
"v_exp_f32 v23, v23\n"
"v_cvt_pk_bf16_f32 v236, v20, v21\n"
"v_cvt_pk_bf16_f32 v237, v22, v23\n"
"v_mfma_f32_16x16x32_bf16 v[28:31], a[164:167], a[68:71], 0\n"
"v_mfma_f32_16x16x32_bf16 v[28:31], a[168:171], a[72:75], v[28:31]\n"
"v_mfma_f32_16x16x32_bf16 v[28:31], a[172:175], a[76:79], v[28:31]\n"
"v_mfma_f32_16x16x32_bf16 v[28:31], a[176:179], a[80:83], v[28:31]\n"
"s_nop 7\n"
"v_sub_f32 v28, v28, v5\n"
"v_sub_f32 v29, v29, v5\n"
"v_sub_f32 v30, v30, v5\n"
"v_sub_f32 v31, v31, v5\n"
"v_exp_f32 v28, v28\n"
"v_exp_f32 v29, v29\n"
"v_exp_f32 v30, v30\n"
"v_exp_f32 v31, v31\n"
"v_cvt_pk_bf16_f32 v240, v28, v29\n"
"v_cvt_pk_bf16_f32 v241, v30, v31\n"
"v_mfma_f32_16x16x32_bf16 v[36:39], a[164:167], a[84:87], 0\n"
"v_mfma_f32_16x16x32_bf16 v[36:39], a[168:171], a[88:91], v[36:39]\n"
"v_mfma_f32_16x16x32_bf16 v[36:39], a[172:175], a[92:95], v[36:39]\n"
"v_mfma_f32_16x16x32_bf16 v[36:39], a[176:179], a[96:99], v[36:39]\n"
"s_nop 7\n"
"v_sub_f32 v36, v36, v6\n"
"v_sub_f32 v37, v37, v6\n"
"v_sub_f32 v38, v38, v6\n"
"v_sub_f32 v39, v39, v6\n"
"v_exp_f32 v36, v36\n"
"v_exp_f32 v37, v37\n"
"v_exp_f32 v38, v38\n"
"v_exp_f32 v39, v39\n"
"v_cvt_pk_bf16_f32 v244, v36, v37\n"
"v_cvt_pk_bf16_f32 v245, v38, v39\n"
"v_mfma_f32_16x16x32_bf16 v[20:23], a[164:167], a[100:103], 0\n"
"v_mfma_f32_16x16x32_bf16 v[20:23], a[168:171], a[104:107], v[20:23]\n"
"v_mfma_f32_16x16x32_bf16 v[20:23], a[172:175], a[108:111], v[20:23]\n"
"v_mfma_f32_16x16x32_bf16 v[20:23], a[176:179], a[112:115], v[20:23]\n"
"s_nop 7\n"
"v_sub_f32 v20, v20, v7\n"
"v_sub_f32 v21, v21, v7\n"
"v_sub_f32 v22, v22, v7\n"
"v_sub_f32 v23, v23, v7\n"
"v_exp_f32 v20, v20\n"
"v_exp_f32 v21, v21\n"
"v_exp_f32 v22, v22\n"
"v_exp_f32 v23, v23\n"
"v_cvt_pk_bf16_f32 v248, v20, v21\n"
"v_cvt_pk_bf16_f32 v249, v22, v23\n"
"s_xor_b32 s95, s17, 0\n"
"s_lshl_b32 s95, s95, 6\n"
"s_add_u32 s95, s95, 118784\n"
"buffer_load_dwordx4 a[164:167], v1, s[52:55], s95 offen\n"
"s_xor_b32 s95, s17, 1\n"
"s_lshl_b32 s95, s95, 6\n"
"s_add_u32 s95, s95, 118784\n"
"buffer_load_dwordx4 a[168:171], v1, s[52:55], s95 offen\n"
"s_xor_b32 s95, s17, 2\n"
"s_lshl_b32 s95, s95, 6\n"
"s_add_u32 s95, s95, 118784\n"
"buffer_load_dwordx4 a[172:175], v1, s[52:55], s95 offen\n"
"s_xor_b32 s95, s17, 3\n"
"s_lshl_b32 s95, s95, 6\n"
"s_add_u32 s95, s95, 118784\n"
"buffer_load_dwordx4 a[176:179], v1, s[52:55], s95 offen\n"
"s_waitcnt vmcnt(3)\n"
"v_mfma_f32_16x16x32_bf16 v[20:23], a[164:167], a[52:55], 0\n"
"s_waitcnt vmcnt(2)\n"
"v_mfma_f32_16x16x32_bf16 v[20:23], a[168:171], a[56:59], v[20:23]\n"
"s_waitcnt vmcnt(1)\n"
"v_mfma_f32_16x16x32_bf16 v[20:23], a[172:175], a[60:63], v[20:23]\n"
"s_waitcnt vmcnt(0)\n"
"v_mfma_f32_16x16x32_bf16 v[20:23], a[176:179], a[64:67], v[20:23]\n"
"s_nop 7\n"
"v_sub_f32 v20, v20, v4\n"
"v_sub_f32 v21, v21, v4\n"
"v_sub_f32 v22, v22, v4\n"
"v_sub_f32 v23, v23, v4\n"
"v_exp_f32 v20, v20\n"
"v_exp_f32 v21, v21\n"
"v_exp_f32 v22, v22\n"
"v_exp_f32 v23, v23\n"
"v_cvt_pk_bf16_f32 v238, v20, v21\n"
"v_cvt_pk_bf16_f32 v239, v22, v23\n"
"v_mfma_f32_16x16x32_bf16 v[28:31], a[164:167], a[68:71], 0\n"
"v_mfma_f32_16x16x32_bf16 v[28:31], a[168:171], a[72:75], v[28:31]\n"
"v_mfma_f32_16x16x32_bf16 v[28:31], a[172:175], a[76:79], v[28:31]\n"
"v_mfma_f32_16x16x32_bf16 v[28:31], a[176:179], a[80:83], v[28:31]\n"
"s_nop 7\n"
"v_sub_f32 v28, v28, v5\n"
"v_sub_f32 v29, v29, v5\n"
"v_sub_f32 v30, v30, v5\n"
"v_sub_f32 v31, v31, v5\n"
"v_exp_f32 v28, v28\n"
"v_exp_f32 v29, v29\n"
"v_exp_f32 v30, v30\n"
"v_exp_f32 v31, v31\n"
"v_cvt_pk_bf16_f32 v242, v28, v29\n"
"v_cvt_pk_bf16_f32 v243, v30, v31\n"
"v_mfma_f32_16x16x32_bf16 v[36:39], a[164:167], a[84:87], 0\n"
"v_mfma_f32_16x16x32_bf16 v[36:39], a[168:171], a[88:91], v[36:39]\n"
"v_mfma_f32_16x16x32_bf16 v[36:39], a[172:175], a[92:95], v[36:39]\n"
"v_mfma_f32_16x16x32_bf16 v[36:39], a[176:179], a[96:99], v[36:39]\n"
"s_nop 7\n"
"v_sub_f32 v36, v36, v6\n"
"v_sub_f32 v37, v37, v6\n"
"v_sub_f32 v38, v38, v6\n"
"v_sub_f32 v39, v39, v6\n"
"v_exp_f32 v36, v36\n"
"v_exp_f32 v37, v37\n"
"v_exp_f32 v38, v38\n"
"v_exp_f32 v39, v39\n"
"v_cvt_pk_bf16_f32 v246, v36, v37\n"
"v_cvt_pk_bf16_f32 v247, v38, v39\n"
"v_mfma_f32_16x16x32_bf16 v[20:23], a[164:167], a[100:103], 0\n"
"v_mfma_f32_16x16x32_bf16 v[20:23], a[168:171], a[104:107], v[20:23]\n"
"v_mfma_f32_16x16x32_bf16 v[20:23], a[172:175], a[108:111], v[20:23]\n"
"v_mfma_f32_16x16x32_bf16 v[20:23], a[176:179], a[112:115], v[20:23]\n"
"s_nop 7\n"
"v_sub_f32 v20, v20, v7\n"
"v_sub_f32 v21, v21, v7\n"
"v_sub_f32 v22, v22, v7\n"
"v_sub_f32 v23, v23, v7\n"
"v_exp_f32 v20, v20\n"
"v_exp_f32 v21, v21\n"
"v_exp_f32 v22, v22\n"
"v_exp_f32 v23, v23\n"
"v_cvt_pk_bf16_f32 v250, v20, v21\n"
"v_cvt_pk_bf16_f32 v251, v22, v23\n"
"s_waitcnt lgkmcnt(0)\n"
"v_mfma_f32_16x16x32_bf16 a[20:23], a[196:199], v[236:239], a[20:23]\n"
"v_mfma_f32_16x16x32_bf16 a[36:39], a[200:203], v[236:239], a[36:39]\n"
"v_mfma_f32_16x16x32_bf16 a[4:7], a[0:3], v[236:239], a[4:7]\n"
"v_mfma_f32_16x16x32_bf16 a[24:27], a[196:199], v[240:243], a[24:27]\n"
"v_mfma_f32_16x16x32_bf16 a[40:43], a[200:203], v[240:243], a[40:43]\n"
"v_mfma_f32_16x16x32_bf16 a[8:11], a[0:3], v[240:243], a[8:11]\n"
"v_mfma_f32_16x16x32_bf16 a[28:31], a[196:199], v[244:247], a[28:31]\n"
"v_mfma_f32_16x16x32_bf16 a[44:47], a[200:203], v[244:247], a[44:47]\n"
"v_mfma_f32_16x16x32_bf16 a[12:15], a[0:3], v[244:247], a[12:15]\n"
"v_mfma_f32_16x16x32_bf16 a[32:35], a[196:199], v[248:251], a[32:35]\n"
"v_mfma_f32_16x16x32_bf16 a[48:51], a[200:203], v[248:251], a[48:51]\n"
"v_mfma_f32_16x16x32_bf16 a[16:19], a[0:3], v[248:251], a[16:19]\n"
"ds_read_b64_tr_b16 a[196:197], v8 offset:61440\n"
"ds_read_b64_tr_b16 a[198:199], v8 offset:63488\n"
"ds_read_b64_tr_b16 a[200:201], v9 offset:61440\n"
"ds_read_b64_tr_b16 a[202:203], v9 offset:63488\n"
"s_xor_b32 s95, s17, 0\n"
"s_lshl_b32 s95, s95, 6\n"
"s_add_u32 s95, s95, 122880\n"
"buffer_load_dwordx4 a[164:167], v1, s[52:55], s95 offen\n"
"s_xor_b32 s95, s17, 1\n"
"s_lshl_b32 s95, s95, 6\n"
"s_add_u32 s95, s95, 122880\n"
"buffer_load_dwordx4 a[168:171], v1, s[52:55], s95 offen\n"
"s_xor_b32 s95, s17, 2\n"
"s_lshl_b32 s95, s95, 6\n"
"s_add_u32 s95, s95, 122880\n"
"buffer_load_dwordx4 a[172:175], v1, s[52:55], s95 offen\n"
"s_xor_b32 s95, s17, 3\n"
"s_lshl_b32 s95, s95, 6\n"
"s_add_u32 s95, s95, 122880\n"
"buffer_load_dwordx4 a[176:179], v1, s[52:55], s95 offen\n"
"s_waitcnt vmcnt(3)\n"
"v_mfma_f32_16x16x32_bf16 v[20:23], a[164:167], a[52:55], 0\n"
"s_waitcnt vmcnt(2)\n"
"v_mfma_f32_16x16x32_bf16 v[20:23], a[168:171], a[56:59], v[20:23]\n"
"s_waitcnt vmcnt(1)\n"
"v_mfma_f32_16x16x32_bf16 v[20:23], a[172:175], a[60:63], v[20:23]\n"
"s_waitcnt vmcnt(0)\n"
"v_mfma_f32_16x16x32_bf16 v[20:23], a[176:179], a[64:67], v[20:23]\n"
"s_nop 7\n"
"v_sub_f32 v20, v20, v4\n"
"v_sub_f32 v21, v21, v4\n"
"v_sub_f32 v22, v22, v4\n"
"v_sub_f32 v23, v23, v4\n"
"v_exp_f32 v20, v20\n"
"v_exp_f32 v21, v21\n"
"v_exp_f32 v22, v22\n"
"v_exp_f32 v23, v23\n"
"v_cvt_pk_bf16_f32 v252, v20, v21\n"
"v_cvt_pk_bf16_f32 v253, v22, v23\n"
"v_mfma_f32_16x16x32_bf16 v[28:31], a[164:167], a[68:71], 0\n"
"v_mfma_f32_16x16x32_bf16 v[28:31], a[168:171], a[72:75], v[28:31]\n"
"v_mfma_f32_16x16x32_bf16 v[28:31], a[172:175], a[76:79], v[28:31]\n"
"v_mfma_f32_16x16x32_bf16 v[28:31], a[176:179], a[80:83], v[28:31]\n"
"s_nop 7\n"
"v_sub_f32 v28, v28, v5\n"
"v_sub_f32 v29, v29, v5\n"
"v_sub_f32 v30, v30, v5\n"
"v_sub_f32 v31, v31, v5\n"
"v_exp_f32 v28, v28\n"
"v_exp_f32 v29, v29\n"
"v_exp_f32 v30, v30\n"
"v_exp_f32 v31, v31\n"
"v_cvt_pk_bf16_f32 v24, v28, v29\n"
"v_cvt_pk_bf16_f32 v25, v30, v31\n"
"v_mfma_f32_16x16x32_bf16 v[36:39], a[164:167], a[84:87], 0\n"
"v_mfma_f32_16x16x32_bf16 v[36:39], a[168:171], a[88:91], v[36:39]\n"
"v_mfma_f32_16x16x32_bf16 v[36:39], a[172:175], a[92:95], v[36:39]\n"
"v_mfma_f32_16x16x32_bf16 v[36:39], a[176:179], a[96:99], v[36:39]\n"
"s_nop 7\n"
"v_sub_f32 v36, v36, v6\n"
"v_sub_f32 v37, v37, v6\n"
"v_sub_f32 v38, v38, v6\n"
"v_sub_f32 v39, v39, v6\n"
"v_exp_f32 v36, v36\n"
"v_exp_f32 v37, v37\n"
"v_exp_f32 v38, v38\n"
"v_exp_f32 v39, v39\n"
"v_cvt_pk_bf16_f32 v32, v36, v37\n"
"v_cvt_pk_bf16_f32 v33, v38, v39\n"
"v_mfma_f32_16x16x32_bf16 v[20:23], a[164:167], a[100:103], 0\n"
"v_mfma_f32_16x16x32_bf16 v[20:23], a[168:171], a[104:107], v[20:23]\n"
"v_mfma_f32_16x16x32_bf16 v[20:23], a[172:175], a[108:111], v[20:23]\n"
"v_mfma_f32_16x16x32_bf16 v[20:23], a[176:179], a[112:115], v[20:23]\n"
"s_nop 7\n"
"v_sub_f32 v20, v20, v7\n"
"v_sub_f32 v21, v21, v7\n"
"v_sub_f32 v22, v22, v7\n"
"v_sub_f32 v23, v23, v7\n"
"v_exp_f32 v20, v20\n"
"v_exp_f32 v21, v21\n"
"v_exp_f32 v22, v22\n"
"v_exp_f32 v23, v23\n"
"v_cvt_pk_bf16_f32 v16, v20, v21\n"
"v_cvt_pk_bf16_f32 v17, v22, v23\n"
"s_xor_b32 s95, s17, 0\n"
"s_lshl_b32 s95, s95, 6\n"
"s_add_u32 s95, s95, 126976\n"
"buffer_load_dwordx4 a[164:167], v1, s[52:55], s95 offen\n"
"s_xor_b32 s95, s17, 1\n"
"s_lshl_b32 s95, s95, 6\n"
"s_add_u32 s95, s95, 126976\n"
"buffer_load_dwordx4 a[168:171], v1, s[52:55], s95 offen\n"
"s_xor_b32 s95, s17, 2\n"
"s_lshl_b32 s95, s95, 6\n"
"s_add_u32 s95, s95, 126976\n"
"buffer_load_dwordx4 a[172:175], v1, s[52:55], s95 offen\n"
"s_xor_b32 s95, s17, 3\n"
"s_lshl_b32 s95, s95, 6\n"
"s_add_u32 s95, s95, 126976\n"
"buffer_load_dwordx4 a[176:179], v1, s[52:55], s95 offen\n"
"s_waitcnt vmcnt(3)\n"
"v_mfma_f32_16x16x32_bf16 v[20:23], a[164:167], a[52:55], 0\n"
"s_waitcnt vmcnt(2)\n"
"v_mfma_f32_16x16x32_bf16 v[20:23], a[168:171], a[56:59], v[20:23]\n"
"s_waitcnt vmcnt(1)\n"
"v_mfma_f32_16x16x32_bf16 v[20:23], a[172:175], a[60:63], v[20:23]\n"
"s_waitcnt vmcnt(0)\n"
"v_mfma_f32_16x16x32_bf16 v[20:23], a[176:179], a[64:67], v[20:23]\n"
"s_nop 7\n"
"v_sub_f32 v20, v20, v4\n"
"v_sub_f32 v21, v21, v4\n"
"v_sub_f32 v22, v22, v4\n"
"v_sub_f32 v23, v23, v4\n"
"v_exp_f32 v20, v20\n"
"v_exp_f32 v21, v21\n"
"v_exp_f32 v22, v22\n"
"v_exp_f32 v23, v23\n"
"v_cvt_pk_bf16_f32 v254, v20, v21\n"
"v_cvt_pk_bf16_f32 v255, v22, v23\n"
"v_mfma_f32_16x16x32_bf16 v[28:31], a[164:167], a[68:71], 0\n"
"v_mfma_f32_16x16x32_bf16 v[28:31], a[168:171], a[72:75], v[28:31]\n"
"v_mfma_f32_16x16x32_bf16 v[28:31], a[172:175], a[76:79], v[28:31]\n"
"v_mfma_f32_16x16x32_bf16 v[28:31], a[176:179], a[80:83], v[28:31]\n"
"s_nop 7\n"
"v_sub_f32 v28, v28, v5\n"
"v_sub_f32 v29, v29, v5\n"
"v_sub_f32 v30, v30, v5\n"
"v_sub_f32 v31, v31, v5\n"
"v_exp_f32 v28, v28\n"
"v_exp_f32 v29, v29\n"
"v_exp_f32 v30, v30\n"
"v_exp_f32 v31, v31\n"
"v_cvt_pk_bf16_f32 v26, v28, v29\n"
"v_cvt_pk_bf16_f32 v27, v30, v31\n"
"v_mfma_f32_16x16x32_bf16 v[36:39], a[164:167], a[84:87], 0\n"
"v_mfma_f32_16x16x32_bf16 v[36:39], a[168:171], a[88:91], v[36:39]\n"
"v_mfma_f32_16x16x32_bf16 v[36:39], a[172:175], a[92:95], v[36:39]\n"
"v_mfma_f32_16x16x32_bf16 v[36:39], a[176:179], a[96:99], v[36:39]\n"
"s_nop 7\n"
"v_sub_f32 v36, v36, v6\n"
"v_sub_f32 v37, v37, v6\n"
"v_sub_f32 v38, v38, v6\n"
"v_sub_f32 v39, v39, v6\n"
"v_exp_f32 v36, v36\n"
"v_exp_f32 v37, v37\n"
"v_exp_f32 v38, v38\n"
"v_exp_f32 v39, v39\n"
"v_cvt_pk_bf16_f32 v34, v36, v37\n"
"v_cvt_pk_bf16_f32 v35, v38, v39\n"
"v_mfma_f32_16x16x32_bf16 v[20:23], a[164:167], a[100:103], 0\n"
"v_mfma_f32_16x16x32_bf16 v[20:23], a[168:171], a[104:107], v[20:23]\n"
"v_mfma_f32_16x16x32_bf16 v[20:23], a[172:175], a[108:111], v[20:23]\n"
"v_mfma_f32_16x16x32_bf16 v[20:23], a[176:179], a[112:115], v[20:23]\n"
"s_nop 7\n"
"v_sub_f32 v20, v20, v7\n"
"v_sub_f32 v21, v21, v7\n"
"v_sub_f32 v22, v22, v7\n"
"v_sub_f32 v23, v23, v7\n"
"v_exp_f32 v20, v20\n"
"v_exp_f32 v21, v21\n"
"v_exp_f32 v22, v22\n"
"v_exp_f32 v23, v23\n"
"v_cvt_pk_bf16_f32 v18, v20, v21\n"
"v_cvt_pk_bf16_f32 v19, v22, v23\n"
"s_waitcnt lgkmcnt(0)\n"
"v_mfma_f32_16x16x32_bf16 a[20:23], a[196:199], v[252:255], a[20:23]\n"
"v_mfma_f32_16x16x32_bf16 a[36:39], a[200:203], v[252:255], a[36:39]\n"
"v_mfma_f32_16x16x32_bf16 a[4:7], a[0:3], v[252:255], a[4:7]\n"
"v_mfma_f32_16x16x32_bf16 a[24:27], a[196:199], v[24:27], a[24:27]\n"
"v_mfma_f32_16x16x32_bf16 a[40:43], a[200:203], v[24:27], a[40:43]\n"
"v_mfma_f32_16x16x32_bf16 a[8:11], a[0:3], v[24:27], a[8:11]\n"
"v_mfma_f32_16x16x32_bf16 a[28:31], a[196:199], v[32:35], a[28:31]\n"
"v_mfma_f32_16x16x32_bf16 a[44:47], a[200:203], v[32:35], a[44:47]\n"
"v_mfma_f32_16x16x32_bf16 a[12:15], a[0:3], v[32:35], a[12:15]\n"
"v_mfma_f32_16x16x32_bf16 a[32:35], a[196:199], v[16:19], a[32:35]\n"
"v_mfma_f32_16x16x32_bf16 a[48:51], a[200:203], v[16:19], a[48:51]\n"
"v_mfma_f32_16x16x32_bf16 a[16:19], a[0:3], v[16:19], a[16:19]\n"
"v_accvgpr_read_b32 v4, a4\n"
"s_nop 0\n"
"v_accvgpr_read_b32 v5, a8\n"
"s_nop 1\n"
"v_accvgpr_read_b32 v6, a12\n"
"s_nop 1\n"
"v_accvgpr_read_b32 v7, a16\n"
"s_nop 7\n"
"s_branch .Lfbdone\n"
".Lend:\n"
      :
      : "s"(__builtin_amdgcn_kernarg_segment_ptr()), "s"(blockIdx.x), "s"(wave), "s"((unsigned)(size_t)lds)
      : "v0","v1","v2","v3","v4","v5","v6","v7","v8","v9","v10","v11","v12","v13","v14","v15","v16","v17","v18","v19","v20","v21","v22","v23","v24","v25","v26","v27","v28","v29","v30","v31","v32","v33","v34","v35","v36","v37","v38","v39","v40","v41","v42","v43","v44","v45","v46","v47","v48","v49","v50","v51","v52","v53","v54","v55","v56","v57","v58","v59","v60","v61","v62","v63","v64","v65","v66","v67","v68","v69","v70","v71","v72","v73","v74","v75","v76","v77","v78","v79","v80","v81","v82","v83","v84","v85","v86","v87","v88","v89","v90","v91","v92","v93","v94","v95","v96","v97","v98","v99","v100","v101","v102","v103","v104","v105","v106","v107","v108","v109","v110","v111","v112","v113","v114","v115","v116","v117","v118","v119","v120","v121","v122","v123","v124","v125","v126","v127","v128","v129","v130","v131","v132","v133","v134","v135","v136","v137","v138","v139","v140","v141","v142","v143","v144","v145","v146","v147","v148","v149","v150","v151","v152","v153","v154","v155","v156","v157","v158","v159","v160","v161","v162","v163","v164","v165","v166","v167","v168","v169","v170","v171","v172","v173","v174","v175","v176","v177","v178","v179","v180","v181","v182","v183","v184","v185","v186","v187","v188","v189","v190","v191","v192","v193","v194","v195","v196","v197","v198","v199","v200","v201","v202","v203","v204","v205","v206","v207","v208","v209","v210","v211","v212","v213","v214","v215","v216","v217","v218","v219","v220","v221","v222","v223","v224","v225","v226","v227","v228","v229","v230","v231","v232","v233","v234","v235","v236","v237","v238","v239","v240","v241","v242","v243","v244","v245","v246","v247","v248","v249","v250","v251","v252","v253","v254","v255","a0","a1","a2","a3","a4","a5","a6","a7","a8","a9","a10","a11","a12","a13","a14","a15","a16","a17","a18","a19","a20","a21","a22","a23","a24","a25","a26","a27","a28","a29","a30","a31","a32","a33","a34","a35","a36","a37","a38","a39","a40","a41","a42","a43","a44","a45","a46","a47","a48","a49","a50","a51","a52","a53","a54","a55","a56","a57","a58","a59","a60","a61","a62","a63","a64","a65","a66","a67","a68","a69","a70","a71","a72","a73","a74","a75","a76","a77","a78","a79","a80","a81","a82","a83","a84","a85","a86","a87","a88","a89","a90","a91","a92","a93","a94","a95","a96","a97","a98","a99","a100","a101","a102","a103","a104","a105","a106","a107","a108","a109","a110","a111","a112","a113","a114","a115","a116","a117","a118","a119","a120","a121","a122","a123","a124","a125","a126","a127","a128","a129","a130","a131","a132","a133","a134","a135","a136","a137","a138","a139","a140","a141","a142","a143","a144","a145","a146","a147","a148","a149","a150","a151","a152","a153","a154","a155","a156","a157","a158","a159","a160","a161","a162","a163","a164","a165","a166","a167","a168","a169","a170","a171","a172","a173","a174","a175","a176","a177","a178","a179","a180","a181","a182","a183","a184","a185","a186","a187","a188","a189","a190","a191","a192","a193","a194","a195","a196","a197","a198","a199","a200","a201","a202","a203","a204","a205","a206","a207","a208","a209","a210","a211","a212","a213","a214","a215","a216","a217","a218","a219","a220","a221","a222","a223","a224","a225","a226","a227","a228","a229","a230","a231","a232","a233","a234","a235","a236","a237","a238","a239","a240","a241","a242","a243","a244","a245","a246","a247","a248","a249","a250","a251","a252","a253","a254","a255","s6","s7","s8","s9","s10","s11","s12","s13","s14","s15","s16","s17","s18","s19","s20","s21","s22","s23","s24","s25","s26","s27","s28","s29","s30","s31","s33","s34","s35","s36","s37","s38","s39","s40","s41","s42","s43","s44","s45","s46","s47","s48","s49","s50","s51","s52","s53","s54","s55","s56","s57","s58","s59","s60","s61","s62","s63","s64","s65","s66","s67","s68","s69","s70","s71","s72","s73","s74","s75","s76","s77","s78","s79","s80","s81","s82","s83","s84","s85","s86","s87","s88","s89","s90","s91","s92","s93","s94","s95","s96","s97","s98","s99","vcc","scc","memory");
}

extern "C" void kernel_launch(void* const* d_in, const int* in_sizes, int n_in, void* d_out, int out_size, void* d_ws, size_t ws_size,
                              hipStream_t stream) {
  fused_kernel<<<256, 256, 0, stream>>>(d_in[0], d_in[3], d_in[1], d_in[2], d_in[4], d_in[6], d_in[7], d_in[8], d_in[9], d_out, d_ws);
}
```
